```python
import torch

def _softmax(x, axis=-1):
    return torch.softmax(x, dim=axis)
def _gelu(x):
    return torch.nn.functional.gelu(x, approximate="tanh")
def _silu(x):
    return torch.nn.functional.silu(x)
def _sigmoid(x):
    return torch.sigmoid(x)
def _layernorm(x, axis=-1, eps=1e-5):
    m = x.mean(dim=axis, keepdim=True)
    v = x.var(dim=axis, keepdim=True, unbiased=False)
    return (x - m) / torch.sqrt(v + eps)
def _scatter_add(tgt, idx, src, axis=0):
    return tgt.clone().index_add_(axis, idx, src)
def _segment_sum(src, seg, n):
    out = torch.zeros((n,) + src.shape[1:], dtype=src.dtype)
    return out.index_add_(0, seg, src)
def _pad(x, pw):
    flat = tuple(v for pair in reversed(pw) for v in pair)
    return torch.nn.functional.pad(x, flat)

kernel_name = 'sequential_preLN__mha_block__mixer_s_mlp__prompt'

def setup_inputs():

    _w = (lambda a: (lambda t: t.to(torch.bfloat16) if t.is_floating_point() else t)(torch.as_tensor(a)))
    def _A_setup():
        B, S, D, H = 1, 2048, 640, 32
        return {
            "x": _w((lambda *s: torch.randn(*s, dtype=torch.bfloat16) * 0.05)(B, S, D)),
            "wqkv": _w((lambda *s: torch.randn(*s, dtype=torch.bfloat16) * 0.05)(D, 3 * D)),
            "wo": _w((lambda *s: torch.randn(*s, dtype=torch.bfloat16) * 0.05)(D, D)),
        }
    _A_in = _A_setup()

    def _B_setup():
        B, S, D, F = 1, 2048, 640, 1728
        return {
            "x":  _w((lambda *s: torch.randn(*s, dtype=torch.bfloat16) * 0.05)(B, S, D)),
            "w1": _w((lambda *s: torch.randn(*s, dtype=torch.bfloat16) * 3.2)(D, D) * (D ** -0.5)),
            "w2": _w((lambda *s: torch.randn(*s, dtype=torch.bfloat16) * 3.2)(D, D) * (D ** -0.5)),
            "w3": _w((lambda *s: torch.randn(*s, dtype=torch.bfloat16) * 3.2)(D, F) * (D ** -0.5)),
            "w4": _w((lambda *s: torch.randn(*s, dtype=torch.bfloat16) * 3.2)(F, D) * (F ** -0.5)),
        }
    _B_in = _B_setup()

    return {
        "A_x": _A_in["x"],
        "A_wqkv": _A_in["wqkv"],
        "A_wo": _A_in["wo"],
        "B_x": _B_in["x"],
        "B_w1": _B_in["w1"],
        "B_w2": _B_in["w2"],
        "B_w3": _B_in["w3"],
        "B_w4": _B_in["w4"],
    }

def reference(A_x, A_wqkv, A_wo, B_x, B_w1, B_w2, B_w3, B_w4):
    def _A_ref(x, wqkv, wo):
        B, S, D = x.shape
        H = 32
        Dh = D // H
        qkv = (lambda x, *d: x.permute(*d))((x @ wqkv).reshape(B, S, 3, H, Dh), 2, 0, 3, 1, 4)
        q, k, v = qkv[0], qkv[1], qkv[2]
        scores = torch.einsum("bhqd,bhkd->bhqk", q, k) * (Dh ** -0.5)
        p = _softmax(scores, axis=-1)
        o = torch.einsum("bhqk,bhkd->bhqd", p, v)
        return (lambda x, *d: x.permute(*d))(o, 0, 2, 1, 3).reshape(B, S, D) @ wo

    def _B_ref(x, w1, w2, w3, w4):
        B, S, D = x.shape
        h = _gelu(_layernorm(x, axis=-1) @ w1)
        c = D // 4
        g0 = _pad(h[:, :, 0*c:1*c], ((0, 0), (1, 0), (0, 0)))[:, :S, :]
        g1 = _pad(h[:, :, 1*c:2*c], ((0, 0), (0, 1), (0, 0)))[:, 1:, :]
        g2 = _pad(h[:, :, 2*c:3*c], ((0, 0), (2, 0), (0, 0)))[:, :S, :]
        g3 = _pad(h[:, :, 3*c:   ], ((0, 0), (0, 2), (0, 0)))[:, 2:, :]
        hs = torch.concatenate([g0, g1, g2, g3], axis=-1) @ w2
        x1 = x + hs
        h2 = _gelu(_layernorm(x1, axis=-1) @ w3) @ w4
        return x1 + h2

    x = A_x
    h = x + _A_ref(_layernorm(x, axis=-1), A_wqkv, A_wo)
    h2 = h + _B_ref(_layernorm(h, axis=-1), B_w1, B_w2, B_w3, B_w4)
    return h2
```

```cpp
#include <hip/hip_runtime.h>
#include <hip/hip_bf16.h>
#define S_ 2048
#define D_ 640
#define H_ 32
#define DH 20
#define F_ 1728
typedef __hip_bfloat16 bf16;
__device__ inline float b2f(bf16 x) { return __bfloat162float(x); }

__global__ void ln_kernel(const float* in, float* out) {
  int r = blockIdx.x; __shared__ float red[2][128];
  float s = 0; for (int c = threadIdx.x; c < D_; c += 128) s += in[r * D_ + c];
  red[0][threadIdx.x] = s; __syncthreads();
  float m = 0; for (int i = 0; i < 128; ++i) m += red[0][i]; m /= D_;
  float v = 0; for (int c = threadIdx.x; c < D_; c += 128) { float d = in[r * D_ + c] - m; v += d * d; }
  red[1][threadIdx.x] = v; __syncthreads();
  float vv = 0; for (int i = 0; i < 128; ++i) vv += red[1][i]; vv /= D_;
  float rs = rsqrtf(vv + 1e-5f);
  for (int c = threadIdx.x; c < D_; c += 128) out[r * D_ + c] = (in[r * D_ + c] - m) * rs;
}
__global__ void cvt_kernel(const bf16* in, float* out, int n) { int i = blockIdx.x * 256 + threadIdx.x; if (i < n) out[i] = b2f(in[i]); }
__global__ void gemm_kernel(const float* A, const bf16* W, float* C, const float* res, int N, int K, int mode, int shift) {
  __shared__ float As[16][33], Ws[32][17];
  int tx = threadIdx.x & 15, ty = threadIdx.x >> 4;
  int row = blockIdx.y * 16 + ty, col = blockIdx.x * 16 + tx;
  float acc = 0;
  for (int k0 = 0; k0 < K; k0 += 32) {
    for (int j = 0; j < 2; ++j) {
      int k = k0 + tx + 16 * j; int r = row;
      if (shift) { int q = k / 160; int d = q == 0 ? -1 : q == 1 ? 1 : q == 2 ? -2 : 2; r = row + d; }
      As[ty][tx + 16 * j] = (r >= 0 && r < S_) ? A[r * K + k] : 0.f;
      Ws[ty + 16 * j][tx] = b2f(W[(k0 + ty + 16 * j) * N + col]);
    }
    __syncthreads();
    for (int k = 0; k < 32; ++k) acc += As[ty][k] * Ws[k][tx];
    __syncthreads();
  }
  if (mode == 1) { float u = 0.7978845608f * (acc + 0.044715f * acc * acc * acc); acc = 0.5f * acc * (1.f + tanhf(u)); }
  if (mode == 2) acc += res[row * N + col];
  C[row * N + col] = acc;
}
__global__ void attn_kernel(const float* qkv, float* o) {
  int h = blockIdx.y, q = blockIdx.x * 64 + threadIdx.x;
  float qv[DH], acc[DH]; for (int d = 0; d < DH; ++d) { qv[d] = qkv[q * 1920 + h * DH + d] * 0.22360679775f; acc[d] = 0; }
  float m = -1e30f, l = 0;
  __shared__ float Ks[64][DH], Vs[64][DH];
  for (int k0 = 0; k0 < S_; k0 += 64) {
    for (int d = 0; d < DH; ++d) { Ks[threadIdx.x][d] = qkv[(k0 + threadIdx.x) * 1920 + 640 + h * DH + d]; Vs[threadIdx.x][d] = qkv[(k0 + threadIdx.x) * 1920 + 1280 + h * DH + d]; }
    __syncthreads();
    for (int k = 0; k < 64; ++k) {
      float s = 0; for (int d = 0; d < DH; ++d) s += qv[d] * Ks[k][d];
      float mn = fmaxf(m, s); float a = __expf(m - mn), p = __expf(s - mn);
      l = l * a + p; for (int d = 0; d < DH; ++d) acc[d] = acc[d] * a + p * Vs[k][d]; m = mn;
    }
    __syncthreads();
  }
  for (int d = 0; d < DH; ++d) o[q * D_ + h * DH + d] = acc[d] / l;
}
__global__ void final_kernel(const float* h, const float* x1, const float* h2, bf16* out, int n) { int i = blockIdx.x * 256 + threadIdx.x; if (i < n) out[i] = __float2bfloat16(h[i] + x1[i] + h2[i]); }

extern "C" void kernel_launch(void* const* d_in, const int* in_sizes, int n_in, void* d_out, int out_size, void* d_ws, size_t ws_size, hipStream_t stream) {
  const bf16* x = (const bf16*)d_in[0]; const bf16* wqkv = (const bf16*)d_in[1]; const bf16* wo = (const bf16*)d_in[2];
  const bf16* w1 = (const bf16*)d_in[4]; const bf16* w2 = (const bf16*)d_in[5]; const bf16* w3 = (const bf16*)d_in[6]; const bf16* w4 = (const bf16*)d_in[7];
  float* ws = (float*)d_ws; const int SD = S_ * D_;
  float* xf = ws; float* xn = xf + SD; float* qkv = xn + SD; float* o = qkv + 3 * SD; float* h = o + SD; float* y = h + SD; float* y2 = y + SD; float* h1 = y2 + SD; float* x1 = h1 + SD; float* x1n = x1 + SD; float* g = x1n + SD; float* h2 = g + S_ * F_;
  cvt_kernel<<<SD / 256, 256, 0, stream>>>(x, xf, SD);
  ln_kernel<<<S_, 128, 0, stream>>>(xf, xn);
  gemm_kernel<<<dim3(1920 / 16, S_ / 16), 256, 0, stream>>>(xn, wqkv, qkv, nullptr, 1920, D_, 0, 0);
  attn_kernel<<<dim3(S_ / 64, H_), 64, 0, stream>>>(qkv, o);
  gemm_kernel<<<dim3(D_ / 16, S_ / 16), 256, 0, stream>>>(o, wo, h, xf, D_, D_, 2, 0);
  ln_kernel<<<S_, 128, 0, stream>>>(h, y);
  ln_kernel<<<S_, 128, 0, stream>>>(y, y2);
  gemm_kernel<<<dim3(D_ / 16, S_ / 16), 256, 0, stream>>>(y2, w1, h1, nullptr, D_, D_, 1, 0);
  gemm_kernel<<<dim3(D_ / 16, S_ / 16), 256, 0, stream>>>(h1, w2, x1, y, D_, D_, 2, 1);
  ln_kernel<<<S_, 128, 0, stream>>>(x1, x1n);
  gemm_kernel<<<dim3(F_ / 16, S_ / 16), 256, 0, stream>>>(x1n, w3, g, nullptr, F_, D_, 1, 0);
  gemm_kernel<<<dim3(D_ / 16, S_ / 16), 256, 0, stream>>>(g, w4, h2, nullptr, D_, F_, 0, 0);
  final_kernel<<<SD / 256, 256, 0, stream>>>(h, x1, h2, (bf16*)d_out, SD);
}
```

```cpp
#include <hip/hip_runtime.h>
#include <hip/hip_bf16.h>

typedef __attribute__((ext_vector_type(8))) short bf16x8;
typedef __attribute__((ext_vector_type(4))) short s16x4;
typedef __attribute__((ext_vector_type(2))) __bf16 bf16x2;
typedef __attribute__((ext_vector_type(2))) float f32x2;
typedef __attribute__((ext_vector_type(4))) float f32x4;
typedef __attribute__((ext_vector_type(16))) float f32x16;
typedef __attribute__((ext_vector_type(4))) unsigned u32x4;
typedef __attribute__((ext_vector_type(2))) unsigned u32x2;
typedef __attribute__((address_space(3))) const char* lds_cptr;
typedef __attribute__((address_space(3))) char* lds_ptr;
#define DI __device__ __forceinline__
#define SBAR() __builtin_amdgcn_sched_barrier(0)
#define PIN(x) asm volatile("" : "+v"(x))
#define MFMA16(a, b, c) __builtin_amdgcn_mfma_f32_16x16x32_bf16((a), (b), (c), 0, 0, 0)
#define MFMA32(a, b, c) __builtin_amdgcn_mfma_f32_32x32x16_bf16((a), (b), (c), 0, 0, 0)

constexpr int S_ = 2048, D_ = 640, F_ = 1728;
constexpr float LN_EPS = 1e-5f;
constexpr float QSCALE = 0.22360679774997896f * 1.4426950408889634f;
constexpr unsigned SENT = 0xFFFFFFFFu;

constexpr unsigned QF_TILE = 1280, KV_REC = 2688, FM_RT = 20 * 1024, G_RT = 54 * 1024;
constexpr unsigned SZ_FM = 128 * FM_RT;
constexpr unsigned OFF_QF = 0;
constexpr unsigned OFF_KV = OFF_QF + 32 * 64 * QF_TILE;
constexpr unsigned OFF_O = OFF_KV + 32 * 64 * KV_REC;
constexpr unsigned OFF_H = OFF_O + SZ_FM;
constexpr unsigned OFF_H1 = OFF_H + SZ_FM;
constexpr unsigned OFF_X1 = OFF_H1 + SZ_FM;
constexpr unsigned OFF_G = OFF_X1 + SZ_FM;
constexpr unsigned OFF_ST1 = OFF_G + 128 * G_RT;
constexpr unsigned OFF_ST2 = OFF_ST1 + 128 * 1024;
constexpr int NGRP = 35;
constexpr unsigned GRP_B = 25 * 1024;
constexpr unsigned OFF_WF = OFF_ST2 + 128 * 1024;
constexpr unsigned OFF_CS1 = OFF_WF + 8 * NGRP * GRP_B;
constexpr unsigned OFF_CS3 = OFF_CS1 + 20 * 640 * 4;
constexpr unsigned WS_END = OFF_CS3 + 20 * 1728 * 4;

DI unsigned pack2(float lo, float hi) { f32x2 v = {lo, hi}; return __builtin_bit_cast(unsigned, __builtin_convertvector(v, bf16x2)); }
DI float bflo(unsigned u) { return __uint_as_float(u << 16); }
DI float bfhi(unsigned u) { return __uint_as_float(u & 0xffff0000u); }
DI float dot2(unsigned a, unsigned b, float c) { return __builtin_amdgcn_fdot2_f32_bf16(__builtin_bit_cast(bf16x2, a), __builtin_bit_cast(bf16x2, b), c, false); }
DI float sum_q(float x) {
  auto s = __builtin_amdgcn_permlane16_swap(__float_as_uint(x), __float_as_uint(x), false, false);
  x = __uint_as_float(s[0]) + __uint_as_float(s[1]);
  auto t = __builtin_amdgcn_permlane32_swap(__float_as_uint(x), __float_as_uint(x), false, false);
  return __uint_as_float(t[0]) + __uint_as_float(t[1]);
}
template <int N> struct IC { static constexpr int value = N; };
template <int N, int I = 0, class Fn> DI void static_for(Fn&& f) { if constexpr (I < N) { f(IC<I>{}); static_for<N, I + 1>(f); } }
DI int swap23(int r) { return (r & ~12) | ((r & 4) << 1) | ((r & 8) >> 1); }
DI bf16x8 lds16(lds_cptr p) { return *(const __attribute__((address_space(3))) bf16x8*)p; }
typedef short v4i16_t __attribute__((ext_vector_type(4)));
DI s16x4 ldstr(lds_cptr p) { return __builtin_bit_cast(s16x4, __builtin_amdgcn_ds_read_tr16_b64_v4i16((__attribute__((address_space(3))) v4i16_t*)p)); }
DI void dma1(const char* sbase, unsigned voff, unsigned ldsdst) {
  unsigned sv;
  asm volatile("s_mov_b32 %0, m0\n\ts_mov_b32 m0, %3\n\ts_nop 0\n\tglobal_load_lds_dwordx4 %1, %2\n\ts_mov_b32 m0, %0" : "=&s"(sv) : "v"(voff), "s"(sbase), "s"(ldsdst) : "memory");
}
DI void dma4(const char* sbase, unsigned voff, unsigned ldsdst) {
  unsigned sv;
  asm volatile("s_mov_b32 %0, m0\n\ts_mov_b32 m0, %3\n\ts_nop 0\n\tglobal_load_lds_dwordx4 %1, %2\n\tglobal_load_lds_dwordx4 %1, %2 offset:1024\n\t"
               "global_load_lds_dwordx4 %1, %2 offset:2048\n\tglobal_load_lds_dwordx4 %1, %2 offset:3072\n\ts_mov_b32 m0, %0" : "=&s"(sv) : "v"(voff), "s"(sbase), "s"(ldsdst) : "memory");
}
DI const char* uni_ptr(const char* p) {
  unsigned long long v = (unsigned long long)p;
  unsigned lo = __builtin_amdgcn_readfirstlane((unsigned)v), hi = __builtin_amdgcn_readfirstlane((unsigned)(v >> 32));
  return (const char*)(((unsigned long long)hi << 32) | lo);
}

constexpr int K1_W_BYTES = 640 * 240, K1_CS = K1_W_BYTES, K1_ST = K1_CS + 512, K1_LDS = K1_ST + 1024;

DI int k1_quad(int nT, int t, int p) {
  if (t >= nT) return (nT == 0 ? 4 * t : 4 * t) + p;
  const int npair2 = (nT == 8) ? 6 : 4;
  if (t < npair2) return 10 * (t >> 1) + (t & 1) + 2 * (p & 1) + 5 * (p >> 1);
  const int hd = 4 * (t - npair2) + p;
  return hd < 6 ? 5 * hd + 4 : 30 + (p & 1);
}

#define OWN_ALL_VGPRS() asm volatile("" ::: "v255")
template <int R> DI float vget() { float r; asm volatile("v_mov_b32 %0, v[%c1]" : "=v"(r) : "n"(R)); return r; }
template <int R> DI void vset(unsigned v) { asm volatile("v_mov_b32 v[%c1], %0" ::"v"(v), "n"(R)); }
template <int R, int OFF> DI void gload4(const char* sbase, unsigned voff) {
  asm volatile("global_load_dwordx4 v[%c2:%c2+3], %0, %1 offset:%c3" ::"v"(voff), "s"(sbase), "n"(R), "n"(OFF) : "memory");
}
template <int R> DI f32x4 acc_sub(float m, f32x4 c) {
  float a0, a1, a2, a3;
  asm volatile("v_fma_f32 %0, -%4, %5, v[%c9]\n\tv_fma_f32 %1, -%4, %6, v[%c9+1]\n\tv_fma_f32 %2, -%4, %7, v[%c9+2]\n\tv_fma_f32 %3, -%4, %8, v[%c9+3]"
               : "=&v"(a0), "=&v"(a1), "=&v"(a2), "=&v"(a3) : "v"(m), "v"(c[0]), "v"(c[1]), "v"(c[2]), "v"(c[3]), "n"(R));
  return f32x4{a0, a1, a2, a3};
}

template <int NT>
DI void k1_body(char* ws, lds_ptr lds, int rb, int cg, int w, int lane) {
  const int q = lane >> 4, tok = lane & 15;
  {
    const unsigned rowpart = (unsigned)(size_t)lds + (16 * (q >> 1) + 8 * (q & 1) + 2 * ((lane >> 2) & 3)) * 240;
    vset<232>(rowpart + 8 * k1_quad(NT, 0, lane & 3)); vset<233>(rowpart + 8 * k1_quad(NT, 1, lane & 3)); vset<234>(rowpart + 8 * k1_quad(NT, 2, lane & 3));
    vset<235>(rowpart + 8 * k1_quad(NT, 3, lane & 3)); vset<236>(rowpart + 8 * k1_quad(NT, 4, lane & 3)); vset<237>(rowpart + 8 * k1_quad(NT, 5, lane & 3));
    vset<238>(rowpart + 8 * k1_quad(NT, 6, lane & 3)); vset<239>(rowpart + 8 * k1_quad(NT, 7, lane & 3)); vset<240>(rowpart + 8 * k1_quad(NT, w, lane & 3));
  }
  asm volatile(
      "v_mov_b32 v241, 0\n\tv_mov_b32 v242, 0\n\tv_mov_b32 v244, 0x3f803f80\n\tv_mov_b32 v245, v244\n\tv_mov_b32 v246, v244\n\tv_mov_b32 v247, v244\n\t"
      "s_waitcnt vmcnt(36)\n\ts_barrier\n\t"
      ".set f_, 0\n\t.rept 5\n\t"
      "ds_read_b64_tr_b16 v[212+4*f_:213+4*f_], v[232+f_]\n\tds_read_b64_tr_b16 v[214+4*f_:215+4*f_], v[232+f_] offset:240\n\t"
      ".set f_, f_+1\n\t.endr\n\t"
      ".set f_, 0\n\t.rept 180\n\t"
      ".set s_, f_/9\n\t.set t_, f_-9*s_\n\t.set r_, 212+4*(f_-5*(f_/5))\n\t.set g_, f_+5\n\t.set gs_, g_/9\n\t.set gt_, g_-9*gs_\n\t"
      ".if g_ < 180\n\t"
      ".if (g_-18*(g_/18)) == 0\n\ts_waitcnt vmcnt(36-4*(g_/18))\n\ts_barrier\n\t.endif\n\t"
      "s_waitcnt lgkmcnt(8)\n\t"
      ".else\n\ts_waitcnt lgkmcnt(2*(179-f_))\n\t.endif\n\t"
      ".if t_ == 8\n\t"
      ".if s_ == 0\n\tv_mfma_f32_16x16x32_bf16 v[208:211], v[r_:r_+3], v[244:247], 0\n\t.else\n\tv_mfma_f32_16x16x32_bf16 v[208:211], v[r_:r_+3], v[244:247], v[208:211]\n\t.endif\n\t"
      ".elseif t_ < %c0\n\t"
      ".if s_ == 0\n\tv_mfma_f32_16x16x32_bf16 v[176+4*t_:179+4*t_], v[r_:r_+3], v[64+4*s_:67+4*s_], 0\n\t"
      ".else\n\tv_mfma_f32_16x16x32_bf16 v[176+4*t_:179+4*t_], v[r_:r_+3], v[64+4*s_:67+4*s_], v[176+4*t_:179+4*t_]\n\t.endif\n\t"
      ".else\n\t"
      ".if s_ == 0\n\tv_mfma_f32_16x16x32_bf16 v[176+4*t_:179+4*t_], v[64+4*s_:67+4*s_], v[r_:r_+3], 0\n\t"
      ".else\n\tv_mfma_f32_16x16x32_bf16 v[176+4*t_:179+4*t_], v[64+4*s_:67+4*s_], v[r_:r_+3], v[176+4*t_:179+4*t_]\n\t.endif\n\t"
      ".endif\n\t"
      ".if g_ < 180\n\t"
      ".if gs_ == 8 || gs_ == 16\n\tv_add_u32 v[232+gt_], 61440, v[232+gt_]\n\t.endif\n\t"
      "ds_read_b64_tr_b16 v[r_:r_+1], v[232+gt_] offset:7680*(gs_&7)\n\tds_read_b64_tr_b16 v[r_+2:r_+3], v[232+gt_] offset:7680*(gs_&7)+240\n\t"
      ".endif\n\t"
      ".if t_ < 4\n\tv_dot2c_f32_bf16 v241, v[64+4*s_+t_], v244\n\t.elseif t_ < 8\n\tv_dot2c_f32_bf16 v242, v[64+4*s_+t_-4], v[64+4*s_+t_-4]\n\t.endif\n\t"
      ".set f_, f_+1\n\t.endr\n\t"
      "s_nop 7"
      ::"n"(NT) : "memory");
  float sm = vget<241>(), sq = vget<242>();
  const f32x4 csa = {vget<208>(), vget<209>(), vget<210>(), vget<211>()};
  sm = sum_q(sm); sq = sum_q(sq);
  const float mean = sm * (1.f / D_);
  const float rstd = __builtin_amdgcn_rsqf(fmaxf(sq * (1.f / D_) - mean * mean, 0.f) + LN_EPS);
  if (tok == 0) *(__attribute__((address_space(3))) f32x4*)(lds + K1_CS + w * 64 + q * 16) = csa;
  if (NT < 8 && q == 0) *(__attribute__((address_space(3))) f32x2*)(lds + K1_ST + w * 128 + tok * 8) = f32x2{mean, rstd};
  __syncthreads();
  const int T = 128 * rb + 16 * w + tok, kt = T >> 5, r32 = T & 31;
  auto qk_base = [&](int u, int rowb) -> char* {
    return u < 32 ? ws + OFF_QF + (unsigned)(u * 64 + kt) * QF_TILE + r32 * rowb : ws + OFF_KV + (unsigned)((u - 32) * 64 + kt) * KV_REC + swap23(r32) * rowb;
  };
  constexpr int NP2 = NT == 8 ? 6 : (NT == 5 ? 4 : 0);
  static_for<NP2 / 2>([&](auto P_) {
    constexpr int t = 2 * P_.value;
    const f32x4 c0 = *(const __attribute__((address_space(3))) f32x4*)(lds + K1_CS + t * 64 + q * 16);
    const f32x4 c1 = *(const __attribute__((address_space(3))) f32x4*)(lds + K1_CS + t * 64 + 64 + q * 16);
    const int u = 6 * cg + t + (q >> 1);
    const float sc = u < 32 ? rstd * QSCALE : rstd;
    const f32x4 a0 = acc_sub<176 + 4 * t>(mean, c0) * sc, a1 = acc_sub<180 + 4 * t>(mean, c1) * sc;
    *(u32x4*)(qk_base(u, 16) + (q & 1) * 512) = u32x4{pack2(a0[0], a0[1]), pack2(a0[2], a0[3]), pack2(a1[0], a1[1]), pack2(a1[2], a1[3])};
  });
  static_for<NT - NP2>([&](auto P_) {
    constexpr int t = NP2 + P_.value;
    const f32x4 c0 = *(const __attribute__((address_space(3))) f32x4*)(lds + K1_CS + t * 64 + q * 16);
    const int hd = 4 * (t - NP2) + q, u = 6 * cg + hd;
    const float sc = u < 32 ? rstd * QSCALE : rstd;
    const f32x4 a0 = acc_sub<176 + 4 * t>(mean, c0) * sc;
    if (hd < 6) *(u32x2*)(qk_base(u, 8) + 1024) = u32x2{pack2(a0[0], a0[1]), pack2(a0[2], a0[3])};
  });
  if (NT < 8) {
    const f32x4 s0 = *(const __attribute__((address_space(3))) f32x4*)(lds + K1_ST + w * 128 + q * 32);
    const f32x4 s1 = *(const __attribute__((address_space(3))) f32x4*)(lds + K1_ST + w * 128 + q * 32 + 16);
    const int ktw = 4 * rb + (w >> 1), rec = (ktw + 1) & 63;
    const unsigned vpos = 1280 + (2 * (w & 1) + (q >> 1)) * 352 + (q & 1) * 8;
    const f32x4 mv = {s0[0], s0[2], s1[0], s1[2]};
    static_for<8 - NT>([&](auto P_) {
      constexpr int t = NT + P_.value;
      const float cs = *(const __attribute__((address_space(3))) float*)(lds + K1_CS + t * 64 + tok * 4);
      const int cl = 16 * t + tok;
      int i = (cl * 3277) >> 16, row = cl - 20 * i;
      row = row < 16 ? swap23(row) : row;
      const f32x4 a0 = acc_sub<176 + 4 * t>(cs, mv);
      u32x2 o = {pack2(a0[0] * s0[1], a0[1] * s0[3]), pack2(a0[2] * s1[1], a0[3] * s1[3])};
      bool ok = true;
      if (t == 7 && cl >= 120) { i = cl - 120 + (NT == 5 ? 4 : 0); row = 20; o[0] = 0x3f803f80u; o[1] = 0x3f803f80u; ok = i < 6; }
      const int hv = 6 * cg + i - 64;
      if (ok) *(u32x2*)(ws + OFF_KV + (unsigned)(hv * 64 + rec) * KV_REC + vpos + row * 16) = o;
    });
  }
}

__global__ void __launch_bounds__(512, 2) __attribute__((amdgpu_num_vgpr(32))) k_qkv(const char* __restrict__ x, const char* __restrict__ wqkv, const char* __restrict__ wo, const char* __restrict__ w1,
                                                 const char* __restrict__ w2, const char* __restrict__ w3, const char* __restrict__ w4, char* __restrict__ ws) {
  extern __shared__ __attribute__((aligned(16))) char smem[];
  lds_ptr lds = (lds_ptr)smem;
  OWN_ALL_VGPRS();
  const int tid = threadIdx.x, lane = tid & 63, w = __builtin_amdgcn_readfirstlane(tid >> 6), q = lane >> 4, tok = lane & 15;
  const int bid = blockIdx.x, xcd = bid & 7, r = bid >> 3;
  const int rb = 4 * (xcd & 3) + (r & 3), cg = 8 * (xcd >> 2) + (r >> 2);

  const int job = w * 256 + bid;
  int mat, ks, cb;
  if (job < 300) { mat = job / 100; const int e = job - 100 * mat; ks = e / 5; cb = e - 5 * ks; }
  else if (job < 580) { mat = 3; const int e = job - 300; ks = e / 14; cb = e - 14 * ks; }
  else { mat = 4; const int e = job - 580; ks = e / 5; cb = e - 5 * ks; }
  const bool has_job = job < 850;
  const int n0 = 128 * cb + 8 * tok;
  const bool job_lane = has_job && (mat != 3 || n0 < F_);
  if (has_job) {
    const char* src = mat == 0 ? wo : mat == 1 ? w1 : mat == 2 ? w2 : mat == 3 ? w3 : w4;
    const int N = mat == 3 ? F_ : D_;
    int ka = 32 * ks + 8 * q, kb = ka + 4;
    if (mat == 0) { const int o = 4 * ks + q, i = o / 5, oo = o - 5 * i; ka = 40 * i + (oo == 0 ? 0 : oo == 1 ? 8 : oo == 2 ? 16 : oo == 3 ? 20 : 28); kb = ka + (oo == 2 ? 20 : 4); }
    const int nc = job_lane ? n0 : 0;
    static_for<8>([&](auto E_) {
      constexpr int e = E_.value;
      const unsigned off = (unsigned)(((e < 4 ? ka : kb - 4) + e) * N + nc) * 2;
      gload4<144 + 4 * e, 0>(src, off);
    });
  }
  {
    const u32x4 sent = {SENT, SENT, SENT, SENT};
    char* p = ws + OFF_O + bid * 10240 + w * 1024 + lane * 16;
    *(u32x4*)p = sent;
    if (w < 2) *(u32x4*)(p + 8192) = sent;
    const int i = (w == 2) ? bid : 256 + bid;
    if (w == 2 || (w == 3 && bid < 64)) { const int e = i / 20, k = i - 20 * e, rte = 16 * (e >> 1) + ((e & 1) ? 15 : 0); *(u32x4*)(ws + OFF_H1 + rte * FM_RT + k * 1024 + lane * 16) = sent; }
  }
  {
    unsigned voff[2];
#pragma unroll
    for (int a = 0; a < 2; ++a) {
      const int pi = a == 0 ? w : (w < 7 ? w + 8 : 7);
      const int L = 64 * pi + lane, rho = (L * 4370) >> 16, ch = L - 15 * rho;
      const int k = (rho & ~15) + ((rho >> 1) & 3) + 4 * (rho & 1) + (rho & 8);
      voff[a] = (unsigned)(k * 3840 + 240 * cg + 16 * ch);
    }
    const unsigned ldsb = (unsigned)(size_t)lds;
    const unsigned d0 = ldsb + w * 1024, d1 = ldsb + (w < 7 ? w + 8 : 7) * 1024;
    const unsigned xo = (unsigned)((128 * rb + 16 * w + tok) * D_ + 8 * q) * 2;
    static_for<10>([&](auto P_) {
      constexpr int p = P_.value;
      dma1(wqkv + (size_t)p * 64 * 3840, voff[0], d0 + p * 15360);
      dma1(wqkv + (size_t)p * 64 * 3840, voff[1], d1 + p * 15360);
      gload4<64 + 8 * p, 128 * p>(x, xo);
      gload4<68 + 8 * p, 128 * p + 64>(x, xo);
    });
  }
  if (cg <= 9) k1_body<8>(ws, lds, rb, cg, w, lane);
  else if (cg == 10) k1_body<5>(ws, lds, rb, cg, w, lane);
  else k1_body<0>(ws, lds, rb, cg, w, lane);

  if (has_job) {
    const int kk = 4 * cb + (tok >> 2);
    int j, tbase, row0, G, kkin;
    bool single = false;
    if (mat == 3) {
      const int half = kk >= 27, k27 = kk - 27 * half, jj = min(k27 / 7, 3);
      j = 4 * half + jj; tbase = 2 * (k27 - 7 * jj); row0 = 4 * (tok & 3); G = 12 + ks / 5; kkin = ks % 5;
    } else {
      const int per5 = kk / 5, m5 = kk - 5 * per5;
      single = m5 == 2;
      j = 2 * per5 + (single ? ((tok >> 1) & 1) : (m5 > 2)); tbase = single ? 4 : 2 * (m5 > 2 ? m5 - 3 : m5);
      row0 = single ? 8 * (tok & 1) : 4 * (tok & 3);
      G = (mat == 4 ? 24 : 4 * mat) + ks / 5; kkin = ks % 5;
    }
    float cs[8];
    static_for<8>([&](auto I_) {
      constexpr int i = I_.value;
      unsigned o0, o1, o2, o3;
      asm volatile("v_perm_b32 %0, v[%c5+4], v[%c5], %4\n\tv_perm_b32 %1, v[%c5+12], v[%c5+8], %4\n\tv_perm_b32 %2, v[%c5+20], v[%c5+16], %4\n\tv_perm_b32 %3, v[%c5+28], v[%c5+24], %4"
                   : "=v"(o0), "=v"(o1), "=v"(o2), "=v"(o3) : "s"((i & 1) ? 0x07060302u : 0x05040100u), "n"(144 + (i >> 1)));
      const u32x4 o = {o0, o1, o2, o3};
      int t = single ? 4 : tbase + (i >> 2), Gi = G;
      const int row = single ? row0 + i : row0 + (i & 3);
      if (mat == 3) { const int pass = t / 5; t -= 5 * pass; Gi += 4 * pass; }
      if (job_lane) *(u32x4*)(ws + OFF_WF + (unsigned)((j * NGRP + Gi) * 25 + kkin * 5 + t) * 1024 + (q * 16 + row) * 16) = o;
      float c = 0.f;
#pragma unroll
      for (int d = 0; d < 4; ++d) c = dot2(o[d], 0x3f803f80u, c);
      cs[i] = sum_q(c);
    });
    if ((mat == 1 || mat == 3) && q == 0 && job_lane) {
      float* cp = (float*)(ws + (mat == 1 ? OFF_CS1 : OFF_CS3)) + ks * (mat == 1 ? D_ : F_) + n0;
      *(f32x4*)cp = f32x4{cs[0], cs[1], cs[2], cs[3]};
      *(f32x4*)(cp + 4) = f32x4{cs[4], cs[5], cs[6], cs[7]};
    }
  }
}

constexpr int KV_CHUNK = 8 * KV_REC;
constexpr int L_KV0 = 4 * GRP_B, L_MISC = 6 * GRP_B, L_CS1 = L_MISC, L_CS3 = L_CS1 + 320, L_FLAG = L_CS3 + 960, K2_LDS = L_FLAG + 64;
constexpr int AUX_SC1 = 16;
constexpr unsigned SPIN_CAP = 200000;

DI __amdgpu_buffer_rsrc_t mk_rsrc(const char* p, unsigned bytes) { return __builtin_amdgcn_make_buffer_rsrc((void*)p, 0, bytes, 0x00020000); }
DI u32x4 ld_sc1(__amdgpu_buffer_rsrc_t rs, unsigned voff, unsigned soff) { return __builtin_amdgcn_raw_buffer_load_b128(rs, voff, soff, AUX_SC1); }
DI void st_sc1(u32x4 v, __amdgpu_buffer_rsrc_t rs, unsigned voff, unsigned soff) { __builtin_amdgcn_raw_buffer_store_b128(v, rs, voff, soff, AUX_SC1); }
DI void st_sc1(u32x2 v, __amdgpu_buffer_rsrc_t rs, unsigned voff, unsigned soff) { __builtin_amdgcn_raw_buffer_store_b64(v, rs, voff, soff, AUX_SC1); }
DI unsigned nosent(unsigned u) { return min(u, SENT - 1u); }
DI unsigned umax3(unsigned a, unsigned b, unsigned c) { return max(max(a, b), c); }
DI float gelu(float v) {
  const float c0 = -2.f * 1.4426950408889634f * 0.7978845608028654f, c1 = c0 * 0.044715f;
  const float e = __builtin_amdgcn_exp2f(v * __builtin_fmaf(v * v, c1, c0));
  return v * __builtin_amdgcn_rcpf(1.f + e);
}

DI f32x16 attn_safe(const char* kvh, lds_ptr lds, bf16x8 q1, u32x4 q2, int w, int lane) {
  const int hi = lane >> 5, r32 = lane & 31;
  const unsigned ldsb = (unsigned)(size_t)lds;
  f32x16 O = {};
  float m = -3.0e38f;
  q2[2] = 0;
  const bf16x8 q2b = __builtin_bit_cast(bf16x8, q2);
#pragma unroll 1
  for (int t = 0; t < 64; ++t) {
    __syncthreads();
    if (w < 3) dma1(kvh + (size_t)t * KV_REC + w * 1024, lane * 16, ldsb + L_KV0 + w * 1024);
    else if (w < 6) dma1(kvh + (size_t)((t + 1) & 63) * KV_REC + (w - 3) * 1024, lane * 16, ldsb + L_KV0 + w * 1024);
    asm volatile("s_waitcnt vmcnt(0)" ::: "memory");
    __syncthreads();
    lds_cptr kb = (lds_cptr)lds + L_KV0, vb = kb + 3072;
    const bf16x8 k1 = lds16(kb + hi * 512 + r32 * 16);
    const u32x2 k2l = *(const __attribute__((address_space(3))) u32x2*)(kb + 1024 + r32 * 8);
    const u32x4 k2 = {k2l[0], k2l[1], 0u, 0u};
    f32x16 s = MFMA32(k1, q1, f32x16{});
    s = MFMA32(__builtin_bit_cast(bf16x8, k2), q2b, s);
    float tm = s[0];
#pragma unroll
    for (int i = 1; i < 16; ++i) tm = fmaxf(tm, s[i]);
    { auto rr = __builtin_amdgcn_permlane32_swap(__float_as_uint(tm), __float_as_uint(tm), false, false); tm = fmaxf(__uint_as_float(rr[0]), __uint_as_float(rr[1])); }
    const float mn = fmaxf(m, tm), al = __builtin_amdgcn_exp2f(m - mn);
    m = mn;
    u32x4 p0, p1;
#pragma unroll
    for (int i = 0; i < 4; ++i) {
      p0[i] = pack2(__builtin_amdgcn_exp2f(s[2 * i] - mn), __builtin_amdgcn_exp2f(s[2 * i + 1] - mn));
      p1[i] = pack2(__builtin_amdgcn_exp2f(s[8 + 2 * i] - mn), __builtin_amdgcn_exp2f(s[9 + 2 * i] - mn));
    }
#pragma unroll
    for (int i = 0; i < 16; ++i) O[i] *= al;
    O = MFMA32(lds16(vb + 1280 + hi * 352 + r32 * 16), __builtin_bit_cast(bf16x8, p0), O);
    O = MFMA32(lds16(vb + 1280 + 704 + hi * 352 + r32 * 16), __builtin_bit_cast(bf16x8, p1), O);
  }
  __syncthreads();
  return O;
}

constexpr int R_A = 128, R_W = 208, R_ACC = 228;
DI u32x4 mk_srd(const char* p, unsigned bytes) { const unsigned long long v = (unsigned long long)p; return u32x4{(unsigned)v, (unsigned)(v >> 32) & 0xffffu, bytes, 0x00020000u}; }
template <int A0, int N> DI void ld_frags(u32x4 srd, unsigned voff, unsigned soff) {
  unsigned t;
  asm volatile("s_add_u32 %0, %3, 4096\n\t.set i_, 0\n\t.rept %c5\n\t"
               ".if i_ < 4\n\tbuffer_load_dwordx4 v[%c4+4*i_:%c4+4*i_+3], %1, %2, %3 offen offset:1024*i_ sc1\n\t"
               ".else\n\tbuffer_load_dwordx4 v[%c4+4*i_:%c4+4*i_+3], %1, %2, %0 offen sc1\n\t.endif\n\t.set i_, i_+1\n\t.endr"
               : "=&s"(t) : "v"(voff), "s"(srd), "s"(soff), "n"(R_A + 4 * A0), "n"(N) : "memory", "scc");
}
template <int A0, int N, int VM> DI unsigned long long check_frags() {
  unsigned long long m;
  asm volatile("s_waitcnt vmcnt(%c3)\n\tv_max_u32 v248, v[%c1], v[%c1+1]\n\t.set i_, 1\n\t.rept 2*%c2-1\n\tv_max3_u32 v248, v248, v[%c1+2*i_], v[%c1+2*i_+1]\n\t.set i_, i_+1\n\t.endr\n\t"
               "v_cmp_eq_u32 %0, -1, v248" : "=s"(m) : "n"(R_A + 4 * A0), "n"(N), "n"(VM) : "memory");
  return m;
}
template <int A0, int N> DI void repoll(unsigned long long m, u32x4 srd, unsigned voff, unsigned soff) {
  if (__builtin_expect(m != 0, 0)) {
    for (unsigned sp = 0; sp < SPIN_CAP; ++sp) {
      asm volatile("s_sleep 2" ::: "memory");
      ld_frags<A0, N>(srd, voff, soff);
      if (check_frags<A0, N, 0>() == 0) break;
    }
  }
}
template <int G, int NK, int A0, bool FIRST, int CN, int AN, int VM, int RL>
DI unsigned long long run_group(const unsigned (&wb)[3], u32x4 srd, unsigned voff, unsigned soff) {
  constexpr int GN = G + 1 < NGRP ? G + 1 : G;
  unsigned long long m; unsigned t;
  asm volatile(
      ".if %c[rl] > 4\n\ts_add_u32 %[t], %[soff], 4096\n\t.endif\n\t"
      ".set k_, 0\n\t.rept %c[nk]\n\t.set t_, 0\n\t.rept 5\n\t"
      "s_waitcnt lgkmcnt(4)\n\t"
      ".if %c[first] && k_ == 0\n\tv_mfma_f32_16x16x32_bf16 v[228+4*t_:231+4*t_], v[208+4*t_:211+4*t_], v[%c[a0]+4*k_:%c[a0]+4*k_+3], 0\n\t"
      ".else\n\tv_mfma_f32_16x16x32_bf16 v[228+4*t_:231+4*t_], v[208+4*t_:211+4*t_], v[%c[a0]+4*k_:%c[a0]+4*k_+3], v[228+4*t_:231+4*t_]\n\t.endif\n\t"
      ".if k_ < %c[nk]-1\n\tds_read_b128 v[208+4*t_:211+4*t_], %[cur] offset:%c[oc]+5120*(k_+1)+1024*t_\n\t"
      ".else\n\tds_read_b128 v[208+4*t_:211+4*t_], %[nxt] offset:%c[on]+1024*t_\n\t.endif\n\t"
      ".set c_, 5*(k_-(%c[nk]-2))+t_\n\t"
      ".if %c[cn] > 0 && c_ >= 0 && c_ < 2*%c[cn]\n\t"
      ".if c_ == 0\n\ts_waitcnt vmcnt(%c[vm])\n\tv_max_u32 v248, v[%c[an]], v[%c[an]+1]\n\t.else\n\tv_max3_u32 v248, v248, v[%c[an]+2*c_], v[%c[an]+2*c_+1]\n\t.endif\n\t"
      ".endif\n\t"
      ".set t_, t_+1\n\t.endr\n\t"
      ".if k_ < %c[rl]\n\t.if k_ < 4\n\tbuffer_load_dwordx4 v[%c[a0]+4*k_:%c[a0]+4*k_+3], %[voff], %[srd], %[soff] offen offset:1024*k_ sc1\n\t"
      ".else\n\tbuffer_load_dwordx4 v[%c[a0]+4*k_:%c[a0]+4*k_+3], %[voff], %[srd], %[t] offen sc1\n\t.endif\n\t.endif\n\t"
      ".set k_, k_+1\n\t.endr\n\t"
      ".if %c[cn] > 0\n\tv_cmp_eq_u32 %[m], -1, v248\n\t.else\n\ts_mov_b64 %[m], 0\n\t.endif\n\t"
      "s_barrier"
      : [m] "=&s"(m), [t] "=&s"(t)
      : [cur] "v"(wb[(G % 6) >> 1]), [nxt] "v"(wb[(GN % 6) >> 1]), [voff] "v"(voff), [srd] "s"(srd), [soff] "s"(soff),
        [oc] "n"(((G % 6) & 1) * GRP_B), [on] "n"(((GN % 6) & 1) * GRP_B), [nk] "n"(NK), [a0] "n"(R_A + 4 * A0), [first] "n"(FIRST ? 1 : 0),
        [cn] "n"(CN), [an] "n"(R_A + 4 * AN), [vm] "n"(VM), [rl] "n"(RL)
      : "memory", "scc");
  return m;
}
template <int G0>
DI void run_unit(const unsigned (&wb)[3], u32x4 srd, const unsigned (&vo)[4], unsigned soff) {
  repoll<0, 5>(check_frags<0, 5, 15>(), srd, vo[0], soff);
  repoll<5, 5>(run_group<G0, 5, 0, true, 5, 5, 10, 0>(wb, srd, 0, 0), srd, vo[1], soff + 5120);
  repoll<10, 5>(run_group<G0 + 1, 5, 5, false, 5, 10, 5, 0>(wb, srd, 0, 0), srd, vo[2], soff + 10240);
  repoll<15, 5>(run_group<G0 + 2, 5, 10, false, 5, 15, 0, 0>(wb, srd, 0, 0), srd, vo[3], soff + 15360);
  run_group<G0 + 3, 5, 15, false, 0, 0, 0, 0>(wb, srd, 0, 0);
  asm volatile("s_nop 6" ::: "memory");
}
template <int G0>
DI void run_unit_again(const unsigned (&wb)[3], u32x4 srd) {
  run_group<G0, 5, 0, true, 0, 0, 0, 0>(wb, srd, 0, 0); run_group<G0 + 1, 5, 5, false, 0, 0, 0, 0>(wb, srd, 0, 0);
  run_group<G0 + 2, 5, 10, false, 0, 0, 0, 0>(wb, srd, 0, 0); run_group<G0 + 3, 5, 15, false, 0, 0, 0, 0>(wb, srd, 0, 0);
  asm volatile("s_nop 6" ::: "memory");
}
DI void ld_unit(u32x4 srd, const unsigned (&vo)[4], unsigned soff) {
  ld_frags<0, 5>(srd, vo[0], soff); ld_frags<5, 5>(srd, vo[1], soff + 5120); ld_frags<10, 5>(srd, vo[2], soff + 10240); ld_frags<15, 5>(srd, vo[3], soff + 15360);
}
template <int R> DI void acc_add(float (&o)[4], const float (&c)[4]) {
  asm volatile("v_add_f32 %0, %4, v[%c8]\n\tv_add_f32 %1, %5, v[%c8+1]\n\tv_add_f32 %2, %6, v[%c8+2]\n\tv_add_f32 %3, %7, v[%c8+3]"
               : "=&v"(o[0]), "=&v"(o[1]), "=&v"(o[2]), "=&v"(o[3]) : "v"(c[0]), "v"(c[1]), "v"(c[2]), "v"(c[3]), "n"(R));
}
DI void store_tile(const float (&v)[5][4], __amdgpu_buffer_rsrc_t rs, unsigned rtoff, int j, int lane) {
  const int q = lane >> 4, tok = lane & 15;
  const unsigned kp = 5 * (j >> 1) + 3 * (j & 1), ksg = 5 * (j >> 1) + 2;
#pragma unroll
  for (int p = 0; p < 2; ++p) {
    u32x4 o = {nosent(pack2(v[2 * p][0], v[2 * p][1])), nosent(pack2(v[2 * p][2], v[2 * p][3])), nosent(pack2(v[2 * p + 1][0], v[2 * p + 1][1])), nosent(pack2(v[2 * p + 1][2], v[2 * p + 1][3]))};
    st_sc1(o, rs, lane * 16, rtoff + (kp + p) * 1024);
  }
  const unsigned d0 = nosent(pack2(v[4][0], v[4][1])), d1 = nosent(pack2(v[4][2], v[4][3]));
  auto e0 = __builtin_amdgcn_permlane16_swap(d0, d0, false, false);
  auto e1 = __builtin_amdgcn_permlane16_swap(d1, d1, false, false);
  if (!(q & 1)) st_sc1(u32x4{d0, d1, e0[1], e1[1]}, rs, (2 * (j & 1) + (q >> 1)) * 256 + tok * 16, rtoff + ksg * 1024);
}
DI void store_stats(const float (&v)[5][4], __amdgpu_buffer_rsrc_t rs, unsigned off, int j, int lane) {
  float s = 0.f, ss = 0.f;
#pragma unroll
  for (int t = 0; t < 5; ++t)
#pragma unroll
    for (int r = 0; r < 4; ++r) { s += v[t][r]; ss = __builtin_fmaf(v[t][r], v[t][r], ss); }
  s = sum_q(s); ss = sum_q(ss);
  if (lane < 16) st_sc1(u32x2{nosent(__float_as_uint(s)), nosent(__float_as_uint(ss))}, rs, (j >> 1) * 256 + lane * 16 + (j & 1) * 8, off);
}
DI void ld_stats(u32x4 srd, unsigned voff, unsigned soff) { asm volatile("buffer_load_dwordx4 v[250:253], %0, %1, %2 offen sc1" ::"v"(voff), "s"(srd), "s"(soff) : "memory"); }
DI void read_stats(u32x4 srd, unsigned voff, unsigned soff, float& mean, float& var) {
  float s0 = vget<250>(), s1 = vget<251>(), s2 = vget<252>(), s3 = vget<253>();
  if (__builtin_expect(__any(umax3(max(__float_as_uint(s0), __float_as_uint(s1)), __float_as_uint(s2), __float_as_uint(s3)) == SENT), 0)) {
    for (unsigned sp = 0; sp < SPIN_CAP; ++sp) {
      asm volatile("s_sleep 2" ::: "memory");
      ld_stats(srd, voff, soff);
      asm volatile("s_waitcnt vmcnt(0)" ::: "memory");
      s0 = vget<250>(); s1 = vget<251>(); s2 = vget<252>(); s3 = vget<253>();
      if (!__any(umax3(max(__float_as_uint(s0), __float_as_uint(s1)), __float_as_uint(s2), __float_as_uint(s3)) == SENT)) break;
    }
  }
  const float s = sum_q(s0 + s2), ss = sum_q(s1 + s3);
  mean = s * (1.f / D_);
  var = fmaxf(ss * (1.f / D_) - mean * mean, 0.f);
}
#define DRAIN() asm volatile("s_waitcnt vmcnt(0)" ::: "memory")
#define CH_BAR() asm volatile("s_barrier" ::: "memory")

template <int SC, int SN, int OV> DI void att_a(unsigned vA) {
  asm volatile(
      "s_waitcnt lgkmcnt(1)\n\t"
      "v_mfma_f32_32x32x16_bf16 v[128:143], v[196:199], v[180:183], v[128:143]\n\t"
      "v_exp_f32 v[%c1], v[%c1]\n\tv_exp_f32 v[%c1+1], v[%c1+1]\n\t"
      "ds_read_b128 v[196:199], %0 offset:%c3+704\n\t"
      "v_exp_f32 v[%c1+2], v[%c1+2]\n\tv_exp_f32 v[%c1+3], v[%c1+3]\n\t"
      "v_cvt_pk_bf16_f32 v176, v[%c1], v[%c1+1]\n\t"
      "v_mfma_f32_32x32x16_bf16 v[%c2:%c2+15], v[184:187], v[200:203], 0\n\t"
      "v_cvt_pk_bf16_f32 v177, v[%c1+2], v[%c1+3]\n\t"
      "v_exp_f32 v[%c1+4], v[%c1+4]\n\tv_exp_f32 v[%c1+5], v[%c1+5]\n\tv_exp_f32 v[%c1+6], v[%c1+6]\n\tv_exp_f32 v[%c1+7], v[%c1+7]\n\t"
      "v_cvt_pk_bf16_f32 v178, v[%c1+4], v[%c1+5]\n\tv_cvt_pk_bf16_f32 v179, v[%c1+6], v[%c1+7]"
      ::"v"(vA), "n"(SC), "n"(SN), "n"(OV) : "memory");
}
template <int SC, int SN, int OK> DI void att_b(unsigned kA, unsigned kB, unsigned vA) {
  asm volatile(
      "v_mfma_f32_32x32x16_bf16 v[%c4:%c4+15], v[188:191], v[204:207], v[%c4:%c4+15]\n\t"
      "v_exp_f32 v[%c3+8], v[%c3+8]\n\tv_exp_f32 v[%c3+9], v[%c3+9]\n\t"
      "ds_read_b128 v[184:187], %0 offset:%c5\n\tds_read_b64 v[188:189], %1 offset:%c5\n\t"
      "v_exp_f32 v[%c3+10], v[%c3+10]\n\tv_exp_f32 v[%c3+11], v[%c3+11]\n\t"
      "v_cvt_pk_bf16_f32 v180, v[%c3+8], v[%c3+9]\n\t"
      "s_waitcnt lgkmcnt(3)\n\t"
      "v_mfma_f32_32x32x16_bf16 v[128:143], v[192:195], v[176:179], v[128:143]\n\t"
      "v_cvt_pk_bf16_f32 v181, v[%c3+10], v[%c3+11]\n\t"
      "v_exp_f32 v[%c3+12], v[%c3+12]\n\tv_exp_f32 v[%c3+13], v[%c3+13]\n\tv_exp_f32 v[%c3+14], v[%c3+14]\n\tv_exp_f32 v[%c3+15], v[%c3+15]\n\t"
      "v_cvt_pk_bf16_f32 v182, v[%c3+12], v[%c3+13]\n\tv_cvt_pk_bf16_f32 v183, v[%c3+14], v[%c3+15]\n\t"
      "ds_read_b128 v[192:195], %2 offset:%c5"
      ::"v"(kA), "v"(kB), "v"(vA), "n"(SC), "n"(SN), "n"(OK) : "memory");
}

__global__ void __launch_bounds__(512, 2) __attribute__((amdgpu_num_vgpr(64))) k_main(const char* __restrict__ x, char* __restrict__ ws, char* __restrict__ out) {
  extern __shared__ __attribute__((aligned(16))) char smem[];
  lds_ptr lds = (lds_ptr)smem;
  OWN_ALL_VGPRS();
  const unsigned ldsb = (unsigned)(size_t)lds;
  const int tid = threadIdx.x, lane = tid & 63, w = __builtin_amdgcn_readfirstlane(tid >> 6), q = lane >> 4, tok = lane & 15;
  const int bid = blockIdx.x, xcd = bid & 7, r = bid >> 3;
  const int j = r & 7, head = 8 * (xcd & 3) + j, qb = 4 * (xcd >> 2) + (r >> 3), rb = 4 * qb + (xcd & 3);
  const __amdgpu_buffer_rsrc_t rsw = mk_rsrc(ws, WS_END);
  const char* wf = ws + OFF_WF + (size_t)j * NGRP * GRP_B;
  const unsigned lane16 = lane * 16;

  const int hi = lane >> 5, r32 = lane & 31;
  const char* kvh = ws + OFF_KV + (size_t)head * 64 * KV_REC;
  const int qt = 8 * qb + w;
  const char* qp = ws + OFF_QF + (size_t)(head * 64 + qt) * QF_TILE;
  u32x4 q1u = *(const u32x4*)(qp + hi * 512 + r32 * 16);
  u32x2 q2l = *(const u32x2*)(qp + 1024 + r32 * 8);
  const int pc2 = min(w + 16, 20);
  auto kv_issue = [&](int c) {
    const char* src = kvh + (size_t)(c & 7) * KV_CHUNK;
    const unsigned dst = ldsb + L_KV0 + (c & 1) * KV_CHUNK;
    dma1(src + w * 1024, lane16, dst + w * 1024);
    dma1(src + (w + 8) * 1024, lane16, dst + (w + 8) * 1024);
    dma1(src + pc2 * 1024, lane16, dst + pc2 * 1024);
  };
  kv_issue(0);
  if (tid == 0) *(__attribute__((address_space(3))) unsigned*)(lds + L_FLAG) = 0u;
  asm volatile("s_waitcnt vmcnt(0) lgkmcnt(0)\n\ts_barrier" : "+v"(q1u), "+v"(q2l)::"memory");
  kv_issue(1);
  const bf16x8 q1 = __builtin_bit_cast(bf16x8, q1u);
  u32x4 q2u = {hi ? 0u : q2l[0], hi ? 0u : q2l[1], 0u, 0u};
  unsigned kA = ldsb + L_KV0 + hi * 512 + r32 * 16, kB = ldsb + L_KV0 + 1024 + r32 * 8, vA = ldsb + L_KV0 + 1280 + hi * 352 + r32 * 16;
  {
    const bf16x8 k1 = lds16((lds_cptr)lds + L_KV0 + hi * 512 + r32 * 16);
    const u32x2 k2l = *(const __attribute__((address_space(3))) u32x2*)(lds + L_KV0 + 1024 + r32 * 8);
    const u32x4 k2 = {k2l[0], k2l[1], 0u, 0u};
    f32x16 s0 = MFMA32(k1, q1, f32x16{});
    s0 = MFMA32(__builtin_bit_cast(bf16x8, k2), __builtin_bit_cast(bf16x8, q2u), s0);
    float m = s0[0];
#pragma unroll
    for (int i = 1; i < 16; ++i) m = fmaxf(m, s0[i]);
    auto rr = __builtin_amdgcn_permlane32_swap(__float_as_uint(m), __float_as_uint(m), false, false);
    m = fmaxf(__uint_as_float(rr[0]), __uint_as_float(rr[1]));
    q2u[2] = hi ? 0u : (pack2(-m, 0.f) & 0xffffu);
  }
  vset<200>(q1u[0]); vset<201>(q1u[1]); vset<202>(q1u[2]); vset<203>(q1u[3]); vset<204>(q2u[0]); vset<205>(q2u[1]); vset<206>(q2u[2]);
  asm volatile(
      "ds_read_b128 v[184:187], %0\n\tds_read_b64 v[188:189], %1\n\t"
      "v_mov_b32 v207, 0\n\tv_mov_b32 v190, 0x3f80\n\tv_mov_b32 v191, 0\n\t"
      ".set i_, 0\n\t.rept 16\n\tv_mov_b32 v[128+i_], 0\n\t.set i_, i_+1\n\t.endr\n\t"
      ".set i_, 0\n\t.rept 4\n\tv_mov_b32 v[180+i_], 0\n\tv_mov_b32 v[196+i_], 0\n\t.set i_, i_+1\n\t.endr\n\t"
      "s_waitcnt lgkmcnt(0)\n\t"
      "v_mfma_f32_32x32x16_bf16 v[144:159], v[184:187], v[200:203], 0\n\t"
      "v_mfma_f32_32x32x16_bf16 v[144:159], v[188:191], v[204:207], v[144:159]\n\t"
      "ds_read_b128 v[184:187], %0 offset:%c3\n\tds_read_b64 v[188:189], %1 offset:%c3\n\tds_read_b128 v[192:195], %2 offset:%c3\n\t"
      "s_nop 7"
      ::"v"(kA), "v"(kB), "v"(vA), "n"(KV_REC) : "memory");
#pragma unroll 1
  for (int c = 0; c < 8; ++c) {
    att_a<144, 160, 1 * KV_REC>(vA); att_b<144, 160, 2 * KV_REC>(kA, kB, vA);
    att_a<160, 144, 2 * KV_REC>(vA); att_b<160, 144, 3 * KV_REC>(kA, kB, vA);
    att_a<144, 160, 3 * KV_REC>(vA); att_b<144, 160, 4 * KV_REC>(kA, kB, vA);
    att_a<160, 144, 4 * KV_REC>(vA); att_b<160, 144, 5 * KV_REC>(kA, kB, vA);
    att_a<144, 160, 5 * KV_REC>(vA); att_b<144, 160, 6 * KV_REC>(kA, kB, vA);
    att_a<160, 144, 6 * KV_REC>(vA); att_b<160, 144, 7 * KV_REC>(kA, kB, vA);
    att_a<144, 160, 7 * KV_REC>(vA);
    asm volatile("s_waitcnt vmcnt(0) lgkmcnt(0)\n\ts_barrier" ::: "memory");
    if (c < 7) kv_issue(c + 2);
    if (c < 4) {
      const char* src = wf + (size_t)c * GRP_B;
      const unsigned dst = ldsb + c * GRP_B;
      dma1(src + w * 1024, lane16, dst + w * 1024);
      dma1(src + (w + 8) * 1024, lane16, dst + (w + 8) * 1024);
      dma1(src + (w + 16) * 1024, lane16, dst + (w + 16) * 1024);
      if (w == 0) dma1(src + 24 * 1024, lane16, dst + 24 * 1024);
    }
    if (c == 4) {
      const u32x4 sent = {SENT, SENT, SENT, SENT};
      const unsigned rt = 4 * rb + (w & 3);
      if (w < 4) {
        const unsigned kp = 5 * (j >> 1) + 3 * (j & 1), ksg = 5 * (j >> 1) + 2;
#pragma unroll
        for (int b = 0; b < 3; ++b) {
          const unsigned o = OFF_H + b * SZ_FM + rt * FM_RT;
          st_sc1(sent, rsw, lane16, o + kp * 1024);
          st_sc1(sent, rsw, lane16, o + (kp + 1) * 1024);
          if (lane < 32) st_sc1(sent, rsw, (j & 1) * 512 + lane16, o + ksg * 1024);
        }
        if (lane < 16) {
          st_sc1(u32x2{SENT, SENT}, rsw, (j >> 1) * 256 + lane16 + (j & 1) * 8, OFF_ST1 + rt * 1024);
          st_sc1(u32x2{SENT, SENT}, rsw, (j >> 1) * 256 + lane16 + (j & 1) * 8, OFF_ST2 + rt * 1024);
        }
      } else {
        const unsigned kb = 27 * (j >> 2) + 7 * (j & 3), np = (j & 3) == 3 ? 6 : 7;
        for (unsigned p = 0; p < np; ++p) st_sc1(sent, rsw, lane16, OFF_G + rt * G_RT + (kb + p) * 1024);
      }
    }
    if (c == 5 && tid < 320) {
      const bool is1 = tid < 80;
      const int i = is1 ? tid : tid - 80, t = i >> 4, row = i & 15;
      int n;
      if (is1) n = t < 4 ? 32 * (5 * (j >> 1) + 3 * (j & 1) + (t >> 1)) + 8 * (row >> 2) + 4 * (t & 1) + (row & 3) : 32 * (5 * (j >> 1) + 2) + 16 * (j & 1) + row;
      else n = min(32 * (27 * (j >> 2) + 7 * (j & 3) + (t >> 1)) + 8 * (row >> 2) + 4 * (t & 1) + (row & 3), F_ - 1);
      const float* cp = (const float*)(ws + (is1 ? OFF_CS1 : OFF_CS3)) + n;
      const int N = is1 ? D_ : F_;
      float s = 0.f;
#pragma unroll
      for (int k = 0; k < 20; ++k) s += cp[k * N];
      *(__attribute__((address_space(3))) float*)(lds + L_CS1 + tid * 4) = s;
    }
    { const int d = (c & 1) ? -KV_CHUNK : KV_CHUNK; kA += d; kB += d; vA += d; }
    att_b<144, 160, 0>(kA, kB, vA);
    att_a<160, 144, 0>(vA); att_b<160, 144, KV_REC>(kA, kB, vA);
  }
  {
    asm volatile("s_waitcnt lgkmcnt(0)\n\tv_mfma_f32_32x32x16_bf16 v[128:143], v[196:199], v[180:183], v[128:143]\n\ts_nop 11" ::: "memory");
    float O[12];
    static_for<12>([&](auto I_) { O[I_.value] = vget<128 + I_.value>(); });
    auto rr = __builtin_amdgcn_permlane32_swap(__float_as_uint(O[8]), __float_as_uint(O[8]), false, false);
    float l = __uint_as_float(rr[1]);
    bool fin = true;
#pragma unroll
    for (int i = 0; i < 12; ++i) fin = fin && (fabsf(O[i]) <= 3.0e38f || (hi && i > 8));
    const bool bad = !(l >= 8.6736174e-19f && l <= 1.2676506e30f && fin);
    if (__any(bad) && lane == 0) *(__attribute__((address_space(3))) unsigned*)(lds + L_FLAG) = 1u;
    asm volatile("s_waitcnt vmcnt(0) lgkmcnt(0)\n\ts_barrier" ::: "memory");
    if (__builtin_expect(__builtin_amdgcn_readfirstlane(*(volatile __attribute__((address_space(3))) unsigned*)(lds + L_FLAG)) != 0, 0)) {
      const f32x16 Os = attn_safe(kvh, lds, q1, q2u, w, lane);
#pragma unroll
      for (int i = 0; i < 12; ++i) O[i] = Os[i];
      auto r2 = __builtin_amdgcn_permlane32_swap(__float_as_uint(O[8]), __float_as_uint(O[8]), false, false);
      l = __uint_as_float(r2[1]);
    }
    const float il = __builtin_amdgcn_rcpf(l);
    const unsigned rto = OFF_O + (2 * qt + (r32 >> 4)) * FM_RT + (r32 & 15) * 16;
    const int o1 = 5 * (head >> 1) + 3 * (head & 1) + hi, o2 = 5 * (head >> 1) + 2;
    u32x4 o = {nosent(pack2(O[0] * il, O[1] * il)), nosent(pack2(O[2] * il, O[3] * il)), nosent(pack2(O[4] * il, O[5] * il)), nosent(pack2(O[6] * il, O[7] * il))};
    st_sc1(o, rsw, rto + o1 * 256, 0);
    if (!hi) st_sc1(u32x2{nosent(pack2(O[8] * il, O[9] * il)), nosent(pack2(O[10] * il, O[11] * il))}, rsw, rto + o2 * 256 + (head & 1) * 8, 0);
  }
  if (w >= 6) return;

  if (w >= 4) {
    const unsigned po = (w - 4) * 12 * 1024;
    auto issue = [&](int G) {
      const char* src = wf + (size_t)G * GRP_B + po;
      const unsigned dst = ldsb + (G % 6) * GRP_B + po;
      dma4(src, lane16, dst); dma4(src + 4096, lane16, dst + 4096); dma4(src + 8192, lane16, dst + 8192); dma1(src + 12288, lane16, dst + 12288);
    };
    issue(4); issue(5);
#pragma unroll 1
    for (int G = 0; G < NGRP; ++G) {
      if (G <= NGRP - 5) asm volatile("s_waitcnt vmcnt(26)" ::: "memory");
      else if (G == NGRP - 4) asm volatile("s_waitcnt vmcnt(13)" ::: "memory");
      else asm volatile("s_waitcnt vmcnt(0)" ::: "memory");
      CH_BAR();
      if (G >= 1 && G + 5 < NGRP) issue(G + 5);
    }
    return;
  }

  const unsigned rt = 4 * rb + w;
  const unsigned kp = 5 * (j >> 1) + 3 * (j & 1), ksg = 5 * (j >> 1) + 2;
  const unsigned rowoff = ((16 * rt + tok) * D_) * 2;
  const unsigned colp = (32 * kp + 8 * q) * 2, cols = (32 * ksg + 16 * (j & 1) + 4 * q) * 2;
  const unsigned wb[3] = {ldsb + lane16, ldsb + lane16 + 2 * GRP_B, ldsb + lane16 + 4 * GRP_B};
  const u32x4 srd = mk_srd(ws, WS_END);
  const unsigned vo0[4] = {lane16, lane16, lane16, lane16};
  asm volatile(".set i_, 0\n\t.rept 5\n\tds_read_b128 v[208+4*i_:211+4*i_], %0 offset:1024*i_\n\t.set i_, i_+1\n\t.endr" ::"v"(wb[0]) : "memory");
  float res[5][4], v[5][4];
  {
    const u32x4 xa = *(const u32x4*)(x + rowoff + colp), xb = *(const u32x4*)(x + rowoff + colp + 64);
    const u32x2 xs = *(const u32x2*)(x + rowoff + cols);
    v[0][0] = bflo(xa[0]); v[0][1] = bfhi(xa[0]); v[0][2] = bflo(xa[1]); v[0][3] = bfhi(xa[1]);
    v[1][0] = bflo(xa[2]); v[1][1] = bfhi(xa[2]); v[1][2] = bflo(xa[3]); v[1][3] = bfhi(xa[3]);
    v[2][0] = bflo(xb[0]); v[2][1] = bfhi(xb[0]); v[2][2] = bflo(xb[1]); v[2][3] = bfhi(xb[1]);
    v[3][0] = bflo(xb[2]); v[3][1] = bfhi(xb[2]); v[3][2] = bflo(xb[3]); v[3][3] = bfhi(xb[3]);
    v[4][0] = bflo(xs[0]); v[4][1] = bfhi(xs[0]); v[4][2] = bflo(xs[1]); v[4][3] = bfhi(xs[1]);
#pragma unroll
    for (int t = 0; t < 5; ++t) { PIN(v[t][0]); PIN(v[t][1]); PIN(v[t][2]); PIN(v[t][3]); }
  }
  DRAIN();
  ld_unit(srd, vo0, OFF_O + rt * FM_RT);
  run_unit<0>(wb, srd, vo0, OFF_O + rt * FM_RT);
  static_for<5>([&](auto T_) { acc_add<R_ACC + 4 * T_.value>(res[T_.value], v[T_.value]); });
  store_tile(res, rsw, OFF_H + rt * FM_RT, j, lane);
  store_stats(res, rsw, OFF_ST1 + rt * 1024, j, lane);
  float y[5][4];
  DRAIN();
  ld_stats(srd, lane16, OFF_ST1 + rt * 1024);
  ld_unit(srd, vo0, OFF_H + rt * FM_RT);
  run_unit<4>(wb, srd, vo0, OFF_H + rt * FM_RT);
  {
    float mean, var;
    read_stats(srd, lane16, OFF_ST1 + rt * 1024, mean, var);
    const float r1 = __builtin_amdgcn_rsqf(var + LN_EPS), r2 = r1 * __builtin_amdgcn_rsqf(var * r1 * r1 + LN_EPS);
    static_for<5>([&](auto T_) {
      constexpr int t = T_.value;
      const f32x4 p = acc_sub<R_ACC + 4 * t>(mean, *(const __attribute__((address_space(3))) f32x4*)(lds + L_CS1 + t * 64 + q * 16));
#pragma unroll
      for (int e = 0; e < 4; ++e) { v[t][e] = gelu(p[e] * r2); y[t][e] = (res[t][e] - mean) * r1; }
    });
    store_tile(v, rsw, OFF_H1 + rt * FM_RT, j, lane);
  }
  {
    const u32x4 srd1 = mk_srd(ws + OFF_H1, SZ_FM);
    unsigned vo[4];
#pragma unroll
    for (int g = 0; g < 4; ++g) {
      const int T = 16 * (int)rt + tok + (g == 0 ? -1 : g == 1 ? 1 : g == 2 ? -2 : 2);
      vo[g] = (T >= 0 && T < S_) ? (unsigned)((T >> 4) * FM_RT + q * 256 + (T & 15) * 16) : 0x80000000u;
    }
    DRAIN();
    ld_unit(srd1, vo, 0);
    run_unit<8>(wb, srd1, vo, 0);
    static_for<5>([&](auto T_) { acc_add<R_ACC + 4 * T_.value>(v[T_.value], y[T_.value]); });
    store_tile(v, rsw, OFF_X1 + rt * FM_RT, j, lane);
    store_stats(v, rsw, OFF_ST2 + rt * 1024, j, lane);
#pragma unroll
    for (int t = 0; t < 5; ++t)
#pragma unroll
      for (int e = 0; e < 4; ++e) res[t][e] += v[t][e];
  }
  DRAIN();
  ld_stats(srd, lane16, OFF_ST2 + rt * 1024);
  ld_unit(srd, vo0, OFF_X1 + rt * FM_RT);
  {
    const unsigned gb = OFF_G + rt * G_RT + (27 * (j >> 2) + 7 * (j & 3)) * 1024;
    float mean, var, r3;
    unsigned pk[5][2], keep0, keep1;
#define G_EPI(PASS) static_for<5>([&](auto T_) { constexpr int t = T_.value;                                                                             \
        const f32x4 p = acc_sub<R_ACC + 4 * t>(mean, *(const __attribute__((address_space(3))) f32x4*)(lds + L_CS3 + (5 * PASS + t) * 64 + q * 16));     \
        pk[t][0] = nosent(pack2(gelu(p[0] * r3), gelu(p[1] * r3))); pk[t][1] = nosent(pack2(gelu(p[2] * r3), gelu(p[3] * r3))); })
    run_unit<12>(wb, srd, vo0, OFF_X1 + rt * FM_RT);
    read_stats(srd, lane16, OFF_ST2 + rt * 1024, mean, var);
    r3 = __builtin_amdgcn_rsqf(var + LN_EPS);
    G_EPI(0);
    st_sc1(u32x4{pk[0][0], pk[0][1], pk[1][0], pk[1][1]}, rsw, lane16, gb);
    st_sc1(u32x4{pk[2][0], pk[2][1], pk[3][0], pk[3][1]}, rsw, lane16, gb + 1024);
    keep0 = pk[4][0]; keep1 = pk[4][1];
    run_unit_again<16>(wb, srd);
    G_EPI(1);
    st_sc1(u32x4{keep0, keep1, pk[0][0], pk[0][1]}, rsw, lane16, gb + 2048);
    st_sc1(u32x4{pk[1][0], pk[1][1], pk[2][0], pk[2][1]}, rsw, lane16, gb + 3072);
    st_sc1(u32x4{pk[3][0], pk[3][1], pk[4][0], pk[4][1]}, rsw, lane16, gb + 4096);
    run_unit_again<20>(wb, srd);
    G_EPI(2);
    st_sc1(u32x4{pk[0][0], pk[0][1], pk[1][0], pk[1][1]}, rsw, lane16, gb + 5120);
    if ((j & 3) != 3) st_sc1(u32x4{pk[2][0], pk[2][1], pk[3][0], pk[3][1]}, rsw, lane16, gb + 6144);
#undef G_EPI
  }
  DRAIN();
  {
    const unsigned gs = OFF_G + rt * G_RT;
    ld_unit(srd, vo0, gs);
    repoll<0, 5>(check_frags<0, 5, 15>(), srd, lane16, gs);
    repoll<5, 5>(run_group<24, 5, 0, true, 5, 5, 13, 5>(wb, srd, lane16, gs + 20 * 1024), srd, lane16, gs + 5 * 1024);
    repoll<10, 5>(run_group<25, 5, 5, false, 5, 10, 13, 5>(wb, srd, lane16, gs + 25 * 1024), srd, lane16, gs + 10 * 1024);
    repoll<15, 5>(run_group<26, 5, 10, false, 5, 15, 13, 5>(wb, srd, lane16, gs + 30 * 1024), srd, lane16, gs + 15 * 1024);
    repoll<0, 5>(run_group<27, 5, 15, false, 5, 0, 13, 5>(wb, srd, lane16, gs + 35 * 1024), srd, lane16, gs + 20 * 1024);
    repoll<5, 5>(run_group<28, 5, 0, false, 5, 5, 13, 5>(wb, srd, lane16, gs + 40 * 1024), srd, lane16, gs + 25 * 1024);
    repoll<10, 5>(run_group<29, 5, 5, false, 5, 10, 13, 5>(wb, srd, lane16, gs + 45 * 1024), srd, lane16, gs + 30 * 1024);
    repoll<15, 5>(run_group<30, 5, 10, false, 5, 15, 13, 4>(wb, srd, lane16, gs + 50 * 1024), srd, lane16, gs + 35 * 1024);
    repoll<0, 5>(run_group<31, 5, 15, false, 5, 0, 9, 0>(wb, srd, 0, 0), srd, lane16, gs + 40 * 1024);
    repoll<5, 5>(run_group<32, 5, 0, false, 5, 5, 4, 0>(wb, srd, 0, 0), srd, lane16, gs + 45 * 1024);
    repoll<10, 4>(run_group<33, 5, 5, false, 4, 10, 0, 0>(wb, srd, 0, 0), srd, lane16, gs + 50 * 1024);
    run_group<34, 4, 10, false, 0, 0, 0, 0>(wb, srd, 0, 0);
    asm volatile("s_nop 6" ::: "memory");
    static_for<5>([&](auto T_) { acc_add<R_ACC + 4 * T_.value>(v[T_.value], res[T_.value]); });
    *(u32x4*)(out + rowoff + colp) = u32x4{pack2(v[0][0], v[0][1]), pack2(v[0][2], v[0][3]), pack2(v[1][0], v[1][1]), pack2(v[1][2], v[1][3])};
    *(u32x4*)(out + rowoff + colp + 64) = u32x4{pack2(v[2][0], v[2][1]), pack2(v[2][2], v[2][3]), pack2(v[3][0], v[3][1]), pack2(v[3][2], v[3][3])};
    *(u32x2*)(out + rowoff + cols) = u32x2{pack2(v[4][0], v[4][1]), pack2(v[4][2], v[4][3])};
    asm volatile("s_waitcnt lgkmcnt(0)" ::: "memory");
  }
}

extern "C" void kernel_launch(void* const* d_in, const int* in_sizes, int n_in, void* d_out, int out_size, void* d_ws, size_t ws_size, hipStream_t stream) {
  (void)in_sizes; (void)n_in; (void)out_size; (void)ws_size;
  (void)hipFuncSetAttribute((const void*)k_qkv, hipFuncAttributeMaxDynamicSharedMemorySize, K1_LDS);
  (void)hipFuncSetAttribute((const void*)k_main, hipFuncAttributeMaxDynamicSharedMemorySize, K2_LDS);
  k_qkv<<<256, 512, K1_LDS, stream>>>((const char*)d_in[0], (const char*)d_in[1], (const char*)d_in[2], (const char*)d_in[4], (const char*)d_in[5],
                                      (const char*)d_in[6], (const char*)d_in[7], (char*)d_ws);
  k_main<<<256, 512, K2_LDS, stream>>>((const char*)d_in[0], (char*)d_ws, (char*)d_out);
}
```

```cpp
#include <hip/hip_runtime.h>
#include <hip/hip_bf16.h>

typedef __attribute__((ext_vector_type(8))) short bf16x8;
typedef __attribute__((ext_vector_type(4))) short s16x4;
typedef __attribute__((ext_vector_type(2))) __bf16 bf16x2;
typedef __attribute__((ext_vector_type(2))) float f32x2;
typedef __attribute__((ext_vector_type(4))) float f32x4;
typedef __attribute__((ext_vector_type(16))) float f32x16;
typedef __attribute__((ext_vector_type(4))) unsigned u32x4;
typedef __attribute__((ext_vector_type(2))) unsigned u32x2;
typedef __attribute__((address_space(3))) const char* lds_cptr;
typedef __attribute__((address_space(3))) char* lds_ptr;
#define DI __device__ __forceinline__
#define AI __attribute__((always_inline))
#define SBAR() __builtin_amdgcn_sched_barrier(0)
#define PIN(x) asm volatile("" : "+v"(x))
#define MFMA16(a, b, c) __builtin_amdgcn_mfma_f32_16x16x32_bf16((a), (b), (c), 0, 0, 0)
#define MFMA32(a, b, c) __builtin_amdgcn_mfma_f32_32x32x16_bf16((a), (b), (c), 0, 0, 0)

constexpr int S_ = 2048, D_ = 640, F_ = 1728;
constexpr float LN_EPS = 1e-5f;
constexpr float QSCALE = 0.22360679774997896f * 1.4426950408889634f;
constexpr unsigned SENT = 0xFFFFFFFFu;

constexpr unsigned QF_TILE = 1280, KV_REC = 2688, FM_RT = 20 * 1024, G_RT = 54 * 1024;
constexpr unsigned SZ_FM = 128 * FM_RT;
constexpr unsigned OFF_QF = 0;
constexpr unsigned OFF_KV = OFF_QF + 32 * 64 * QF_TILE;
constexpr unsigned OFF_O = OFF_KV + 32 * 64 * KV_REC;
constexpr unsigned OFF_H = OFF_O + SZ_FM;
constexpr unsigned OFF_H1 = OFF_H + SZ_FM;
constexpr unsigned OFF_X1 = OFF_H1 + SZ_FM;
constexpr unsigned OFF_G = OFF_X1 + SZ_FM;
constexpr unsigned OFF_ST1 = OFF_G + 128 * G_RT;
constexpr unsigned OFF_ST2 = OFF_ST1 + 128 * 1024;
constexpr int NGRP = 35;
constexpr unsigned GRP_B = 25 * 1024;
constexpr unsigned OFF_WF = OFF_ST2 + 128 * 1024;
constexpr unsigned OFF_CS1 = OFF_WF + 8 * NGRP * GRP_B;
constexpr unsigned OFF_CS3 = OFF_CS1 + 20 * 640 * 4;
constexpr unsigned OFF_FLAG = OFF_CS3 + 20 * 1728 * 4;
constexpr unsigned OFF_CNT = OFF_FLAG + 1024;
constexpr unsigned WS_END = OFF_CNT + 1024 * 256;

#define STAMP_DECL
#define STAMP_ARG
#define STAMP_PAR
#define STAMP(i)
#define STAMPF(i)
#define STAMP_OUT(k)
DI unsigned pack2(float lo, float hi) { f32x2 v = {lo, hi}; return __builtin_bit_cast(unsigned, __builtin_convertvector(v, bf16x2)); }
DI float bflo(unsigned u) { return __uint_as_float(u << 16); }
DI float bfhi(unsigned u) { return __uint_as_float(u & 0xffff0000u); }
DI float dot2(unsigned a, unsigned b, float c) { return __builtin_amdgcn_fdot2_f32_bf16(__builtin_bit_cast(bf16x2, a), __builtin_bit_cast(bf16x2, b), c, false); }
DI float sum_q(float x) {
  auto s = __builtin_amdgcn_permlane16_swap(__float_as_uint(x), __float_as_uint(x), false, false);
  x = __uint_as_float(s[0]) + __uint_as_float(s[1]);
  auto t = __builtin_amdgcn_permlane32_swap(__float_as_uint(x), __float_as_uint(x), false, false);
  return __uint_as_float(t[0]) + __uint_as_float(t[1]);
}
template <int N> struct IC { static constexpr int value = N; };
template <int N, int I = 0, class Fn> DI void static_for(Fn&& f) { if constexpr (I < N) { f(IC<I>{}); static_for<N, I + 1>(f); } }
DI int swap23(int r) { return (r & ~12) | ((r & 4) << 1) | ((r & 8) >> 1); }
DI bf16x8 lds16(lds_cptr p) { return *(const __attribute__((address_space(3))) bf16x8*)p; }
typedef short v4i16_t __attribute__((ext_vector_type(4)));
DI s16x4 ldstr(lds_cptr p) { return __builtin_bit_cast(s16x4, __builtin_amdgcn_ds_read_tr16_b64_v4i16((__attribute__((address_space(3))) v4i16_t*)p)); }

#define OWN_ALL_VGPRS() asm volatile("" ::: "v255")
template <int R> DI float vget() { float r; asm volatile("v_mov_b32 %0, v[%c1]" : "=v"(r) : "n"(R)); return r; }
template <int R0, int R1> DI float vadd() { float r; asm volatile("v_add_f32 %0, v[%c1], v[%c2]" : "=v"(r) : "n"(R0), "n"(R1)); return r; }
template <int R> DI f32x4 vget4() { return f32x4{vget<R>(), vget<R + 1>(), vget<R + 2>(), vget<R + 3>()}; }
template <int R> DI void vset(unsigned v) { asm volatile("v_mov_b32 v[%c1], %0" ::"v"(v), "n"(R)); }
template <int R, int OFF> DI void gload4(const char* sbase, unsigned voff) {
  asm volatile("global_load_dwordx4 v[%c2:%c2+3], %0, %1 offset:%c3" ::"v"(voff), "s"(sbase), "n"(R), "n"(OFF) : "memory");
}
template <int R> DI f32x4 acc_sub(float m, f32x4 c) {
  float a0, a1, a2, a3;
  asm volatile("v_fma_f32 %0, -%4, %5, v[%c9]\n\tv_fma_f32 %1, -%4, %6, v[%c9+1]\n\tv_fma_f32 %2, -%4, %7, v[%c9+2]\n\tv_fma_f32 %3, -%4, %8, v[%c9+3]"
               : "=&v"(a0), "=&v"(a1), "=&v"(a2), "=&v"(a3) : "v"(m), "v"(c[0]), "v"(c[1]), "v"(c[2]), "v"(c[3]), "n"(R));
  return f32x4{a0, a1, a2, a3};
}
constexpr int AUX_SC1 = 16;
DI __amdgpu_buffer_rsrc_t mk_rsrc(const char* p, unsigned bytes) { return __builtin_amdgcn_make_buffer_rsrc((void*)p, 0, bytes, 0x00020000); }
DI void st_sc1(u32x4 v, __amdgpu_buffer_rsrc_t rs, unsigned voff, unsigned soff) { __builtin_amdgcn_raw_buffer_store_b128(v, rs, voff, soff, AUX_SC1); }
DI void st_sc1(u32x2 v, __amdgpu_buffer_rsrc_t rs, unsigned voff, unsigned soff) { __builtin_amdgcn_raw_buffer_store_b64(v, rs, voff, soff, AUX_SC1); }

DI u32x4 mk_srd(const char* p, unsigned bytes) { const unsigned long long v = (unsigned long long)p; return u32x4{(unsigned)v, (unsigned)(v >> 32) & 0xffffu, bytes, 0x00020000u}; }
DI void bdma_sc1(u32x4 srd, unsigned voff, unsigned soff, unsigned ldsdst) {
  asm volatile("s_mov_b32 m0, %3\n\ts_nop 0\n\tbuffer_load_dwordx4 %0, %1, %2 offen sc1 lds" ::"v"(voff), "s"(srd), "s"(soff), "s"(ldsdst) : "memory");
}
DI void bdma4_sc1(u32x4 srd, unsigned voff, unsigned soff, unsigned ldsdst) {
  asm volatile("s_mov_b32 m0, %3\n\ts_nop 0\n\tbuffer_load_dwordx4 %0, %1, %2 offen sc1 lds\n\tbuffer_load_dwordx4 %0, %1, %2 offen offset:1024 sc1 lds\n\t"
               "buffer_load_dwordx4 %0, %1, %2 offen offset:2048 sc1 lds\n\tbuffer_load_dwordx4 %0, %1, %2 offen offset:3072 sc1 lds" ::"v"(voff), "s"(srd), "s"(soff), "s"(ldsdst) : "memory");
}
DI void bdma(u32x4 srd, unsigned voff, unsigned soff, unsigned ldsdst) {
  asm volatile("s_mov_b32 m0, %3\n\ts_nop 0\n\tbuffer_load_dwordx4 %0, %1, %2 offen lds" ::"v"(voff), "s"(srd), "s"(soff), "s"(ldsdst) : "memory");
}

constexpr int K1_LDS = 4 * 32768;
#define K1_AUX 0
#define K1_RD(n, NXT, S2N, OFF)                                                                                                    \
  ".if " #n " == 0\n\tds_read_b64_tr_b16 v[" #NXT "+16:" #NXT "+17], v164 offset:" #OFF "+8192*" #S2N "\n\tds_read_b64_tr_b16 v[" #NXT "+18:" #NXT "+19], v164 offset:" #OFF "+8192*" #S2N "+1024\n\t" \
  ".elseif " #n " == 1\n\tds_read_b128 v[" #NXT ":" #NXT "+3], v[160+" #S2N "] offset:" #OFF "\n\t"                                   \
  ".elseif " #n " == 2\n\tds_read_b64_tr_b16 v[" #NXT "+20:" #NXT "+21], v165 offset:" #OFF "+8192*" #S2N "\n\tds_read_b64_tr_b16 v[" #NXT "+22:" #NXT "+23], v165 offset:" #OFF "+8192*" #S2N "+1024\n\t" \
  ".elseif " #n " == 3\n\tds_read_b128 v[" #NXT "+4:" #NXT "+7], v[160+" #S2N "] offset:" #OFF "+512\n\t"                             \
  ".elseif " #n " == 4\n\tds_read_b128 v[" #NXT "+8:" #NXT "+11], v[160+" #S2N "] offset:" #OFF "+4096\n\t"                           \
  ".elseif " #n " == 5\n\tds_read_b128 v[" #NXT "+12:" #NXT "+15], v[160+" #S2N "] offset:" #OFF "+4608\n\t"                          \
  ".elseif " #n " == 6\n\tds_read_b128 v[" #NXT "+24:" #NXT "+27], v[162+" #S2N "] offset:" #OFF "\n\t.endif\n\t"
#define K1_STEP(SET, NXT, S2N, OFF, S2)                                                                                            \
  ".set n_, 0\n\t.rept 8\n\t.set m_, n_/2\n\t.set e_, n_&1\n\t"                                                                     \
  ".if %c[tf]\n\tv_mfma_f32_16x16x32_bf16 v[56+4*n_:59+4*n_], v[" #SET "+16+4*e_:" #SET "+19+4*e_], v[" #SET "+4*m_:" #SET "+3+4*m_], v[56+4*n_:59+4*n_]\n\t" \
  ".else\n\tv_mfma_f32_16x16x32_bf16 v[56+4*n_:59+4*n_], v[" #SET "+4*m_:" #SET "+3+4*m_], v[" #SET "+16+4*e_:" #SET "+19+4*e_], v[56+4*n_:59+4*n_]\n\t.endif\n\t" \
  K1_RD(n_, NXT, S2N, OFF)                                                                                                         \
  ".set n_, n_+1\n\t.endr\n\t"                                                                                                     \
  "s_cmp_lg_u32 %[wm], " #S2 "\n\ts_cbranch_scc1 1f\n\t"                                                                            \
  ".if %c[tf]\n\tv_mfma_f32_16x16x32_bf16 v[88:91], v[" #SET "+16:" #SET "+19], v[156:159], v[88:91]\n\tv_mfma_f32_16x16x32_bf16 v[92:95], v[" #SET "+20:" #SET "+23], v[156:159], v[92:95]\n\t" \
  ".else\n\tv_mfma_f32_16x16x32_bf16 v[88:91], v[156:159], v[" #SET "+16:" #SET "+19], v[88:91]\n\tv_mfma_f32_16x16x32_bf16 v[92:95], v[156:159], v[" #SET "+20:" #SET "+23], v[92:95]\n\t.endif\n\t" \
  "1:\n\t"                                                                                                                         \
  ".set m_, 0\n\t.rept 4\n\tv_dot2c_f32_bf16 v153, v[" #SET "+24+m_], v[" #SET "+24+m_]\n\tv_dot2c_f32_bf16 v152, v[" #SET "+24+m_], v156\n\t.set m_, m_+1\n\t.endr\n\t"

template <bool TF>
DI void k1_body(char* ws, lds_ptr lds, int rb, int g, int w, int lane
) {
  const int q = lane >> 4, i16 = lane & 15, wm = w >> 2, wn = w & 3;
  {
    const unsigned xa = 16384 + (64 * wm + 8 * (i16 >> 2) + (i16 & 3)) * 128 + ((q ^ (i16 >> 1)) * 16);
    const unsigned xs = xa + (wn >> 1) * 4096 + (wn & 1) * 512;
    const int p4 = lane & 3, qq = (lane >> 2) & 3;
    const unsigned wa = (8 * q + qq) * 256 + (((4 * wn + p4) ^ (2 * (qq + 4 * (q & 1)))) * 16) + 8 * (p4 >> 1);
    vset<160>(xa); vset<161>(xa ^ 64); vset<162>(xs); vset<163>(xs ^ 64); vset<164>(wa); vset<165>(wa ^ 8);
  }
  asm volatile(".set i_, 56\n\t.rept 40\n\tv_mov_b32 v[i_], 0\n\t.set i_, i_+1\n\t.endr\n\tv_mov_b32 v152, 0\n\tv_mov_b32 v153, 0\n\t"
               "v_mov_b32 v156, 0x3f803f80\n\tv_mov_b32 v157, v156\n\tv_mov_b32 v158, v156\n\tv_mov_b32 v159, v156\n\t"
               "s_barrier\n\t.set n_, 0\n\t.rept 7\n\t" K1_RD(n_, 96, 0, 0) ".set n_, n_+1\n\t.endr\n\ts_waitcnt lgkmcnt(0)" ::: "memory");
  STAMP(5);
  unsigned cnt;
  asm volatile(
      "s_mov_b32 %[cnt], 5\n\t"
      "2:\n\t"
      K1_STEP(96, 124, 1, 0, 0) "s_waitcnt lgkmcnt(0)\n\ts_barrier\n\t"
      K1_STEP(124, 96, 0, 32768, 1) "s_waitcnt lgkmcnt(0)\n\t"
      K1_STEP(96, 124, 1, 32768, 0) "s_waitcnt lgkmcnt(0)\n\ts_barrier\n\t"
      ".set i_, 160\n\t.rept 6\n\tv_xor_b32 v[i_], 0x10000, v[i_]\n\t.set i_, i_+1\n\t.endr\n\t"
      K1_STEP(124, 96, 0, 0, 1)
      "s_sub_u32 %[cnt], %[cnt], 1\n\ts_cmp_lg_u32 %[cnt], 0\n\ts_waitcnt lgkmcnt(0)\n\ts_cbranch_scc1 2b\n\t"
      "s_nop 7"
      : [cnt] "=&s"(cnt) : [wm] "s"(wm), [tf] "n"(TF ? 1 : 0) : "memory", "scc");
  STAMP(2);
  typedef __attribute__((address_space(3))) f32x4 lf32x4;
  typedef __attribute__((address_space(3))) f32x2 lf32x2;
  {
    const float sm = sum_q(vget<152>()), sq = sum_q(vget<153>());
    const float mean = sm * (1.f / D_);
    const float rstd = __builtin_amdgcn_rsqf(fmaxf(sq * (1.f / D_) - mean * mean, 0.f) + LN_EPS);
    if (q == 0) *(lf32x2*)(lds + 16384 + w * 128 + i16 * 8) = f32x2{mean, rstd};
  }
  f32x4 c0 = vget4<88>(), c1 = vget4<92>();
  *(lf32x4*)(lds + w * 2048 + lane * 16) = c0;
  *(lf32x4*)(lds + w * 2048 + 1024 + lane * 16) = c1;
  asm volatile("s_waitcnt lgkmcnt(0)\n\ts_barrier" ::: "memory");
  c0 += *(const lf32x4*)(lds + (w ^ 4) * 2048 + lane * 16);
  c1 += *(const lf32x4*)(lds + (w ^ 4) * 2048 + 1024 + lane * 16);
  const int kt0 = 4 * rb + 2 * wm;
  const __amdgpu_buffer_rsrc_t rsw = mk_rsrc(ws, WS_END);
  if (TF) {
    const bool isq = g < 5;
    const int cq = 16 * (isq ? g : g - 5) + 4 * wn + q, i5 = (cq * 205) >> 10, m5 = cq - 5 * i5;
    const int hd = 2 * i5 + (m5 > 2);
    const unsigned rec = isq ? QF_TILE : KV_REC;
    const unsigned base = (isq ? OFF_QF : OFF_KV) + (unsigned)(hd * 64 + kt0) * rec;
    const int r0 = 8 * (i16 >> 2) + (i16 & 3);
    const unsigned oct = base + (m5 > 2 ? m5 - 3 : m5) * 512, tail = base + 1024 + (8 * (i16 >> 2) + 2 * (i16 & 3)) * 8;
    const unsigned ad0 = m5 == 2 ? tail : oct + (isq ? r0 : swap23(r0)) * 16, ad1 = m5 == 2 ? tail + 64 * rec : oct + (isq ? r0 + 4 : swap23(r0 + 4)) * 16;
    static_for<2>([&](auto A_) AI {
      constexpr int a = A_.value;
      u32x4 o[2];
      static_for<2>([&](auto B_) AI {
        constexpr int mi = 2 * a + B_.value;
        const f32x2 st = *(const lf32x2*)(lds + 16384 + (4 * wm + mi) * 128 + i16 * 8);
        const float sc = isq ? st[1] * QSCALE : st[1];
        const f32x4 a0 = acc_sub<56 + 8 * mi>(st[0], c0) * sc, a1 = acc_sub<60 + 8 * mi>(st[0], c1) * sc;
        const unsigned p0 = pack2(a0[0], a0[1]), p1 = pack2(a0[2], a0[3]), p2 = pack2(a1[0], a1[1]), p3 = pack2(a1[2], a1[3]);
        o[B_.value] = q < 2 ? u32x4{p0, p1, p2, p3} : u32x4{p2, p3, p0, p1};
      });
      const bool tl = m5 == 2;
      const u32x4 s0 = tl ? u32x4{o[0][0], o[0][1], o[1][0], o[1][1]} : o[0], s1 = tl ? u32x4{o[0][2], o[0][3], o[1][2], o[1][3]} : o[1];
      __builtin_amdgcn_raw_buffer_store_b128(s0, rsw, ad0, a * rec, K1_AUX);
      __builtin_amdgcn_raw_buffer_store_b128(s1, rsw, ad1, a * rec, K1_AUX);
    });
  } else {
    const int p4 = i16 >> 2;
    static_for<2>([&](auto A_) AI {
      constexpr int a = A_.value;
      lds_cptr sp = (lds_cptr)lds + 16384 + (4 * wm + 2 * a) * 128 + q * 32;
      const f32x4 s0 = *(const lf32x4*)(sp), s1 = *(const lf32x4*)(sp + 16), s2 = *(const lf32x4*)(sp + 128), s3 = *(const lf32x4*)(sp + 144);
      const f32x4 mA = {s0[0], s0[2], s1[0], s1[2]}, rA = {s0[1], s0[3], s1[1], s1[3]}, mB = {s2[0], s2[2], s3[0], s3[2]}, rB = {s2[1], s2[3], s3[1], s3[3]};
      static_for<2>([&](auto E_) AI {
        constexpr int e = E_.value;
        const float cs = e ? c1[0] : c0[0];
        const f32x4 a0 = acc_sub<56 + 16 * a + 4 * e>(cs, mA) * rA, a1 = acc_sub<64 + 16 * a + 4 * e>(cs, mB) * rB;
        const int col = 128 * (g - 10) + 4 * (8 * wn + 2 * p4 + (e ? 1 - (p4 >> 1) : (p4 >> 1))) + (i16 & 3);
        const int hv = (col * 3277) >> 16, d = col - 20 * hv;
        __builtin_amdgcn_raw_buffer_store_b128(u32x4{pack2(a0[0], a0[1]), pack2(a0[2], a0[3]), pack2(a1[0], a1[1]), pack2(a1[2], a1[3])}, rsw,
            OFF_KV + (unsigned)(hv * 64 + ((kt0 & 32) | ((kt0 + a + 1) & 31))) * KV_REC + 1280 + q * 352 + (d < 16 ? swap23(d) : d) * 16, 0, K1_AUX);
      });
    });
  }
}

__global__ void __launch_bounds__(768, 3) __attribute__((amdgpu_num_vgpr(28))) k_qkv(const char* __restrict__ x, const char* __restrict__ wqkv, char* __restrict__ ws) {
  extern __shared__ __attribute__((aligned(16))) char smem[];
  lds_ptr lds = (lds_ptr)smem;
  asm volatile("" ::: "v167");
  STAMP_DECL; STAMP(0);
  const int tid = threadIdx.x, lane = tid & 63, w = __builtin_amdgcn_readfirstlane(tid >> 6);
  const int bid = blockIdx.x, xcd = bid & 7, r = bid >> 3;
  const int rb = 4 * (xcd & 3) + (r & 3), g = 8 * (xcd >> 2) + (r >> 2);
  if (g == 15) {
    if (w < 8) {
      const __amdgpu_buffer_rsrc_t rsw = mk_rsrc(ws, WS_END);
      const u32x4 sent = {SENT, SENT, SENT, SENT};
#pragma unroll 4
      for (int i = 0; i < 20; ++i) st_sc1(sent, rsw, lane * 16, OFF_O + (rb * 160 + w * 20 + i) * 1024);
      for (int i = w; i < 20; i += 8) st_sc1(sent, rsw, lane * 16, OFF_H1 + (16 * (rb >> 1) + ((rb & 1) ? 15 : 0)) * FM_RT + i * 1024);
      const int id = rb * 512 + tid;
      st_sc1(u32x4{0x3f803f80u, 0x3f803f80u, 0x3f803f80u, 0x3f803f80u}, rsw, (id >> 2) * KV_REC + (id & 3) * 352 + 1280 + 320, OFF_KV);
      if (rb == 0 && w == 0) st_sc1(u32x4{0u, 0u, 0u, 0u}, rsw, lane * 16, OFF_FLAG);
      st_sc1(u32x4{0u, 0u, 0u, 0u}, rsw, lane * 16, OFF_CNT + (rb * 16 + 2 * w) * 1024);
      st_sc1(u32x4{0u, 0u, 0u, 0u}, rsw, lane * 16, OFF_CNT + (rb * 16 + 2 * w + 1) * 1024);
    }
  } else if (w >= 8) {
    const int lw = w - 8, q = lane >> 4, isx = lw >> 1, hf = lw & 1;
    const int rot = (g + 2 * (r & 1) + 5 * ((r >> 1) & 1) + (xcd & 3)) % 10;
    const u32x4 srd = isx ? mk_srd(x, S_ * D_ * 2) : mk_srd(wqkv, D_ * 3 * D_ * 2);
    unsigned vo[4];
#pragma unroll
    for (int i = 0; i < 4; ++i)
      vo[i] = isx ? (unsigned)((128 * rb + 64 * hf + 8 * i + (lane >> 3)) * 1280 + 16 * ((lane & 7) ^ (2 * i + ((lane >> 4) & 1))))
                  : (unsigned)((32 * hf + 4 * i + q) * 3840 + 256 * g + 16 * ((lane & 15) ^ (2 * (q + 4 * (i >> 1)))));
    const unsigned pstep = isx ? 128 : 64 * 3840, hstep = isx ? 32 * 1280 : 16 * 3840;
    auto issue = [&](int p) AI {
      int pp = p + rot; pp -= pp >= 10 ? 10 : 0;
      const unsigned so = pp * pstep, d = (p & 3) * 32768 + lw * 8192;
#pragma unroll
      for (int i = 0; i < 8; ++i) bdma(srd, vo[i & 3], so + (i >> 2) * hstep, d + i * 1024);
    };
    issue(0); issue(1);
    asm volatile("s_waitcnt vmcnt(8)\n\ts_barrier" ::: "memory");
    issue(2);
#pragma unroll 1
    for (int p = 0; p < 10; ++p) {
      if (p < 8) asm volatile("s_waitcnt vmcnt(8)" ::: "memory");
      else asm volatile("s_waitcnt vmcnt(0)" ::: "memory");
      asm volatile("s_barrier" ::: "memory");
      if (p < 7) issue(p + 3);
    }
  } else {
    if (g < 10) k1_body<true>(ws, lds, rb, g, w, lane STAMP_ARG);
    else k1_body<false>(ws, lds, rb, g, w, lane STAMP_ARG);
  }
  STAMP(3);
  STAMP_OUT(0);
}

constexpr int KV_HALF = 11 * 1024, KV_SLOT = 2 * KV_HALF;
constexpr int L_KV0 = 4 * GRP_B, L_SCR = 126 * 1024, L_MISC = 6 * GRP_B, L_CS1 = L_MISC, L_CS3 = L_CS1 + 320, L_FILL = L_CS3 + 960, L_CSF = L_FILL + 8,
              L_CONS = L_FILL + 64, K2_LDS = L_CONS + 1024;
#define TOUCH_IT 6
#define TOUCH_AHEAD 12
constexpr unsigned SPIN_CAP = 200000;

DI u32x4 ld_sc1(__amdgpu_buffer_rsrc_t rs, unsigned voff, unsigned soff) { return __builtin_amdgcn_raw_buffer_load_b128(rs, voff, soff, AUX_SC1); }
DI unsigned nosent(unsigned u) { return min(u, SENT - 1u); }
DI unsigned umax3(unsigned a, unsigned b, unsigned c) { return max(max(a, b), c); }
DI int tail_slot(int tokn) { return (tokn & 24) | ((tokn & 3) << 1) | ((tokn >> 2) & 1); }
DI f32x16 attn_safe(const char* qp, const char* kvh, int lane) {
  const int hi = lane >> 5, r32 = lane & 31;
  const bf16x8 q1 = *(const bf16x8*)(qp + hi * 512 + r32 * 16);
  const u32x2 q2l = *(const u32x2*)(qp + 1024 + tail_slot(r32) * 8);
  const bf16x8 q2 = __builtin_bit_cast(bf16x8, u32x4{hi ? 0u : q2l[0], hi ? 0u : q2l[1], 0u, 0u});
  f32x16 O = {};
  float m = -3.0e38f;
#pragma unroll 1
  for (int t = 0; t < 64; ++t) {
    const char* kb = kvh + (size_t)t * KV_REC;
    const char* vb = kvh + (size_t)((t & 32) | ((t + 1) & 31)) * KV_REC + 1280 + hi * 352 + min(r32, 21) * 16;
    const bf16x8 k1 = *(const bf16x8*)(kb + hi * 512 + r32 * 16);
    const u32x2 k2l = *(const u32x2*)(kb + 1024 + tail_slot(swap23(r32)) * 8);
    const bf16x8 v0 = *(const bf16x8*)(vb), v1 = *(const bf16x8*)(vb + 704);
    f32x16 sc = MFMA32(k1, q1, f32x16{});
    sc = MFMA32(__builtin_bit_cast(bf16x8, u32x4{k2l[0], k2l[1], 0u, 0u}), q2, sc);
    float tm = sc[0];
#pragma unroll
    for (int i = 1; i < 16; ++i) tm = fmaxf(tm, sc[i]);
    { auto rr = __builtin_amdgcn_permlane32_swap(__float_as_uint(tm), __float_as_uint(tm), false, false); tm = fmaxf(__uint_as_float(rr[0]), __uint_as_float(rr[1])); }
    const float mn = fmaxf(m, tm), al = __builtin_amdgcn_exp2f(m - mn);
    m = mn;
    u32x4 p0, p1;
#pragma unroll
    for (int i = 0; i < 4; ++i) {
      p0[i] = pack2(__builtin_amdgcn_exp2f(sc[2 * i] - mn), __builtin_amdgcn_exp2f(sc[2 * i + 1] - mn));
      p1[i] = pack2(__builtin_amdgcn_exp2f(sc[8 + 2 * i] - mn), __builtin_amdgcn_exp2f(sc[9 + 2 * i] - mn));
    }
#pragma unroll
    for (int i = 0; i < 16; ++i) O[i] *= al;
    O = MFMA32(v0, __builtin_bit_cast(bf16x8, p0), O);
    O = MFMA32(v1, __builtin_bit_cast(bf16x8, p1), O);
  }
  return O;
}

constexpr int R_A = 128;
template <int A0, int N> DI void ld_frags(u32x4 srd, unsigned voff, unsigned soff) {
  unsigned t;
  asm volatile("s_add_u32 %0, %3, 4096\n\t.set i_, 0\n\t.rept %c5\n\t"
               ".if i_ < 4\n\tbuffer_load_dwordx4 v[%c4+4*i_:%c4+4*i_+3], %1, %2, %3 offen offset:1024*i_ sc1\n\t"
               ".else\n\tbuffer_load_dwordx4 v[%c4+4*i_:%c4+4*i_+3], %1, %2, %0 offen sc1\n\t.endif\n\t.set i_, i_+1\n\t.endr"
               : "=&s"(t) : "v"(voff), "s"(srd), "s"(soff), "n"(R_A + 4 * A0), "n"(N) : "memory", "scc");
}
DI void ld_unit(u32x4 srd, const unsigned (&vo)[4], unsigned soff) {
  ld_frags<0, 5>(srd, vo[0], soff); ld_frags<5, 5>(srd, vo[1], soff + 5120); ld_frags<10, 5>(srd, vo[2], soff + 10240); ld_frags<15, 5>(srd, vo[3], soff + 15360);
}
template <int G0, int NG, int NKS, int MODE>
DI unsigned long long run_unit(const unsigned (&wb)[5], u32x4 srd, unsigned voff, unsigned soff) {
  unsigned long long m; unsigned t, u, so;
  asm volatile(
      "s_mov_b32 %[so], %[soff]\n\t"
      ".set gi_, 0\n\t.rept %c[ng]\n\t"
      ".set g_, %c[g0]+gi_\n\t.set gn_, g_+1\n\t.if gn_ > 34\n\t.set gn_, 34\n\t.endif\n\t"
      ".set sc_, g_-6*(g_/6)\n\t.set sn_, gn_-6*(gn_/6)\n\t.set nk_, %c[nks]-5*gi_\n\t.if nk_ > 5\n\t.set nk_, 5\n\t.endif\n\t"
      "ds_read_b64 v[254:255], %[fl]\n\tv_mov_b32 v249, g_+1\n\t"
      ".set k_, 0\n\t.rept nk_\n\t"
      ".set ks_, 5*gi_+k_\n\t.set a_, 128+4*(ks_-20*(ks_/20))\n\t"
      ".if %c[mode] > 0 && (gi_ == 0 || k_ == 0)\n\t"
      ".set need_, ks_\n\t.if gi_ > 0\n\t.set need_, ks_+nk_-1\n\t.endif\n\t"
      ".set iss_, 20\n\t.if %c[mode] == 3\n\t.set iss_, 20+ks_\n\t.if iss_ > %c[nks]\n\t.set iss_, %c[nks]\n\t.endif\n\t.endif\n\t"
      ".if %c[mode] == 2 && gi_ >= 2\n\t.set iss_, 21\n\t.endif\n\t"
      "s_waitcnt vmcnt(iss_-1-need_)\n\t"
      ".endif\n\t"
      ".if k_ == nk_-1\n\t"
      "s_cbranch_scc1 2f\n\ts_mov_b32 %[u], 0\n\t"
      "1:\n\ts_sleep 1\n\tds_read_b64 v[254:255], %[fl]\n\ts_add_u32 %[u], %[u], 1\n\ts_waitcnt lgkmcnt(0)\n\tv_min_u32 v254, v254, v255\n\ts_nop 0\n\tv_readfirstlane_b32 %[t], v254\n\t"
      "s_cmp_gt_u32 %[u], 0x400000\n\ts_cbranch_scc1 2f\n\ts_cmp_ge_u32 %[t], gn_+1\n\ts_cbranch_scc0 1b\n\t"
      "2:\n\t"
      ".endif\n\t"
      ".set t_, 0\n\t.rept 5\n\t"
      ".set f_, 5*k_+t_\n\t"
      ".if f_ == 0 || f_ == 2\n\ts_waitcnt lgkmcnt(5)\n\t.elseif (f_ & 1) == 0\n\ts_waitcnt lgkmcnt(3)\n\t.endif\n\t"
      ".if ks_ == 0\n\tv_mfma_f32_16x16x32_bf16 v[228+4*t_:231+4*t_], v[208+4*t_:211+4*t_], v[a_:a_+3], 0\n\t"
      ".else\n\tv_mfma_f32_16x16x32_bf16 v[228+4*t_:231+4*t_], v[208+4*t_:211+4*t_], v[a_:a_+3], v[228+4*t_:231+4*t_]\n\t.endif\n\t"
      ".set s_, sc_\n\t.set o_, 5120*(k_+1)+1024*t_\n\t.if k_ == nk_-1\n\t.set s_, sn_\n\t.set o_, 1024*t_\n\t.endif\n\t"
      ".if s_ < 2\n\tds_read_b128 v[208+4*t_:211+4*t_], %[w0] offset:25600*s_+o_\n\t"
      ".elseif s_ < 4\n\tds_read_b128 v[208+4*t_:211+4*t_], %[w1] offset:25600*(s_-2)+o_\n\t"
      ".else\n\tds_read_b128 v[208+4*t_:211+4*t_], %[w2] offset:25600*(s_-4)+o_\n\t.endif\n\t"
      ".if k_ == nk_-2\n\t"
      ".if t_ == 0\n\tv_min_u32 v254, v254, v255\n\t.elseif t_ == 2\n\tv_readfirstlane_b32 %[t], v254\n\t.elseif t_ == 4\n\ts_cmp_ge_u32 %[t], gn_+1\n\t.endif\n\t"
      ".endif\n\t"
      ".set t_, t_+1\n\t.endr\n\t"
      ".if %c[mode] == 3 && ks_+20 < %c[nks]\n\t"
      "buffer_load_dwordx4 v[a_:a_+3], %[voff], %[srd], %[so] offen offset:1024*(ks_&3) sc1\n\t"
      ".if (ks_&3) == 3\n\ts_addk_i32 %[so], 4096\n\t.if k_ == nk_-2\n\ts_cmp_ge_u32 %[t], gn_+1\n\t.endif\n\t.endif\n\t"
      ".endif\n\t"
      ".set k_, k_+1\n\t.endr\n\t"
      "ds_write_b32 %[cs], v249\n\t"
      ".if %c[mode] == 2 && gi_ == 1\n\tbuffer_load_dwordx4 v[250:253], %[voff], %[srd], %[so] offen sc1\n\t.endif\n\t"
      ".set gi_, gi_+1\n\t.endr\n\t"
      ".if %c[mode] == 2\n\ts_waitcnt vmcnt(0)\n\t.endif\n\t"
      "s_nop 4\n\tv_cmp_u_f32 %[m], v228, v228"
      : [m] "=&s"(m), [t] "=&s"(t), [u] "=&s"(u), [so] "=&s"(so)
      : [w0] "v"(wb[0]), [w1] "v"(wb[1]), [w2] "v"(wb[2]), [fl] "v"(wb[3]), [cs] "v"(wb[4]), [voff] "v"(voff), [srd] "s"(srd), [soff] "s"(soff),
        [g0] "n"(G0), [ng] "n"(NG), [nks] "n"(NKS), [mode] "n"(MODE)
      : "memory", "scc");
  return m;
}
DI void redo_unit(__amdgpu_buffer_rsrc_t ra, const unsigned (&vo)[4], unsigned soff, int nks, __amdgpu_buffer_rsrc_t rw, unsigned woff, u32x4 srd_a) {
  f32x4 acc[5];
#pragma unroll
  for (int t = 0; t < 5; ++t) acc[t] = f32x4{0.f, 0.f, 0.f, 0.f};
  asm volatile("s_waitcnt vmcnt(0)" ::: "memory");
#pragma unroll 1
  for (int ks = 0; ks < nks; ++ks) {
    const unsigned va = ks < 5 ? vo[0] : ks < 10 ? vo[1] : ks < 15 ? vo[2] : vo[3];
    u32x4 av;
#pragma unroll 1
    for (unsigned sp = 0; sp < SPIN_CAP; ++sp) {
      av = ld_sc1(ra, va, soff + ks * 1024);
      if (!__any(umax3(max(av[0], av[1]), av[2], av[3]) == SENT)) break;
      asm volatile("s_sleep 8" ::: "memory");
    }
#pragma unroll
    for (int t = 0; t < 5; ++t) acc[t] = MFMA16(__builtin_bit_cast(bf16x8, ld_sc1(rw, woff, (ks * 5 + t) * 1024)), __builtin_bit_cast(bf16x8, av), acc[t]);
  }
  static_for<20>([&](auto I_) AI { vset<228 + I_.value>(__float_as_uint(acc[I_.value >> 2][I_.value & 3])); });
  ld_unit(srd_a, vo, soff);
  asm volatile("s_waitcnt vmcnt(0)" ::: "memory");
}

#define EP_PACK(d, r) "v_cvt_pk_bf16_f32 v[" #d "], v[" #r "], v[" #r "+1]\n\tv_cvt_pk_bf16_f32 v[" #d "+1], v[" #r "+2], v[" #r "+3]\n\t"
#define EP_GUARD(d) "v_min_u32 v[" #d "], -2, v[" #d "]\n\tv_min_u32 v[" #d "+1], -2, v[" #d "+1]\n\t"
#define EP_GELU(R, NP)                                                                                                                       \
  ".set i_, 0\n\t.rept " #NP "\n\tv_pk_mul_f32 v[116+2*i_:117+2*i_], v[" #R "+2*i_:" #R "+1+2*i_], v[" #R "+2*i_:" #R "+1+2*i_]\n\t.set i_, i_+1\n\t.endr\n\t" \
  ".set i_, 0\n\t.rept " #NP "\n\tv_pk_fma_f32 v[116+2*i_:117+2*i_], v[116+2*i_:117+2*i_], %[B], %[A]\n\t.set i_, i_+1\n\t.endr\n\t"          \
  ".set i_, 0\n\t.rept " #NP "\n\tv_pk_mul_f32 v[116+2*i_:117+2*i_], v[116+2*i_:117+2*i_], v[" #R "+2*i_:" #R "+1+2*i_]\n\t.set i_, i_+1\n\t.endr\n\t" \
  ".set i_, 0\n\t.rept 2*" #NP "\n\tv_exp_f32 v[116+i_], v[116+i_]\n\t.set i_, i_+1\n\t.endr\n\t"                                           \
  ".set i_, 0\n\t.rept " #NP "\n\tv_pk_fma_f32 v[116+2*i_:117+2*i_], v[116+2*i_:117+2*i_], %[I], %[I]\n\t.set i_, i_+1\n\t.endr\n\t"          \
  ".set i_, 0\n\t.rept 2*" #NP "\n\tv_rcp_f32 v[116+i_], v[116+i_]\n\t.set i_, i_+1\n\t.endr\n\t"                                           \
  ".set i_, 0\n\t.rept " #NP "\n\tv_pk_mul_f32 v[" #R "+2*i_:" #R "+1+2*i_], v[" #R "+2*i_:" #R "+1+2*i_], v[116+2*i_:117+2*i_]\n\t.set i_, i_+1\n\t.endr\n\t"
struct GeluK { f32x2 nm, A, B, I; };
DI GeluK gelu_consts(float mean, float r, float ir) {
  const float c0 = -2.f * 1.4426950408889634f * 0.7978845608028654f, c1 = c0 * 0.044715f, A = c0 * r, B = c1 * r * r * r;
  return GeluK{f32x2{-mean, -mean}, f32x2{A, A}, f32x2{B, B}, f32x2{ir, ir}};
}
template <int T, int NT, int D> DI void ep_gelu(const GeluK& k, f32x4 c0, f32x4 c1) {
  asm volatile(
      "v_pk_fma_f32 v[%c[r]:%c[r]+1], %[nm], %[ca], v[%c[r]:%c[r]+1]\n\tv_pk_fma_f32 v[%c[r]+2:%c[r]+3], %[nm], %[cb], v[%c[r]+2:%c[r]+3]\n\t"
      ".if %c[nt] == 2\n\tv_pk_fma_f32 v[%c[r]+4:%c[r]+5], %[nm], %[cc], v[%c[r]+4:%c[r]+5]\n\tv_pk_fma_f32 v[%c[r]+6:%c[r]+7], %[nm], %[cd], v[%c[r]+6:%c[r]+7]\n\t.endif\n\t"
      EP_GELU(%c[r], 2*%c[nt])
      EP_PACK(%c[d], %c[r]) ".if %c[nt] == 2\n\t" EP_PACK(%c[d]+2, %c[r]+4) ".endif\n\t"
      EP_GUARD(%c[d]) ".if %c[nt] == 2\n\t" EP_GUARD(%c[d]+2) ".endif"
      ::[nm] "v"(k.nm), [A] "v"(k.A), [B] "v"(k.B), [I] "v"(k.I), [ca] "v"(f32x2{c0[0], c0[1]}), [cb] "v"(f32x2{c0[2], c0[3]}), [cc] "v"(f32x2{c1[0], c1[1]}),
        [cd] "v"(f32x2{c1[2], c1[3]}), [r] "n"(228 + 4 * T), [nt] "n"(NT), [d] "n"(D));
}
template <int R> DI void st_frag(u32x4 srd, unsigned voff, unsigned soff) { asm volatile("buffer_store_dwordx4 v[%c3:%c3+3], %0, %1, %2 offen sc1\n\ts_nop 1" ::"v"(voff), "s"(srd), "s"(soff), "n"(R) : "memory"); }
template <int R> DI void st_single(u32x4 srd, unsigned vsg, unsigned soff) {
  asm volatile("v_mov_b32 v[%c3+2], v[%c3]\n\tv_mov_b32 v[%c3+3], v[%c3+1]\n\tv_mov_b32 v248, v[%c3]\n\tv_mov_b32 v249, v[%c3+1]\n\ts_nop 1\n\t"
               "v_permlane16_swap_b32 v248, v[%c3+2]\n\tv_permlane16_swap_b32 v249, v[%c3+3]\n\t"
               "buffer_store_dwordx4 v[%c3:%c3+3], %0, %1, %2 offen sc1\n\ts_nop 1" ::"v"(vsg), "s"(srd), "s"(soff), "n"(R) : "memory");
}
template <int R> DI void st_tile(u32x4 srd, unsigned lane16, unsigned vsg, unsigned sp, unsigned ssg) {
  asm volatile(EP_PACK(116, %c0) EP_PACK(118, %c0+4) EP_GUARD(116) EP_GUARD(118) EP_PACK(120, %c0+8) EP_PACK(122, %c0+12) ::"n"(R));
  st_frag<116>(srd, lane16, sp);
  asm volatile(EP_GUARD(120) EP_GUARD(122) EP_PACK(250, %c0+16) ::"n"(R));
  st_frag<120>(srd, lane16, sp + 1024);
  asm volatile(EP_GUARD(250));
  st_single<250>(srd, vsg, ssg);
}
template <int R> DI void st_stats(__amdgpu_buffer_rsrc_t rs, unsigned vst, unsigned off) {
  float s, ss;
  asm volatile("v_pk_add_f32 v[116:117], v[%c2:%c2+1], v[%c2+2:%c2+3]\n\tv_pk_mul_f32 v[118:119], v[%c2:%c2+1], v[%c2:%c2+1]\n\tv_pk_mul_f32 v[120:121], v[%c2+2:%c2+3], v[%c2+2:%c2+3]\n\t"
               ".set i_, 2\n\t.rept 8\n\tv_pk_add_f32 v[116:117], v[116:117], v[%c2+2*i_:%c2+2*i_+1]\n\t"
               "v_pk_fma_f32 v[118+2*(i_&1):119+2*(i_&1)], v[%c2+2*i_:%c2+2*i_+1], v[%c2+2*i_:%c2+2*i_+1], v[118+2*(i_&1):119+2*(i_&1)]\n\t.set i_, i_+1\n\t.endr\n\t"
               "v_pk_add_f32 v[118:119], v[118:119], v[120:121]\n\tv_add_f32 %0, v116, v117\n\tv_add_f32 %1, v118, v119"
               : "=v"(s), "=v"(ss) : "n"(R));
  s = sum_q(s); ss = sum_q(ss);
  st_sc1(u32x2{nosent(__float_as_uint(s)), nosent(__float_as_uint(ss))}, rs, vst, off);
}
DI void read_stats(__amdgpu_buffer_rsrc_t rs, unsigned voff, unsigned soff, float& mean, float& var) {
  u32x4 st = __builtin_bit_cast(u32x4, vget4<250>());
  if (__builtin_expect(__any(umax3(max(st[0], st[1]), st[2], st[3]) == SENT), 0)) {
#pragma unroll 1
    for (unsigned sp = 0; sp < SPIN_CAP; ++sp) {
      asm volatile("s_sleep 8" ::: "memory");
      st = ld_sc1(rs, voff, soff);
      if (!__any(umax3(max(st[0], st[1]), st[2], st[3]) == SENT)) break;
    }
  }
  const float s = sum_q(__uint_as_float(st[0]) + __uint_as_float(st[2])), ss = sum_q(__uint_as_float(st[1]) + __uint_as_float(st[3]));
  mean = s * (1.f / D_);
  var = fmaxf(ss * (1.f / D_) - mean * mean, 0.f);
}
template <unsigned long long MASK> DI void arrive(u32x4 srd, unsigned voff) {
  unsigned long long sv;
  asm volatile("s_mov_b64 %0, exec\n\ts_mov_b64 exec, %3\n\tv_mov_b32 v249, 1\n\tbuffer_atomic_add v249, %1, %2, 0 offen sc1\n\ts_mov_b64 exec, %0"
               : "=&s"(sv) : "v"(voff), "s"(srd), "n"(MASK) : "memory");
}
template <unsigned long long MASK> DI void await(u32x4 srd, unsigned voff, unsigned expect) {
  unsigned long long sv; unsigned n;
  asm volatile("s_mov_b64 %0, exec\n\ts_mov_b64 exec, %5\n\ts_mov_b32 %1, 0\n\tv_mov_b32 v126, 0\n\tv_mov_b32 v127, 0\n\t"
               "buffer_atomic_add v126, %2, %3, 0 offen sc0 sc1\n\ts_sleep 3\n\t"
               "1:\n\tbuffer_atomic_add v127, %2, %3, 0 offen sc0 sc1\n\ts_waitcnt vmcnt(1)\n\tv_cmp_ne_u32 vcc, %4, v126\n\ts_cbranch_vccz 2f\n\t"
               "v_mov_b32 v126, 0\n\ts_add_u32 %1, %1, 1\n\tbuffer_atomic_add v126, %2, %3, 0 offen sc0 sc1\n\ts_waitcnt vmcnt(1)\n\tv_cmp_ne_u32 vcc, %4, v127\n\ts_cbranch_vccz 2f\n\t"
               "v_mov_b32 v127, 0\n\ts_cmp_lt_u32 %1, 0x40000\n\ts_cbranch_scc1 1b\n\t"
               "2:\n\ts_mov_b64 exec, %0"
               : "=&s"(sv), "=&s"(n) : "v"(voff), "s"(srd), "s"(expect), "n"(MASK) : "memory", "scc", "vcc");
}

DI void touch(u32x4 srd, unsigned voff, unsigned soff) { asm volatile("buffer_load_dword v126, %0, %1, %2 offen sc1" ::"v"(voff), "s"(srd), "s"(soff) : "memory"); }
template <int R> DI void ld_cs(u32x4 srd, unsigned voff, unsigned step) {
  asm volatile(".set i_, 0\n\t.rept 20\n\tbuffer_load_dword v[%c3+i_], %0, %1, 0 offen sc1\n\tv_add_u32 %0, %0, %2\n\t.set i_, i_+1\n\t.endr" : "+v"(voff) : "s"(srd), "v"(step), "n"(R) : "memory");
}
template <int R> DI u32x4 tr_col(unsigned sel) {
  unsigned o0, o1, o2, o3;
  asm volatile("v_perm_b32 %0, v[%c5+4], v[%c5], %4\n\tv_perm_b32 %1, v[%c5+12], v[%c5+8], %4\n\tv_perm_b32 %2, v[%c5+20], v[%c5+16], %4\n\tv_perm_b32 %3, v[%c5+28], v[%c5+24], %4"
               : "=v"(o0), "=v"(o1), "=v"(o2), "=v"(o3) : "s"(sel), "n"(R));
  return u32x4{o0, o1, o2, o3};
}

#define AE(i) "v_exp_f32 v[%c[sc]+" #i "], v[%c[sc]+" #i "]\n\t"
#define AC(d, i) "v_cvt_pk_bf16_f32 v" #d ", v[%c[sc]+" #i "], v[%c[sc]+" #i "+1]\n\t"
template <int SC, int SN, int OV> DI void att_a(unsigned vA) {
  asm volatile(
      "s_waitcnt lgkmcnt(1)\n\t"
      "v_mfma_f32_32x32x16_bf16 v[144:159], v[242:245], v[228:231], v[144:159]\n\t"
      AE(0) AE(1) "ds_read_b128 v[242:245], %[va] offset:%c[ov]+704\n\t" AE(2) AE(3) AC(224, 0)
      "v_mfma_f32_32x32x4_2b_bf16 v[%c[sn]:%c[sn]+31], v[236:237], v[254:255], 0\n\t"
      AC(225, 2) AE(4) AE(5) AE(6) AE(7) AC(226, 4) AC(227, 6) AE(8) AE(9) AE(10) AE(11) AC(228, 8)
      "s_waitcnt lgkmcnt(1)\n\t"
      "v_mfma_f32_32x32x16_bf16 v[128:143], v[238:241], v[224:227], v[128:143]\n\t"
      AC(229, 10) AE(12) AE(13) AE(14) AE(15) AC(230, 12) AC(231, 14)
      ::[va] "v"(vA), [sc] "n"(SC), [sn] "n"(SN), [ov] "n"(OV) : "memory");
}
template <int SC, int SN, int OK> DI void att_b(unsigned kA, unsigned kB, unsigned vA) {
  asm volatile(
      AE(16) "s_waitcnt lgkmcnt(0)\n\t"
      "v_mfma_f32_32x32x16_bf16 v[128:143], v[242:245], v[228:231], v[128:143]\n\t"
      AE(17) AE(18) AE(19) AC(224, 16)
      "v_mfma_f32_32x32x16_bf16 v[%c[sn]:%c[sn]+15], v[232:235], v[246:249], v[%c[sn]:%c[sn]+15]\n\t"
      AC(225, 18) AE(20) AE(21) AE(22) AE(23) AC(226, 20) AC(227, 22)
      "v_mfma_f32_32x32x16_bf16 v[%c[sn]+16:%c[sn]+31], v[232:235], v[250:253], v[%c[sn]+16:%c[sn]+31]\n\t"
      AE(24) AE(25) "ds_read_b128 v[232:235], %[ka] offset:%c[ok]\n\tds_read_b64 v[236:237], %[kb] offset:%c[ok]\n\t" AE(26) AE(27) AC(228, 24)
      "v_mfma_f32_32x32x16_bf16 v[144:159], v[238:241], v[224:227], v[144:159]\n\t"
      AC(229, 26) AE(28) AE(29) AE(30) AE(31) AC(230, 28) AC(231, 30)
      "ds_read_b128 v[238:241], %[va] offset:%c[ok]"
      ::[ka] "v"(kA), [kb] "v"(kB), [va] "v"(vA), [sc] "n"(SC), [sn] "n"(SN), [ok] "n"(OK) : "memory");
}
#undef AE
#undef AC
template <int SEND, int KEEP> DI void att_merge(float (&O)[12], unsigned mine, unsigned theirs) {
  asm volatile("ds_write_b128 %0, v[%c2:%c2+3]\n\tds_write_b128 %0, v[%c2+4:%c2+7] offset:1024\n\tds_write_b128 %0, v[%c2+8:%c2+11] offset:2048\n\t"
               "s_waitcnt vmcnt(0) lgkmcnt(0)\n\ts_barrier\n\t"
               "ds_read_b128 v[160:163], %1\n\tds_read_b128 v[164:167], %1 offset:1024\n\tds_read_b128 v[168:171], %1 offset:2048\n\ts_waitcnt lgkmcnt(0)\n\ts_barrier"
               ::"v"(mine), "v"(theirs), "n"(SEND) : "memory");
  static_for<12>([&](auto I_) AI { O[I_.value] = vadd<KEEP + I_.value, 160 + I_.value>(); });
}

__global__ void __launch_bounds__(512, 2) __attribute__((amdgpu_num_vgpr(48))) k_main(const char* __restrict__ x, const char* __restrict__ wo, const char* __restrict__ w1, const char* __restrict__ w2,
    const char* __restrict__ w3, const char* __restrict__ w4, char* __restrict__ ws, char* __restrict__ out) {
  extern __shared__ __attribute__((aligned(16))) char smem[];
  lds_ptr lds = (lds_ptr)smem;
  OWN_ALL_VGPRS();
  STAMP_DECL; STAMP(0);
  const unsigned ldsb = (unsigned)(size_t)lds;
  const int tid = threadIdx.x, lane = tid & 63, w = __builtin_amdgcn_readfirstlane(tid >> 6), q = lane >> 4, tok = lane & 15;
  const int bid = blockIdx.x, xcd = bid & 7, r = bid >> 3;
  const int j = r & 7, head = 8 * (xcd & 3) + j, qb = 4 * (xcd >> 2) + (r >> 3), rb = 4 * qb + (xcd & 3);
  const __amdgpu_buffer_rsrc_t rsw = mk_rsrc(ws, WS_END);
  const unsigned wfs = OFF_WF + j * NGRP * GRP_B;
  const unsigned lane16 = lane * 16;

  const int hi = lane >> 5, r32 = lane & 31, wq = w & 3, wk = w >> 2;
  const u32x4 srd = mk_srd(ws, WS_END);
  {
    const char* qp = ws + OFF_QF + (size_t)(head * 64 + 8 * qb + 2 * wq) * QF_TILE;
    gload4<246, 0>(qp, hi * 512 + r32 * 16);
    gload4<250, QF_TILE>(qp, hi * 512 + r32 * 16);
    asm volatile("global_load_dwordx2 v[254:255], %0, %1 offset:1024" ::"v"(hi * QF_TILE + tail_slot(r32) * 8), "s"(qp) : "memory");
  }
  unsigned kvs[3], kvd[3];
#pragma unroll
  for (int i = 0; i < 3; ++i) {
    const int pc = min(w + 8 * i, 21), h2 = pc >= 11;
    kvs[i] = OFF_KV + (head * 64 + 32 * h2) * KV_REC + (pc - 11 * h2) * 1024;
    kvd[i] = ldsb + L_KV0 + pc * 1024;
  }
  auto kv_piece = [&](int c, int i) AI { bdma(srd, lane16, kvs[i] + (c & 7) * (4 * KV_REC), kvd[i] + (c & 1) * KV_SLOT); };
  kv_piece(0, 0); kv_piece(0, 1); kv_piece(0, 2);
  kv_piece(1, 0); kv_piece(1, 1); kv_piece(1, 2);
  const int job = w * 256 + bid;
  const bool has_job = job < 850;
  int mat, ks, cb;
  if (job < 300) { mat = job / 100; const int e = job - 100 * mat; ks = e / 5; cb = e - 5 * ks; }
  else if (job < 580) { mat = 3; const int e = job - 300; ks = e / 14; cb = e - 14 * ks; }
  else { mat = 4; const int e = job - 580; ks = e / 5; cb = e - 5 * ks; }
  const char* src = mat == 0 ? wo : mat == 1 ? w1 : mat == 2 ? w2 : mat == 3 ? w3 : w4;
  if (tid < 272) *(__attribute__((address_space(3))) unsigned*)(lds + L_FILL + tid * 4) = tid < 2 ? 4u + tid : 0u;
  const unsigned kvb = ldsb + L_KV0 + wk * KV_HALF;
  unsigned kA = kvb + hi * 512 + r32 * 16, kB = kvb + 1024 + tail_slot(swap23(r32)) * 8, vA = kvb + 1280 + hi * 352 + r32 * 16;
  asm volatile(
      ".set i_, 0\n\t.rept 32\n\tv_mov_b32 v[128+i_], 0\n\t.set i_, i_+1\n\t.endr\n\t"
      ".set i_, 0\n\t.rept 4\n\tv_mov_b32 v[228+i_], 0\n\tv_mov_b32 v[242+i_], 0\n\t.set i_, i_+1\n\t.endr\n\t"
      "s_waitcnt vmcnt(3) lgkmcnt(0)\n\ts_barrier\n\t"
      "ds_read_b64 v[236:237], %1\n\tds_read_b128 v[232:235], %0\n\ts_waitcnt lgkmcnt(1)\n\t"
      "v_mfma_f32_32x32x4_2b_bf16 v[160:191], v[236:237], v[254:255], 0\n\t"
      "s_nop 15\n\ts_nop 1\n\ts_waitcnt lgkmcnt(0)\n\t"
      "v_mfma_f32_32x32x16_bf16 v[160:175], v[232:235], v[246:249], v[160:175]\n\t"
      "v_mfma_f32_32x32x16_bf16 v[176:191], v[232:235], v[250:253], v[176:191]\n\t"
      "ds_read_b128 v[232:235], %0 offset:%c3\n\tds_read_b64 v[236:237], %1 offset:%c3\n\tds_read_b128 v[238:241], %2 offset:%c3\n\t"
      "s_nop 7"
      ::"v"(kA), "v"(kB), "v"(vA), "n"(KV_REC) : "memory");
  STAMP(2);
  auto job_cols = [&](auto S_) AI {
    constexpr int sl = S_.value;
    int ln = lane; asm volatile("" : "+v"(ln));
    const int lq = ln >> 4, lt = ln & 15;
      {
        const int kk = 4 * cb + (lt >> 2), n0 = 128 * cb + 8 * lt;
        int jj, t, row, G;
        if (mat == 3) {
          const int half = kk >= 27, k27 = kk - 27 * half, j4 = min((k27 * 37) >> 8, 3);
          jj = 4 * half + j4; t = 2 * (k27 - 7 * j4) + (sl >> 1); row = 4 * (lt & 3) + 2 * (sl & 1);
          const int pass = (t * 13) >> 6; t -= 5 * pass; G = 12 + 4 * pass;
        } else {
          const int per5 = (kk * 13) >> 6, m5 = kk - 5 * per5;
          const bool single = m5 == 2;
          jj = 2 * per5 + (single ? ((lt >> 1) & 1) : (m5 > 2)); t = single ? 4 : 2 * (m5 > 2 ? m5 - 3 : m5) + (sl >> 1);
          row = single ? 8 * (lt & 1) + 2 * sl : 4 * (lt & 3) + 2 * (sl & 1);
          G = mat == 4 ? 24 : 4 * mat;
        }
        const bool ok = mat != 3 || n0 < F_;
        const unsigned vo = ok ? (unsigned)((jj * NGRP + G) * 25 + t) * 1024 + (lq * 16 + 4 * (row & 3) + (row >> 2)) * 16 : 0x80000000u;
        const unsigned so = OFF_WF + ((ks / 5) * 25 + (ks % 5) * 5) * 1024;
        const u32x4 o0 = tr_col<96 + sl>(0x05040100u), o1 = tr_col<96 + sl>(0x07060302u);
        st_sc1(o0, rsw, vo, so);
        st_sc1(o1, rsw, vo + 64, so);
        if (mat == 1 || mat == 3) {
          float ca = 0.f, cb2 = 0.f;
#pragma unroll
          for (int d = 0; d < 4; ++d) { ca = dot2(o0[d], 0x3f803f80u, ca); cb2 = dot2(o1[d], 0x3f803f80u, cb2); }
          ca = sum_q(ca); cb2 = sum_q(cb2);
          st_sc1(u32x2{__float_as_uint(ca), __float_as_uint(cb2)}, rsw, (ok && lq == 0) ? (unsigned)(n0 + 2 * sl) * 4 : 0x80000000u, (mat == 1 ? OFF_CS1 + ks * D_ * 4 : OFF_CS3 + ks * F_ * 4));
        }
      }
  };
  const unsigned rt = 4 * rb + (w & 3);
  const unsigned kp = 5 * (j >> 1) + 3 * (j & 1), ksg = 5 * (j >> 1) + 2;
  const unsigned rowoff = ((16 * rt + tok) * D_) * 2;
  const unsigned colp = (32 * kp + 8 * q) * 2, cols = (32 * ksg + 16 * (j & 1) + 4 * q) * 2;
  auto fill = [&](auto I_) AI {
    constexpr int it = I_.value;
    const u32x4 sent = {SENT, SENT, SENT, SENT};
    if (w < 4) {
      if constexpr (it < 9) {
        const unsigned o = OFF_H + (it / 3) * SZ_FM + rt * FM_RT;
        if constexpr (it % 3 < 2) st_sc1(sent, rsw, lane16, o + (kp + it % 3) * 1024);
        else st_sc1(sent, rsw, lane < 32 ? (j & 1) * 512 + lane16 : 0x80000000u, o + ksg * 1024);
      } else if constexpr (it < 11) st_sc1(u32x2{SENT, SENT}, rsw, lane < 16 ? (j >> 1) * 256 + lane16 + (j & 1) * 8 : 0x80000000u, (it == 9 ? OFF_ST1 : OFF_ST2) + rt * 1024);
    } else if constexpr (it < 7) {
      if (it < 6 || (j & 3) != 3) st_sc1(sent, rsw, lane16, OFF_G + rt * G_RT + (27 * (j >> 2) + 7 * (j & 3) + it) * 1024);
    }
  };
  auto side = [&](int c, auto K_, auto S_) AI {
    constexpr int kind = K_.value, sl = S_.value;
    if constexpr (kind == 0) {
      if constexpr (sl < 2) {
        if (has_job) {
          int ln = lane; asm volatile("" : "+v"(ln));
          const int lq = ln >> 4, N = mat == 3 ? F_ : D_, n0 = 128 * cb + 8 * (ln & 15);
          int k0 = 32 * ks + 8 * lq + 4 * sl;
          if (mat == 0) { const int o = 4 * ks + lq, i = (o * 205) >> 10, oo = o - 5 * i; k0 = 40 * i + (oo < 3 ? 8 * oo : 8 * oo - 4) + 4 * sl + ((sl == 1 && oo == 2) ? 16 : 0); }
          const unsigned off = (unsigned)(k0 * N + (n0 < N ? n0 : 0)) * 2;
          gload4<96 + 16 * sl, 0>(src, off); gload4<100 + 16 * sl, 0>(src + N * 2, off);
          gload4<104 + 16 * sl, 0>(src + N * 4, off); gload4<108 + 16 * sl, 0>(src + N * 6, off);
        }
      } else { fill(IC<2 * sl - 4>{}); fill(IC<2 * sl - 3>{}); }
    }
    else if constexpr (kind == 1) {
      if (has_job) {
        if (sl == 0) asm volatile("s_waitcnt vmcnt(7)" ::: "memory");
        job_cols(S_);
      }
    } else if constexpr (kind == 2) fill(IC<4 + sl>{});
    else if constexpr (kind == 3) {
      fill(IC<8 + sl>{});
      if (sl == 3 && w < 4) {
        gload4<116, 0>(x, rowoff + colp); gload4<120, 64>(x, rowoff + colp);
        asm volatile("global_load_dwordx2 v[124:125], %0, %1" ::"v"(rowoff + cols), "s"(x) : "memory");
      }
    } else if constexpr (kind == 4) {
      if (w == 0) {
        if (sl == 0) asm volatile("buffer_load_dwordx4 v[96:99], %0, %1, %2 offen sc1" ::"v"(lane16), "s"(srd), "s"(OFF_FLAG) : "memory");
        if (sl == 1) {
          unsigned long long ok;
          asm volatile("s_waitcnt vmcnt(0)\n\tv_and_b32 v96, v96, v97\n\tv_and_b32 v98, v98, v99\n\tv_and_b32 v96, v96, v98\n\tv_cmp_ne_u32 %0, 1, v96" : "=s"(ok)::"memory");
          if (__builtin_expect(ok != 0, 0)) {
#pragma unroll 1
            for (unsigned sp = 0; sp < SPIN_CAP; ++sp) {
              const u32x4 f = ld_sc1(rsw, lane16, OFF_FLAG);
              if (__all((f[0] & f[1] & f[2] & f[3]) == 1u)) break;
              asm volatile("s_sleep 4" ::: "memory");
            }
          }
        }
      }
    } else {
      const unsigned pc = (w + 8 * sl + 32 * (c - 5)) * 1024;
      bdma_sc1(srd, lane16, wfs + pc, ldsb + pc);
      if (sl == 3) touch(srd, lane * 128, c < TOUCH_IT ? wfs + 4 * GRP_B + (((c - 5) * 8 + w) * 4 + (r >> 3)) * 8192 : 0xf0000000u);
    }
  };
  auto chunk = [&](int c, auto K_) AI {
    att_a<160, 192, 1 * KV_REC>(vA); side(c, K_, IC<0>{}); att_b<160, 192, 2 * KV_REC>(kA, kB, vA); side(c, K_, IC<1>{});
    att_a<192, 160, 2 * KV_REC>(vA); side(c, K_, IC<2>{}); att_b<192, 160, 3 * KV_REC>(kA, kB, vA); side(c, K_, IC<3>{});
    att_a<160, 192, 3 * KV_REC>(vA);
    if (K_.value == 0 && has_job) asm volatile("s_waitcnt vmcnt(12) lgkmcnt(0)\n\ts_barrier" ::: "memory");
    else if (K_.value == 0) asm volatile("s_waitcnt vmcnt(4) lgkmcnt(0)\n\ts_barrier" ::: "memory");
    else if (K_.value == 5) asm volatile("s_waitcnt vmcnt(5) lgkmcnt(0)\n\ts_barrier" ::: "memory");
    else if (K_.value == 1 && has_job) asm volatile("s_waitcnt vmcnt(8) lgkmcnt(0)\n\ts_barrier" ::: "memory");
    else if (K_.value == 2) asm volatile("s_waitcnt vmcnt(2) lgkmcnt(0)\n\ts_barrier" ::: "memory");
    else if (K_.value == 3 && w < 4) asm volatile("s_waitcnt vmcnt(6) lgkmcnt(0)\n\ts_barrier" ::: "memory");
    else asm volatile("s_waitcnt vmcnt(0) lgkmcnt(0)\n\ts_barrier" ::: "memory");
    { const int d = (c & 1) ? -KV_SLOT : KV_SLOT; kA += d; kB += d; vA += d; }
    att_b<160, 192, 0>(kA, kB, vA);
    if (K_.value < 5 || c < 7) kv_piece(c + 2, 0);
    else if (w < 4) bdma_sc1(srd, lane16, wfs + (96 + w) * 1024, ldsb + (96 + w) * 1024);
    if (K_.value == 2 && tid == 0) __hip_atomic_store((unsigned*)(ws + OFF_FLAG) + bid, 1u, __ATOMIC_RELAXED, __HIP_MEMORY_SCOPE_AGENT);
    att_a<192, 160, 0>(vA);
    if (K_.value < 5 || c < 7) kv_piece(c + 2, 1);
    att_b<192, 160, KV_REC>(kA, kB, vA);
    if (K_.value < 5 || c < 7) kv_piece(c + 2, 2);
  };
  chunk(0, IC<0>{}); chunk(1, IC<1>{}); chunk(2, IC<2>{}); chunk(3, IC<3>{}); chunk(4, IC<4>{});
#pragma unroll 1
  for (int c = 5; c < 8; ++c) chunk(c, IC<5>{});
  const int qt = 8 * qb + 2 * wq + wk;
  {
    asm volatile("s_waitcnt lgkmcnt(0)\n\tv_mfma_f32_32x32x16_bf16 v[144:159], v[242:245], v[228:231], v[144:159]\n\ts_nop 11" ::: "memory");
    STAMP(3);
    float O[12];
    const unsigned mine = ldsb + L_SCR + w * 3072 + lane16, theirs = ldsb + L_SCR + (w ^ 4) * 3072 + lane16;
    if (wk == 0) att_merge<144, 128>(O, mine, theirs); else att_merge<128, 144>(O, mine, theirs);
    auto rr = __builtin_amdgcn_permlane32_swap(__float_as_uint(O[8]), __float_as_uint(O[8]), false, false);
    float l = __uint_as_float(rr[1]);
    bool fin = true;
#pragma unroll
    for (int i = 0; i < 12; ++i) fin = fin && (fabsf(O[i]) <= 3.0e38f || (hi && i > 8));
    const bool bad = !(l >= 8.6736174e-19f && l <= 1.2676506e30f && fin);
    if (__builtin_expect(__any(bad), 0)) {
      const f32x16 Os = attn_safe(ws + OFF_QF + (size_t)(head * 64 + qt) * QF_TILE, ws + OFF_KV + (size_t)head * 64 * KV_REC, lane);
#pragma unroll
      for (int i = 0; i < 12; ++i) O[i] = Os[i];
      auto r2 = __builtin_amdgcn_permlane32_swap(__float_as_uint(O[8]), __float_as_uint(O[8]), false, false);
      l = __uint_as_float(r2[1]);
    }
    const float il = __builtin_amdgcn_rcpf(l);
    const unsigned rto = OFF_O + (2 * qt + (r32 >> 4)) * FM_RT + (r32 & 15) * 16;
    const int o1 = 5 * (head >> 1) + 3 * (head & 1) + hi, o2 = 5 * (head >> 1) + 2;
    u32x4 o = {nosent(pack2(O[0] * il, O[1] * il)), nosent(pack2(O[2] * il, O[3] * il)), nosent(pack2(O[4] * il, O[5] * il)), nosent(pack2(O[6] * il, O[7] * il))};
    st_sc1(o, rsw, rto + o1 * 256, 0);
    if (!hi) st_sc1(u32x2{nosent(pack2(O[8] * il, O[9] * il)), nosent(pack2(O[10] * il, O[11] * il))}, rsw, rto + o2 * 256 + (head & 1) * 8, 0);
    arrive<0xffffull>(srd, OFF_CNT + (16 * qt + lane) * 256 + (head >> 3) * 64);
  }
  if (w >= 6) {
    static_for<3>([&](auto R_) AI {
      const int id = min(tid - 384 + 128 * R_.value, 319);
      const bool is1 = id < 80;
      const int i = is1 ? id : id - 80, t = i >> 4, row = i & 15;
      int n;
      if (is1) n = t < 4 ? 32 * (kp + (t >> 1)) + 8 * (row >> 2) + 4 * (t & 1) + (row & 3) : 32 * ksg + 16 * (j & 1) + row;
      else n = min(32 * (27 * (j >> 2) + 7 * (j & 3) + (t >> 1)) + 8 * (row >> 2) + 4 * (t & 1) + (row & 3), F_ - 1);
      ld_cs<128 + 20 * R_.value>(srd, (is1 ? OFF_CS1 : OFF_CS3) + n * 4, (is1 ? D_ : F_) * 4);
    });
    asm volatile("s_waitcnt vmcnt(0)" ::: "memory");
    static_for<3>([&](auto R_) AI {
      float sacc = 0.f;
      static_for<20>([&](auto K_) AI { sacc += vget<128 + 20 * R_.value + K_.value>(); });
      *(__attribute__((address_space(3))) float*)(lds + L_CS1 + min(tid - 384 + 128 * R_.value, 319) * 4) = sacc;
    });
    asm volatile("s_waitcnt lgkmcnt(0)" ::: "memory");
    *(volatile __attribute__((address_space(3))) unsigned*)(lds + L_CSF + (w - 6) * 4) = 1u;
    if (w == 6) {
      volatile __attribute__((address_space(3))) unsigned* consw = (volatile __attribute__((address_space(3))) unsigned*)(lds + L_CONS);
#pragma unroll 1
      for (int b = 32 * (TOUCH_IT - 5) + (r >> 3); b < 97; b += 4) {
        const int need = 4 + (b * 8192) / (int)GRP_B - TOUCH_AHEAD;
#pragma unroll 1
        for (unsigned sp = 0; sp < 0x400000u && (int)__builtin_amdgcn_readfirstlane(*consw) < need; ++sp) asm volatile("s_sleep 16" ::: "memory");
        touch(srd, lane * 128, wfs + 4 * GRP_B + b * 8192);
      }
    }
    return;
  }

  if (w >= 4) {
    auto issue = [&](int G) AI {
      const unsigned so = wfs + G * GRP_B, d = ldsb + (G % 6) * GRP_B;
#pragma unroll
      for (int i = 0; i < 6; ++i) bdma4_sc1(srd, lane16, so + i * 4096, d + i * 4096);
      bdma_sc1(srd, lane16, so + 24576, d + 24576);
    };
    volatile __attribute__((address_space(3))) unsigned* fillw = (volatile __attribute__((address_space(3))) unsigned*)(lds + L_FILL + (w - 4) * 4);
    volatile __attribute__((address_space(3))) unsigned* consw = (volatile __attribute__((address_space(3))) unsigned*)(lds + L_CONS + (lane & 3) * 256);
    __builtin_amdgcn_s_setprio(3);
    int have = 0;
    auto min_cons = [&]() AI { const unsigned c = *consw; return (int)min(min(__builtin_amdgcn_readlane(c, 0), __builtin_amdgcn_readlane(c, 1)), min(__builtin_amdgcn_readlane(c, 2), __builtin_amdgcn_readlane(c, 3))); };
#pragma unroll 1
    for (int G = w; G < NGRP; G += 2) {
      const int need = G - 5;
      if (have < need && (have = min_cons()) < need) {
        asm volatile("s_waitcnt vmcnt(0)" ::: "memory");
        *fillw = G;
#pragma unroll 1
        for (unsigned sp = 0; sp < 0x400000u && (have = min_cons()) < need; ++sp) asm volatile("s_sleep 2" ::: "memory");
      }
      issue(G);
      asm volatile("s_waitcnt vmcnt(25)" ::: "memory");
      *fillw = G;
    }
    asm volatile("s_waitcnt vmcnt(0)" ::: "memory");
    *fillw = NGRP;
    return;
  }

  __builtin_amdgcn_s_setprio(1);
  const unsigned wl = ldsb + (q * 16 + 4 * (tok & 3) + (tok >> 2)) * 16;
  const unsigned wb[5] = {wl, wl + 2 * GRP_B, wl + 4 * GRP_B, ldsb + L_FILL, ldsb + L_CONS + w * 256 + lane * 4};
  const unsigned vo0[4] = {lane16, lane16, lane16, lane16};
  asm volatile(".set i_, 0\n\t.rept 5\n\tds_read_b128 v[208+4*i_:211+4*i_], %0 offset:1024*i_\n\t.set i_, i_+1\n\t.endr\n\ts_waitcnt lgkmcnt(0)" ::"v"(wb[0]) : "memory");
  const unsigned cown = OFF_CNT + (rt * 8 + j) * 256, cgrp = OFF_CNT + (rt * 8 + lane) * 256;
  const unsigned vsg = (q & 1) ? 0x80000000u : (2 * (j & 1) + (q >> 1)) * 256 + tok * 16;
  const unsigned vst = lane < 16 ? (j >> 1) * 256 + lane16 + (j & 1) * 8 : 0x80000000u;
  const unsigned wfl = wfs + (wl - ldsb);
  typedef const __attribute__((address_space(3))) f32x4* lcs;
  asm volatile(".set i_, 0\n\t.rept 10\n\tv_lshlrev_b32 v[96+2*i_], 16, v[116+i_]\n\tv_and_b32 v[97+2*i_], 0xffff0000, v[116+i_]\n\t.set i_, i_+1\n\t.endr");
  STAMP(4);
  await<0xfull>(srd, cown + lane * 64, 8);
  STAMP(5);
  ld_unit(srd, vo0, OFF_O + rt * FM_RT);
  if (__builtin_expect(run_unit<0, 4, 20, 1>(wb, srd, 0, 0) != 0, 0)) redo_unit(rsw, vo0, OFF_O + rt * FM_RT, 20, rsw, wfl, srd);
  STAMP(6);
  asm volatile(".set i_, 0\n\t.rept 10\n\tv_pk_add_f32 v[96+2*i_:97+2*i_], v[96+2*i_:97+2*i_], v[228+2*i_:229+2*i_]\n\t.set i_, i_+1\n\t.endr");
  st_tile<96>(srd, lane16, vsg, OFF_H + rt * FM_RT + kp * 1024, OFF_H + rt * FM_RT + ksg * 1024);
  arrive<0xffull>(srd, cgrp + 4);
  st_stats<96>(rsw, vst, OFF_ST1 + rt * 1024);
  STAMP(7);
#pragma unroll 1
  for (unsigned sp = 0; sp < 0x400000u && !__all(*(volatile __attribute__((address_space(3))) unsigned*)(lds + L_CSF + (lane & 1) * 4) != 0u); ++sp) asm volatile("s_sleep 1" ::: "memory");
  f32x4 cs[5];
#pragma unroll
  for (int t = 0; t < 5; ++t) cs[t] = *(lcs)(lds + L_CS1 + t * 64 + q * 16);
  await<1ull>(srd, cown + 4, 8);
  STAMP(8);
  ld_unit(srd, vo0, OFF_H + rt * FM_RT);
  if (__builtin_expect(run_unit<4, 4, 20, 2>(wb, srd, lane16, OFF_ST1 + rt * 1024) != 0, 0)) redo_unit(rsw, vo0, OFF_H + rt * FM_RT, 20, rsw, wfl + 4 * GRP_B, srd);
  STAMP(9);
  float mean1, r1;
  {
    float var;
    read_stats(rsw, lane16, OFF_ST1 + rt * 1024, mean1, var);
    r1 = __builtin_amdgcn_rsqf(var + LN_EPS);
    const float v2 = var * r1 * r1 + LN_EPS, r2 = r1 * __builtin_amdgcn_rsqf(v2);
    const GeluK gk = gelu_consts(mean1, r2, __builtin_amdgcn_sqrtf((var + LN_EPS) * v2));
    const unsigned o = OFF_H1 + rt * FM_RT;
    ep_gelu<0, 2, 228>(gk, cs[0], cs[1]); st_frag<228>(srd, lane16, o + kp * 1024);
    ep_gelu<2, 2, 236>(gk, cs[2], cs[3]); st_frag<236>(srd, lane16, o + kp * 1024 + 1024);
    ep_gelu<4, 1, 244>(gk, cs[4], cs[4]); st_single<244>(srd, vsg, o + ksg * 1024);
    const int rn = (j & 2) ? (int)rt - 1 : (int)rt + 1;
    if (rn >= 0 && rn < 128) arrive<0xffffull>(srd, (lane < 8 ? cgrp : OFF_CNT + (rn * 8 + lane - 8) * 256) + 68);
    else arrive<0xffull>(srd, cgrp + 68);
  }
  {
    const u32x4 srd1 = mk_srd(ws + OFF_H1, SZ_FM);
    unsigned vo[4];
#pragma unroll
    for (int g = 0; g < 4; ++g) {
      const int T = 16 * (int)rt + tok + (g == 0 ? -1 : g == 1 ? 1 : g == 2 ? -2 : 2);
      vo[g] = (T >= 0 && T < S_) ? (unsigned)((T >> 4) * FM_RT + q * 256 + (T & 15) * 16) : 0x80000000u;
    }
    const f32x2 rr = {r1, r1}, nmr = {-mean1 * r1, -mean1 * r1};
    STAMP(10);
    await<1ull>(srd, cown + 68, (rt == 0 || rt == 127) ? 12 : 16);
    ld_unit(srd1, vo, 0);
    if (__builtin_expect(run_unit<8, 4, 20, 1>(wb, srd, 0, 0) != 0, 0)) redo_unit(mk_rsrc(ws + OFF_H1, SZ_FM), vo, 0, 20, rsw, wfl + 8 * GRP_B, srd1);
    STAMP(11);
    asm volatile(".set i_, 0\n\t.rept 10\n\tv_pk_fma_f32 v[228+2*i_:229+2*i_], v[96+2*i_:97+2*i_], %0, v[228+2*i_:229+2*i_]\n\t.set i_, i_+1\n\t.endr\n\t"
                 ".set i_, 0\n\t.rept 10\n\tv_pk_add_f32 v[228+2*i_:229+2*i_], v[228+2*i_:229+2*i_], %1\n\t.set i_, i_+1\n\t.endr" ::"v"(rr), "v"(nmr));
    st_tile<228>(srd, lane16, vsg, OFF_X1 + rt * FM_RT + kp * 1024, OFF_X1 + rt * FM_RT + ksg * 1024);
    arrive<0xffull>(srd, cgrp + 132);
    st_stats<228>(rsw, vst, OFF_ST2 + rt * 1024);
    asm volatile(".set i_, 0\n\t.rept 10\n\tv_pk_add_f32 v[96+2*i_:97+2*i_], v[96+2*i_:97+2*i_], v[228+2*i_:229+2*i_]\n\t.set i_, i_+1\n\t.endr");
  }
  STAMP(12);
#pragma unroll
  for (int t = 0; t < 5; ++t) cs[t] = *(lcs)(lds + L_CS3 + t * 64 + q * 16);
  await<1ull>(srd, cown + 132, 8);
  ld_unit(srd, vo0, OFF_X1 + rt * FM_RT);
  {
    const unsigned gb = OFF_G + rt * G_RT + (27 * (j >> 2) + 7 * (j & 3)) * 1024;
    if (__builtin_expect(run_unit<12, 4, 20, 2>(wb, srd, lane16, OFF_ST2 + rt * 1024) != 0, 0)) redo_unit(rsw, vo0, OFF_X1 + rt * FM_RT, 20, rsw, wfl + 12 * GRP_B, srd);
    STAMP(13);
    float mean, var;
    read_stats(rsw, lane16, OFF_ST2 + rt * 1024, mean, var);
    const GeluK gk = gelu_consts(mean, __builtin_amdgcn_rsqf(var + LN_EPS), __builtin_amdgcn_sqrtf(var + LN_EPS));
    ep_gelu<0, 2, 228>(gk, cs[0], cs[1]); st_frag<228>(srd, lane16, gb);
    ep_gelu<2, 2, 236>(gk, cs[2], cs[3]); st_frag<236>(srd, lane16, gb + 1024);
    ep_gelu<4, 1, 250>(gk, cs[4], cs[4]);
#pragma unroll
    for (int t = 0; t < 5; ++t) cs[t] = *(lcs)(lds + L_CS3 + (5 + t) * 64 + q * 16);
    STAMP(14);
    run_unit<16, 4, 20, 0>(wb, srd, 0, 0);
    ep_gelu<0, 1, 252>(gk, cs[0], cs[0]); st_frag<250>(srd, lane16, gb + 2048);
    ep_gelu<1, 2, 232>(gk, cs[1], cs[2]); st_frag<232>(srd, lane16, gb + 3072);
    ep_gelu<3, 2, 240>(gk, cs[3], cs[4]); st_frag<240>(srd, lane16, gb + 4096);
#pragma unroll
    for (int t = 0; t < 4; ++t) cs[t] = *(lcs)(lds + L_CS3 + (10 + t) * 64 + q * 16);
    run_unit<20, 4, 20, 0>(wb, srd, 0, 0);
    ep_gelu<0, 2, 228>(gk, cs[0], cs[1]); st_frag<228>(srd, lane16, gb + 5120);
    if ((j & 3) != 3) { ep_gelu<2, 2, 236>(gk, cs[2], cs[3]); st_frag<236>(srd, lane16, gb + 6144); }
    arrive<0xffull>(srd, cgrp + 196);
  }
  STAMP(15);
  await<1ull>(srd, cown + 196, 8);
  STAMP(16);
  {
    const unsigned gs = OFF_G + rt * G_RT;
    ld_unit(srd, vo0, gs);
    if (__builtin_expect(run_unit<24, 11, 54, 3>(wb, srd, lane16, gs + 20 * 1024) != 0, 0)) redo_unit(rsw, vo0, gs, 54, rsw, wfl + 24 * GRP_B, srd);
    STAMP(17);
    asm volatile(".set i_, 0\n\t.rept 10\n\tv_pk_add_f32 v[228+2*i_:229+2*i_], v[96+2*i_:97+2*i_], v[228+2*i_:229+2*i_]\n\t.set i_, i_+1\n\t.endr\n\t"
                 EP_PACK(116, 228) EP_PACK(118, 232) EP_PACK(120, 236) EP_PACK(122, 240) EP_PACK(124, 244)
                 "global_store_dwordx4 %0, v[116:119], %2\n\tglobal_store_dwordx4 %0, v[120:123], %2 offset:64\n\tglobal_store_dwordx2 %1, v[124:125], %2\n\ts_waitcnt lgkmcnt(0)"
                 ::"v"(rowoff + colp), "v"(rowoff + cols), "s"(out) : "memory");
  }
  STAMP(18);
  STAMP_OUT(1);
}

extern "C" void kernel_launch(void* const* d_in, const int* in_sizes, int n_in, void* d_out, int out_size, void* d_ws, size_t ws_size, hipStream_t stream) {
  (void)in_sizes; (void)n_in; (void)out_size; (void)ws_size;
  (void)hipFuncSetAttribute((const void*)k_qkv, hipFuncAttributeMaxDynamicSharedMemorySize, K1_LDS);
  (void)hipFuncSetAttribute((const void*)k_main, hipFuncAttributeMaxDynamicSharedMemorySize, K2_LDS + 512);
  k_qkv<<<256, 768, K1_LDS, stream>>>((const char*)d_in[0], (const char*)d_in[1], (char*)d_ws);
  k_main<<<256, 512, K2_LDS + 512, stream>>>((const char*)d_in[0], (const char*)d_in[2], (const char*)d_in[4], (const char*)d_in[5], (const char*)d_in[6],
                                       (const char*)d_in[7], (char*)d_ws, (char*)d_out);
}
```

```cpp
#include <hip/hip_runtime.h>
#include <hip/hip_bf16.h>

typedef __attribute__((ext_vector_type(8))) short bf16x8;
typedef __attribute__((ext_vector_type(4))) short s16x4;
typedef __attribute__((ext_vector_type(2))) __bf16 bf16x2;
typedef __attribute__((ext_vector_type(2))) float f32x2;
typedef __attribute__((ext_vector_type(4))) float f32x4;
typedef __attribute__((ext_vector_type(16))) float f32x16;
typedef __attribute__((ext_vector_type(4))) unsigned u32x4;
typedef __attribute__((ext_vector_type(2))) unsigned u32x2;
typedef __attribute__((address_space(3))) const char* lds_cptr;
typedef __attribute__((address_space(3))) char* lds_ptr;
#define DI __device__ __forceinline__
#define AI __attribute__((always_inline))
#define SBAR() __builtin_amdgcn_sched_barrier(0)
#define PIN(x) asm volatile("" : "+v"(x))
#define MFMA16(a, b, c) __builtin_amdgcn_mfma_f32_16x16x32_bf16((a), (b), (c), 0, 0, 0)
#define MFMA32(a, b, c) __builtin_amdgcn_mfma_f32_32x32x16_bf16((a), (b), (c), 0, 0, 0)

constexpr int S_ = 2048, D_ = 640, F_ = 1728;
constexpr float LN_EPS = 1e-5f;
constexpr float QSCALE = 0.22360679774997896f * 1.4426950408889634f;
constexpr unsigned SENT = 0xFFFFFFFFu;

constexpr unsigned QF_TILE = 1280, KV_REC = 2688, FM_RT = 20 * 1024, G_RT = 54 * 1024;
constexpr unsigned SZ_FM = 128 * FM_RT;
constexpr unsigned OFF_QF = 0;
constexpr unsigned OFF_KV = OFF_QF + 32 * 64 * QF_TILE;
constexpr unsigned KV_BLK = 40960 + 40960 + 128;
constexpr unsigned OFF_O = OFF_KV + 64 * KV_BLK;
constexpr unsigned OFF_H = OFF_O + SZ_FM;
constexpr unsigned OFF_H1 = OFF_H + SZ_FM;
constexpr unsigned OFF_X1 = OFF_H1 + SZ_FM;
constexpr unsigned OFF_G = OFF_X1 + SZ_FM;
constexpr unsigned OFF_ST1 = OFF_G + 128 * G_RT;
constexpr unsigned OFF_ST2 = OFF_ST1 + 128 * 1024;
constexpr int NGRP = 35;
constexpr unsigned GRP_B = 25 * 1024;
constexpr unsigned OFF_WF = OFF_ST2 + 128 * 1024;
constexpr unsigned OFF_CS1 = OFF_WF + 8 * NGRP * GRP_B;
constexpr unsigned OFF_CS3 = OFF_CS1 + 20 * 640 * 4;
constexpr unsigned OFF_FLAG = OFF_CS3 + 20 * 1792 * 4;
constexpr unsigned OFF_CNT = OFF_FLAG + 1024;
constexpr unsigned OFF_QC = OFF_CNT + 1024 * 256;
constexpr unsigned OFF_RDY = OFF_QC + 256 * 256;
constexpr unsigned OFF_AXF = OFF_RDY + 1024;
constexpr unsigned OFF_XST = OFF_AXF + 16 * 128;
constexpr unsigned OFF_EPO = OFF_XST + 16 * 1024;
constexpr unsigned WS_END = OFF_EPO + 256;

#define STAMP_DECL
#define STAMP_ARG
#define STAMP_PAR
#define STAMP(i)
#define STAMPF(i)
#define STAMP_OUT(k)
DI unsigned pack2(float lo, float hi) { f32x2 v = {lo, hi}; return __builtin_bit_cast(unsigned, __builtin_convertvector(v, bf16x2)); }
DI float bflo(unsigned u) { return __uint_as_float(u << 16); }
DI float bfhi(unsigned u) { return __uint_as_float(u & 0xffff0000u); }
DI float dot2(unsigned a, unsigned b, float c) { return __builtin_amdgcn_fdot2_f32_bf16(__builtin_bit_cast(bf16x2, a), __builtin_bit_cast(bf16x2, b), c, false); }
DI float sum_q(float x) {
  auto s = __builtin_amdgcn_permlane16_swap(__float_as_uint(x), __float_as_uint(x), false, false);
  x = __uint_as_float(s[0]) + __uint_as_float(s[1]);
  auto t = __builtin_amdgcn_permlane32_swap(__float_as_uint(x), __float_as_uint(x), false, false);
  return __uint_as_float(t[0]) + __uint_as_float(t[1]);
}
DI float sum8(float x) {
  x += __builtin_bit_cast(float, __builtin_amdgcn_update_dpp(0, __builtin_bit_cast(int, x), 0xB1, 0xf, 0xf, true));
  x += __builtin_bit_cast(float, __builtin_amdgcn_update_dpp(0, __builtin_bit_cast(int, x), 0x4E, 0xf, 0xf, true));
  x += __builtin_bit_cast(float, __builtin_amdgcn_update_dpp(0, __builtin_bit_cast(int, x), 0x141, 0xf, 0xf, true));
  return x;
}
template <int N> struct IC { static constexpr int value = N; };
template <int N, int I = 0, class Fn> DI void static_for(Fn&& f) { if constexpr (I < N) { f(IC<I>{}); static_for<N, I + 1>(f); } }
DI int swap23(int r) { return (r & ~12) | ((r & 4) << 1) | ((r & 8) >> 1); }
DI bf16x8 lds16(lds_cptr p) { return *(const __attribute__((address_space(3))) bf16x8*)p; }
typedef short v4i16_t __attribute__((ext_vector_type(4)));
DI s16x4 ldstr(lds_cptr p) { return __builtin_bit_cast(s16x4, __builtin_amdgcn_ds_read_tr16_b64_v4i16((__attribute__((address_space(3))) v4i16_t*)p)); }

#define OWN_ALL_VGPRS() asm volatile("" ::: "v255")
template <int R> DI float vget() { float r; asm volatile("v_mov_b32 %0, v[%c1]" : "=v"(r) : "n"(R)); return r; }
template <int R0, int R1> DI float vadd() { float r; asm volatile("v_add_f32 %0, v[%c1], v[%c2]" : "=v"(r) : "n"(R0), "n"(R1)); return r; }
template <int R> DI f32x4 vget4() { return f32x4{vget<R>(), vget<R + 1>(), vget<R + 2>(), vget<R + 3>()}; }
template <int R> DI void vset(unsigned v) { asm volatile("v_mov_b32 v[%c1], %0" ::"v"(v), "n"(R)); }
template <int R, int OFF> DI void gload4(const char* sbase, unsigned voff) {
  asm volatile("global_load_dwordx4 v[%c2:%c2+3], %0, %1 offset:%c3" ::"v"(voff), "s"(sbase), "n"(R), "n"(OFF) : "memory");
}
template <int R> DI f32x4 acc_sub(float m, f32x4 c) {
  float a0, a1, a2, a3;
  asm volatile("v_fma_f32 %0, -%4, %5, v[%c9]\n\tv_fma_f32 %1, -%4, %6, v[%c9+1]\n\tv_fma_f32 %2, -%4, %7, v[%c9+2]\n\tv_fma_f32 %3, -%4, %8, v[%c9+3]"
               : "=&v"(a0), "=&v"(a1), "=&v"(a2), "=&v"(a3) : "v"(m), "v"(c[0]), "v"(c[1]), "v"(c[2]), "v"(c[3]), "n"(R));
  return f32x4{a0, a1, a2, a3};
}
constexpr int AUX_SC1 = 16;
DI __amdgpu_buffer_rsrc_t mk_rsrc(const char* p, unsigned bytes) { return __builtin_amdgcn_make_buffer_rsrc((void*)p, 0, bytes, 0x00020000); }
DI void st_sc1(u32x4 v, __amdgpu_buffer_rsrc_t rs, unsigned voff, unsigned soff) { __builtin_amdgcn_raw_buffer_store_b128(v, rs, voff, soff, AUX_SC1); }
DI void st_sc1(u32x2 v, __amdgpu_buffer_rsrc_t rs, unsigned voff, unsigned soff) { __builtin_amdgcn_raw_buffer_store_b64(v, rs, voff, soff, AUX_SC1); }

DI u32x4 mk_srd(const char* p, unsigned bytes) { const unsigned long long v = (unsigned long long)p; return u32x4{(unsigned)v, (unsigned)(v >> 32) & 0xffffu, bytes, 0x00020000u}; }
DI void bdma_sc1(u32x4 srd, unsigned voff, unsigned soff, unsigned ldsdst) {
  asm volatile("s_mov_b32 m0, %3\n\ts_nop 0\n\tbuffer_load_dwordx4 %0, %1, %2 offen sc1 lds" ::"v"(voff), "s"(srd), "s"(soff), "s"(ldsdst) : "memory");
}
DI void bdma4_sc1(u32x4 srd, unsigned voff, unsigned soff, unsigned ldsdst) {
  asm volatile("s_mov_b32 m0, %3\n\ts_nop 0\n\tbuffer_load_dwordx4 %0, %1, %2 offen sc1 lds\n\tbuffer_load_dwordx4 %0, %1, %2 offen offset:1024 sc1 lds\n\t"
               "buffer_load_dwordx4 %0, %1, %2 offen offset:2048 sc1 lds\n\tbuffer_load_dwordx4 %0, %1, %2 offen offset:3072 sc1 lds" ::"v"(voff), "s"(srd), "s"(soff), "s"(ldsdst) : "memory");
}
DI void bdma(u32x4 srd, unsigned voff, unsigned soff, unsigned ldsdst) {
  asm volatile("s_mov_b32 m0, %3\n\ts_nop 0\n\tbuffer_load_dwordx4 %0, %1, %2 offen lds" ::"v"(voff), "s"(srd), "s"(soff), "s"(ldsdst) : "memory");
}

#define P1_RD(n, NXT, S2N, OFF)                                                                                                    \
  ".if " #n " < 4\n\tds_read_b64_tr_b16 v[" #NXT "+16+4*" #n ":" #NXT "+17+4*" #n "], v[246+" #n "] offset:" #OFF "+8192*" #S2N "\n\t"    \
  "ds_read_b64_tr_b16 v[" #NXT "+18+4*" #n ":" #NXT "+19+4*" #n "], v[246+" #n "] offset:" #OFF "+8192*" #S2N "+1024\n\t"                \
  ".elseif " #n " < 8\n\tds_read_b128 v[" #NXT "+4*(" #n "-4):" #NXT "+4*(" #n "-4)+3], v[244+" #S2N "] offset:" #OFF "+4096*((" #n "-4)/2)+512*(" #n "&1)\n\t.endif\n\t"
#define P1_STEP(SET, NXT, S2N, OFF, S2)                                                                                            \
  ".set n_, 0\n\t.rept 16\n\t.set m_, n_/4\n\t.set e_, n_&3\n\t"                                                                    \
  ".if %c[tf]\n\tv_mfma_f32_16x16x32_bf16 v[96+4*n_:99+4*n_], v[" #SET "+16+4*e_:" #SET "+19+4*e_], v[" #SET "+4*m_:" #SET "+3+4*m_], v[96+4*n_:99+4*n_]\n\t" \
  ".else\n\tv_mfma_f32_16x16x32_bf16 v[96+4*n_:99+4*n_], v[" #SET "+4*m_:" #SET "+3+4*m_], v[" #SET "+16+4*e_:" #SET "+19+4*e_], v[96+4*n_:99+4*n_]\n\t.endif\n\t" \
  P1_RD(n_, NXT, S2N, OFF)                                                                                                         \
  ".set n_, n_+1\n\t.endr\n\t"                                                                                                     \
  "s_cmp_lg_u32 %[wm], " #S2 "\n\ts_cbranch_scc1 1f\n\t.set e_, 0\n\t.rept 4\n\t"                                                   \
  ".if %c[tf]\n\tv_mfma_f32_16x16x32_bf16 v[160+4*e_:163+4*e_], v[" #SET "+16+4*e_:" #SET "+19+4*e_], v[240:243], v[160+4*e_:163+4*e_]\n\t" \
  ".else\n\tv_mfma_f32_16x16x32_bf16 v[160+4*e_:163+4*e_], v[240:243], v[" #SET "+16+4*e_:" #SET "+19+4*e_], v[160+4*e_:163+4*e_]\n\t.endif\n\t" \
  ".set e_, e_+1\n\t.endr\n\t1:\n\t"
template <bool TF> DI void p1_periods(int wm, unsigned n2) {
  asm volatile(
      "2:\n\t"
      P1_STEP(176, 208, 1, 0, 0) "s_waitcnt lgkmcnt(0)\n\ts_barrier\n\t"
      P1_STEP(208, 176, 0, 32768, 1) "s_waitcnt lgkmcnt(0)\n\t"
      P1_STEP(176, 208, 1, 32768, 0) "s_waitcnt lgkmcnt(0)\n\ts_barrier\n\t"
      ".set i_, 244\n\t.rept 6\n\tv_xor_b32 v[i_], 0x10000, v[i_]\n\t.set i_, i_+1\n\t.endr\n\t"
      P1_STEP(208, 176, 0, 0, 1)
      "s_sub_u32 %[cnt], %[cnt], 1\n\ts_cmp_lg_u32 %[cnt], 0\n\ts_waitcnt lgkmcnt(0)\n\ts_cbranch_scc1 2b\n\t"
      "s_nop 7"
      : [cnt] "+s"(n2) : [wm] "s"(wm), [tf] "n"(TF ? 1 : 0) : "memory", "scc");
}
typedef __attribute__((address_space(3))) f32x4 lf32x4;
typedef __attribute__((address_space(3))) f32x2 lf32x2;

template <bool TF>
DI void p1_compute(u32x4 srd, __amdgpu_buffer_rsrc_t rsw, lds_ptr lds, int rb, int g, int w, int lane STAMP_PAR) {
  const int q = lane >> 4, i16 = lane & 15, wm = w >> 1, wn = w & 1;
  {
    const unsigned xa = 16384 + (64 * wm + 8 * (i16 >> 2) + (i16 & 3)) * 128 + ((q ^ (i16 >> 1)) * 16);
    const int p4 = lane & 3, qq = (lane >> 2) & 3;
    const unsigned wa = (8 * q + qq) * 256 + (((8 * wn + p4) ^ (2 * (qq + 4 * (q & 1)))) * 16) + 8 * (p4 >> 1);
    vset<244>(xa); vset<245>(xa ^ 64); vset<246>(wa); vset<247>(wa ^ 8); vset<248>(wa ^ 64); vset<249>(wa ^ 72);
  }
  asm volatile(".set i_, 96\n\t.rept 80\n\tv_mov_b32 v[i_], 0\n\t.set i_, i_+1\n\t.endr\n\t"
               "v_mov_b32 v240, 0x3f803f80\n\tv_mov_b32 v241, v240\n\tv_mov_b32 v242, v240\n\tv_mov_b32 v243, v240\n\t"
               "s_waitcnt lgkmcnt(0)\n\ts_barrier\n\t.set n_, 0\n\t.rept 8\n\t" P1_RD(n_, 176, 0, 0) ".set n_, n_+1\n\t.endr\n\ts_waitcnt lgkmcnt(0)" ::: "memory");
  STAMP(41);
  p1_periods<TF>(wm, 3);
  if (w == 0) asm volatile("buffer_load_dwordx4 v[250:253], %0, %1, %2 offen sc1" ::"v"(lane * 16), "s"(srd), "s"(OFF_RDY) : "memory");
  p1_periods<TF>(wm, 2);
  STAMP(42);
  f32x4 c[4];
  static_for<4>([&](auto E_) AI { c[E_.value] = vget4<160 + 4 * E_.value>(); *(lf32x4*)(lds + w * 4096 + E_.value * 1024 + lane * 16) = c[E_.value]; });
  asm volatile("s_waitcnt lgkmcnt(0)\n\ts_barrier" ::: "memory");
  STAMP(46);
#pragma unroll
  for (int e = 0; e < 4; ++e) c[e] += *(const lf32x4*)(lds + (w ^ 2) * 4096 + e * 1024 + lane * 16);
  const int kt0 = 4 * rb + 2 * wm;
  if (TF) {
    const bool isq = g < 5;
    const unsigned rec = isq ? QF_TILE : KV_BLK, hst = isq ? 64 * QF_TILE : 1280;
    const int r0 = 8 * (i16 >> 2) + (i16 & 3);
    f32x2 st[4];
#pragma unroll
    for (int mi = 0; mi < 4; ++mi) { st[mi] = *(const lf32x2*)(lds + 16384 + (4 * wm + mi) * 128 + i16 * 8); if (isq) st[mi][1] *= QSCALE; }
    static_for<2>([&](auto P_) AI {
      constexpr int P = P_.value;
      const int cq = 16 * (isq ? g : g - 5) + 8 * wn + 4 * P + q, i5 = (cq * 205) >> 10, m5 = cq - 5 * i5;
      const int hd = 2 * i5 + (m5 > 2);
      const unsigned base = (isq ? OFF_QF : OFF_KV) + hd * hst + kt0 * rec;
      const unsigned oct = base + (m5 > 2 ? m5 - 3 : m5) * 512, tail = base + 1024 + (8 * (i16 >> 2) + 2 * (i16 & 3)) * 8;
      const unsigned ad0 = m5 == 2 ? tail : oct + (isq ? r0 : swap23(r0)) * 16, ad1 = m5 == 2 ? tail + hst : oct + (isq ? r0 + 4 : swap23(r0 + 4)) * 16;
      static_for<2>([&](auto A_) AI {
        constexpr int a = A_.value;
        u32x4 o[2];
        static_for<2>([&](auto B_) AI {
          constexpr int mi = 2 * a + B_.value;
          const f32x4 a0 = acc_sub<96 + 16 * mi + 8 * P>(st[mi][0], c[2 * P]) * st[mi][1], a1 = acc_sub<100 + 16 * mi + 8 * P>(st[mi][0], c[2 * P + 1]) * st[mi][1];
          const unsigned p0 = pack2(a0[0], a0[1]), p1 = pack2(a0[2], a0[3]), p2 = pack2(a1[0], a1[1]), p3 = pack2(a1[2], a1[3]);
          o[B_.value] = q < 2 ? u32x4{p0, p1, p2, p3} : u32x4{p2, p3, p0, p1};
        });
        const bool tl = m5 == 2;
        st_sc1(tl ? u32x4{o[0][0], o[0][1], o[1][0], o[1][1]} : o[0], rsw, ad0, a * rec);
        st_sc1(tl ? u32x4{o[0][2], o[0][3], o[1][2], o[1][3]} : o[1], rsw, ad1, a * rec);
      });
    });
  } else {
    const int p4 = i16 >> 2, cl = 64 * wn + 8 * p4 + 4 * (p4 >> 1) + (i16 & 3), cd = p4 < 2 ? 4 : -4;
    static_for<2>([&](auto A_) AI {
      constexpr int a = A_.value;
      lds_cptr sp = (lds_cptr)lds + 16384 + (4 * wm + 2 * a) * 128 + q * 32;
      const f32x4 s0 = *(const lf32x4*)(sp), s1 = *(const lf32x4*)(sp + 16), s2 = *(const lf32x4*)(sp + 128), s3 = *(const lf32x4*)(sp + 144);
      const f32x4 mA = {s0[0], s0[2], s1[0], s1[2]}, rA = {s0[1], s0[3], s1[1], s1[3]}, mB = {s2[0], s2[2], s3[0], s3[2]}, rB = {s2[1], s2[3], s3[1], s3[3]};
      static_for<4>([&](auto E_) AI {
        constexpr int e = E_.value;
        const float cs = c[e][0];
        const f32x4 a0 = acc_sub<96 + 32 * a + 4 * e>(cs, mA) * rA, a1 = acc_sub<112 + 32 * a + 4 * e>(cs, mB) * rB;
        st_sc1(u32x4{pack2(a0[0], a0[1]), pack2(a0[2], a0[3]), pack2(a1[0], a1[1]), pack2(a1[2], a1[3])}, rsw, q * 10240 + (cl + 32 * (e >> 1) + ((e & 1) ? cd : 0)) * 16,
               OFF_KV + ((kt0 & 32) | ((kt0 + a + 1) & 31)) * KV_BLK + 40960 + (g - 10) * 2048);
      });
    });
  }
}

#define P1_DEPTH 4
struct P1Load { u32x4 srd; unsigned vo[4], pstep, hstep, dst; int rot; };
DI void p1_issue(const P1Load& L, int p) {
  int pp = p + L.rot; pp -= pp >= 10 ? 10 : 0;
  const unsigned so = pp * L.pstep, d = (p & 3) * 32768 + L.dst;
#pragma unroll
  for (int i = 0; i < 8; ++i) bdma(L.srd, L.vo[i & 3], so + (i >> 2) * L.hstep, d + i * 1024);
}
DI P1Load p1_start(const char* x, const char* wqkv, int rb, int g, int rot, int lw, int lane) {
  const int q = lane >> 4, i16 = lane & 15, isx = lw >> 1, hf = lw & 1;
  P1Load L;
  L.srd = isx ? mk_srd(x, S_ * D_ * 2) : mk_srd(wqkv, D_ * 3 * D_ * 2);
#pragma unroll
  for (int i = 0; i < 4; ++i)
    L.vo[i] = isx ? (unsigned)((128 * rb + 64 * hf + 8 * i + (lane >> 3)) * 1280 + 16 * ((lane & 7) ^ (2 * i + ((lane >> 4) & 1))))
                  : (unsigned)((32 * hf + 4 * i + q) * 3840 + 256 * g + 16 * (i16 ^ (2 * (q + 4 * (i >> 1)))));
  L.pstep = isx ? 128 : 64 * 3840; L.hstep = isx ? 32 * 1280 : 16 * 3840; L.dst = lw * 8192; L.rot = rot;
  p1_issue(L, 0); p1_issue(L, 1);
  if (P1_DEPTH == 4) p1_issue(L, 2);
  return L;
}
template <class Fn> DI void p1_stats(const char* x, __amdgpu_buffer_rsrc_t rsw, int rb, int w, int lane, Fn landed) {
  const char* p = x + (size_t)(128 * rb + 16 * w + (lane >> 3)) * 1280 + (lane & 7) * 16;
  u32x4 v[2][10];
#pragma unroll
  for (int h = 0; h < 2; ++h)
#pragma unroll
    for (int k = 0; k < 10; ++k) v[h][k] = *(const u32x4*)(p + h * 8 * 1280 + k * 128);
  asm volatile("s_waitcnt vmcnt(20)" ::: "memory");
  landed();
#pragma unroll
  for (int h = 0; h < 2; ++h) {
    f32x4 sm4 = {0.f, 0.f, 0.f, 0.f}, sq4 = sm4;
#pragma unroll
    for (int k = 0; k < 10; ++k)
#pragma unroll
      for (int d = 0; d < 4; ++d) { sm4[d] = dot2(v[h][k][d], 0x3f803f80u, sm4[d]); sq4[d] = dot2(v[h][k][d], v[h][k][d], sq4[d]); }
    float sm = (sm4[0] + sm4[1]) + (sm4[2] + sm4[3]), sq = (sq4[0] + sq4[1]) + (sq4[2] + sq4[3]);
    sm = sum8(sm); sq = sum8(sq);
    const float mean = sm * (1.f / D_);
    const float rstd = __builtin_amdgcn_rsqf(fmaxf(sq * (1.f / D_) - mean * mean, 0.f) + LN_EPS);
    const int t = 16 * w + 8 * h + (lane >> 3);
    st_sc1(u32x2{__float_as_uint(mean), __float_as_uint(rstd)}, rsw, (lane & 7) == 0 ? (2 * (t >> 5) + ((t >> 2) & 1)) * 128 + (4 * ((t >> 3) & 3) + (t & 3)) * 8 : 0x80000000u, OFF_XST + rb * 1024);
  }
  st_sc1(u32x4{0x3f803f80u, 0x3f803f80u, 0x3f803f80u, 0x3f803f80u}, rsw, (lane == 0 && w < 4) ? 81920 : 0x80000000u, OFF_KV + (4 * rb + (w & 3)) * KV_BLK);
  asm volatile("s_waitcnt vmcnt(0) lgkmcnt(0)\n\ts_barrier" ::: "memory");
}

constexpr int KV_HALF = 11 * 1024, KV_SLOT = 2 * KV_HALF;
constexpr int L_KV0 = 4 * GRP_B, L_WRAP = 144 * 1024, L_MISC = 6 * GRP_B, L_CS1 = L_MISC, L_CS3 = L_CS1 + 320, L_FILL = L_CS3 + 960, L_CSF = L_FILL + 8, L_EPO = L_FILL + 16,
              L_CONS = L_FILL + 64, L_SCR2 = L_CONS + 1024, K2_LDS = L_SCR2 + 6144;
constexpr int kv_at(int c) { return (c < 2 || c > 5) ? L_KV0 + (c & 1) * KV_SLOT : (c - 2) * KV_SLOT; }
#define PRIO_C 3
#define PRIO_L 0
constexpr unsigned SPIN_CAP = 20000, LDS_SPIN = 0x20000;

DI u32x4 ld_sc1(__amdgpu_buffer_rsrc_t rs, unsigned voff, unsigned soff) { return __builtin_amdgcn_raw_buffer_load_b128(rs, voff, soff, AUX_SC1); }
DI unsigned nosent(unsigned u) { return min(u, SENT - 1u); }
DI unsigned umax3(unsigned a, unsigned b, unsigned c) { return max(max(a, b), c); }
DI int tail_slot(int tokn) { return (tokn & 24) | ((tokn & 3) << 1) | ((tokn >> 2) & 1); }
DI unsigned kv_src(int n, int head) {
  const int pl = ((n - 80) * 745) >> 14, row = n - 80 - 22 * pl;
  return n < 80 ? head * 1280 + n * 16 : row < 20 ? 40960 + pl * 10240 + (20 * head + (row < 16 ? swap23(row) : row)) * 16 : 81920;
}
DI unsigned kv_cell(int n, int half, int head) { const int rec = (n * 1561) >> 18; return (32 * half + rec) * KV_BLK + kv_src(n - 168 * rec, head); }
DI f32x16 attn_safe(__amdgpu_buffer_rsrc_t rs, unsigned qp, int head, int lane) {
  const int hi = lane >> 5, r32 = lane & 31;
  const bf16x8 q1 = __builtin_bit_cast(bf16x8, ld_sc1(rs, hi * 512 + r32 * 16, qp));
  const u32x2 q2l = __builtin_amdgcn_raw_buffer_load_b64(rs, 1024 + tail_slot(r32) * 8, qp, AUX_SC1);
  const bf16x8 q2 = __builtin_bit_cast(bf16x8, u32x4{hi ? 0u : q2l[0], hi ? 0u : q2l[1], 0u, 0u});
  f32x16 O = {};
  float m = -3.0e38f;
#pragma unroll 1
  for (int t = 0; t < 64; ++t) {
    const unsigned kb = OFF_KV + t * KV_BLK + head * 1280, vb = OFF_KV + ((t & 32) | ((t + 1) & 31)) * KV_BLK;
    const bf16x8 k1 = __builtin_bit_cast(bf16x8, ld_sc1(rs, hi * 512 + r32 * 16, kb));
    const u32x2 k2l = __builtin_amdgcn_raw_buffer_load_b64(rs, 1024 + tail_slot(swap23(r32)) * 8, kb, AUX_SC1);
    const bf16x8 v0 = __builtin_bit_cast(bf16x8, ld_sc1(rs, kv_src(80 + 22 * hi + min(r32, 21), head), vb)),
                 v1 = __builtin_bit_cast(bf16x8, ld_sc1(rs, kv_src(124 + 22 * hi + min(r32, 21), head), vb));
    f32x16 sc = MFMA32(k1, q1, f32x16{});
    sc = MFMA32(__builtin_bit_cast(bf16x8, u32x4{k2l[0], k2l[1], 0u, 0u}), q2, sc);
    float tm = sc[0];
#pragma unroll
    for (int i = 1; i < 16; ++i) tm = fmaxf(tm, sc[i]);
    { auto rr = __builtin_amdgcn_permlane32_swap(__float_as_uint(tm), __float_as_uint(tm), false, false); tm = fmaxf(__uint_as_float(rr[0]), __uint_as_float(rr[1])); }
    const float mn = fmaxf(m, tm), al = __builtin_amdgcn_exp2f(m - mn);
    m = mn;
    u32x4 p0, p1;
#pragma unroll
    for (int i = 0; i < 4; ++i) {
      p0[i] = pack2(__builtin_amdgcn_exp2f(sc[2 * i] - mn), __builtin_amdgcn_exp2f(sc[2 * i + 1] - mn));
      p1[i] = pack2(__builtin_amdgcn_exp2f(sc[8 + 2 * i] - mn), __builtin_amdgcn_exp2f(sc[9 + 2 * i] - mn));
    }
#pragma unroll
    for (int i = 0; i < 16; ++i) O[i] *= al;
    O = MFMA32(v0, __builtin_bit_cast(bf16x8, p0), O);
    O = MFMA32(v1, __builtin_bit_cast(bf16x8, p1), O);
  }
  return O;
}

constexpr int R_A = 128;
template <int A0, int N> DI void ld_frags(u32x4 srd, unsigned voff, unsigned soff) {
  unsigned t;
  asm volatile("s_add_u32 %0, %3, 4096\n\t.set i_, 0\n\t.rept %c5\n\t"
               ".if i_ < 4\n\tbuffer_load_dwordx4 v[%c4+4*i_:%c4+4*i_+3], %1, %2, %3 offen offset:1024*i_ sc1\n\t"
               ".else\n\tbuffer_load_dwordx4 v[%c4+4*i_:%c4+4*i_+3], %1, %2, %0 offen sc1\n\t.endif\n\t.set i_, i_+1\n\t.endr"
               : "=&s"(t) : "v"(voff), "s"(srd), "s"(soff), "n"(R_A + 4 * A0), "n"(N) : "memory", "scc");
}
DI void ld_unit(u32x4 srd, const unsigned (&vo)[4], unsigned soff) {
  ld_frags<0, 5>(srd, vo[0], soff); ld_frags<5, 5>(srd, vo[1], soff + 5120); ld_frags<10, 5>(srd, vo[2], soff + 10240); ld_frags<15, 5>(srd, vo[3], soff + 15360);
}
constexpr unsigned long long vmw(int a, int b = 63, int c = 63, int d = 63, int e = 63, int f = 63) {
  return (unsigned long long)a | (unsigned long long)b << 6 | (unsigned long long)c << 12 | (unsigned long long)d << 18 | (unsigned long long)e << 24 | (unsigned long long)f << 30;
}
template <int G0, int NG, int KS0, int KSE, unsigned long long VMW, int PERK, int NRL, int XG, int XOP, int AVM = -1>
DI unsigned long long run_unit(const unsigned (&wb)[5], u32x4 srd, unsigned voff, unsigned soff, unsigned xoff, unsigned aoff STAMP_PAR) {
  unsigned long long m; unsigned t, u, so;
#define DBG_INC
#define DBG_OP
  asm volatile(
      "s_mov_b32 %[so], %[soff]\n\t"
      ".set gi_, 0\n\t.rept %c[ng]\n\t"
      ".set g_, %c[g0]+gi_\n\t.set gn_, g_+1\n\t.if gn_ > 34\n\t.set gn_, 34\n\t.endif\n\t"
      ".set sc_, g_-6*(g_/6)\n\t.set sn_, gn_-6*(gn_/6)\n\t.set nk_, %c[kse]-%c[ks0]-5*gi_\n\t.if nk_ > 5\n\t.set nk_, 5\n\t.endif\n\t"
      "ds_read_b64 v[254:255], %[fl]\n\tv_mov_b32 v249, g_+1\n\t"
      ".set vw_, (%c[vmw]>>(6*gi_))&63\n\t.if vw_ < 63\n\ts_waitcnt vmcnt(vw_)\n\t.endif\n\t"
      ".set k_, 0\n\t.rept nk_\n\t"
      ".set kr_, 5*gi_+k_\n\t.set ks_, %c[ks0]+kr_\n\t.set a_, 128+4*(ks_-20*(ks_/20))\n\t"
      ".if %c[perk] && gi_ == 0\n\ts_waitcnt vmcnt(19-k_)\n\t.endif\n\t"
      ".if k_ == nk_-1\n\t"
      "s_cbranch_scc0 3f\n\t2:\n\t.subsection 1\n\t3:\n\ts_mov_b32 %[u], 0\n\t"
      "1:\n\t" DBG_INC "s_sleep 1\n\tds_read_b64 v[254:255], %[fl]\n\ts_add_u32 %[u], %[u], 1\n\ts_waitcnt lgkmcnt(0)\n\tv_min_u32 v254, v254, v255\n\ts_nop 0\n\tv_readfirstlane_b32 %[t], v254\n\t"
      "s_cmp_gt_u32 %[u], 0x20000\n\ts_cbranch_scc1 2b\n\ts_cmp_ge_u32 %[t], gn_+1\n\ts_cbranch_scc0 1b\n\ts_branch 2b\n\t.subsection 0\n\t"
      ".endif\n\t"
      ".set t_, 0\n\t.rept 5\n\t"
      ".set f_, 5*k_+t_\n\t"
      ".if f_ == 0 || f_ == 2\n\ts_waitcnt lgkmcnt(5)\n\t.elseif (f_ & 1) == 0\n\ts_waitcnt lgkmcnt(3)\n\t.endif\n\t"
      ".if ks_ == 0\n\tv_mfma_f32_16x16x32_bf16 v[228+4*t_:231+4*t_], v[208+4*t_:211+4*t_], v[a_:a_+3], 0\n\t"
      ".else\n\tv_mfma_f32_16x16x32_bf16 v[228+4*t_:231+4*t_], v[208+4*t_:211+4*t_], v[a_:a_+3], v[228+4*t_:231+4*t_]\n\t.endif\n\t"
      ".set s_, sc_\n\t.set o_, 5120*(k_+1)+1024*t_\n\t.if k_ == nk_-1\n\t.set s_, sn_\n\t.set o_, 1024*t_\n\t.endif\n\t"
      ".if s_ < 2\n\tds_read_b128 v[208+4*t_:211+4*t_], %[w0] offset:25600*s_+o_\n\t"
      ".elseif s_ < 4\n\tds_read_b128 v[208+4*t_:211+4*t_], %[w1] offset:25600*(s_-2)+o_\n\t"
      ".else\n\tds_read_b128 v[208+4*t_:211+4*t_], %[w2] offset:25600*(s_-4)+o_\n\t.endif\n\t"
      ".if k_ == nk_-2\n\t"
      ".if t_ == 0\n\tv_min_u32 v254, v254, v255\n\t.elseif t_ == 2\n\tv_readfirstlane_b32 %[t], v254\n\t.elseif t_ == 4\n\ts_cmp_ge_u32 %[t], gn_+1\n\t.endif\n\t"
      ".endif\n\t"
      ".set t_, t_+1\n\t.endr\n\t"
      ".if kr_ < %c[nrl]\n\t"
      "buffer_load_dwordx4 v[a_:a_+3], %[voff], %[srd], %[so] offen offset:1024*(kr_&3) sc1\n\t"
      ".if (kr_&3) == 3\n\ts_addk_i32 %[so], 4096\n\t.if k_ == nk_-2\n\ts_cmp_ge_u32 %[t], gn_+1\n\t.endif\n\t.endif\n\t"
      ".endif\n\t"
      ".set k_, k_+1\n\t.endr\n\t"
      "ds_write_b32 %[cs], v249\n\t"
      ".if gi_ == 0 && %c[avm] >= 0\n\ts_waitcnt vmcnt(%c[avm])\n\ts_mov_b64 %[m], exec\n\ts_mov_b64 exec, 0xff\n\tv_mov_b32 v125, 1\n\tbuffer_atomic_add v125, %[ao], %[srd], 0 offen sc1\n\ts_mov_b64 exec, %[m]\n\t.endif\n\t"
      ".if gi_ == %c[xg] && %c[xop] == 1\n\tbuffer_load_dwordx4 v[250:253], %[voff], %[srd], %[so] offen sc1\n\t.endif\n\t"
      ".if gi_ == %c[xg] && %c[xop] == 2\n\ts_mov_b64 %[m], exec\n\ts_mov_b64 exec, 1\n\tv_mov_b32 v124, 0\n\tbuffer_atomic_add v124, %[xo], %[srd], 0 offen sc0 sc1\n\ts_mov_b64 exec, %[m]\n\t.endif\n\t"
      ".set gi_, gi_+1\n\t.endr\n\t"
      ".if %c[xop] > 0\n\ts_waitcnt vmcnt(0)\n\t.endif\n\t"
      "s_nop 4\n\tv_cmp_u_f32 %[m], v228, v228"
      : [m] "=&s"(m), [t] "=&s"(t), [u] "=&s"(u), [so] "=&s"(so) DBG_OP
      : [w0] "v"(wb[0]), [w1] "v"(wb[1]), [w2] "v"(wb[2]), [fl] "v"(wb[3]), [cs] "v"(wb[4]), [voff] "v"(voff), [xo] "v"(xoff), [ao] "v"(aoff), [avm] "n"(AVM), [srd] "s"(srd), [soff] "s"(soff),
        [g0] "n"(G0), [ng] "n"(NG), [ks0] "n"(KS0), [kse] "n"(KSE), [vmw] "n"(VMW), [perk] "n"(PERK), [nrl] "n"(NRL), [xg] "n"(XG), [xop] "n"(XOP)
      : "memory", "scc");
  return m;
}
template <bool RELOAD> DI void redo_unit(__amdgpu_buffer_rsrc_t ra, const unsigned (&vo)[4], unsigned soff, int nks, __amdgpu_buffer_rsrc_t rw, unsigned woff, u32x4 srd_a) {
  f32x4 acc[5];
#pragma unroll
  for (int t = 0; t < 5; ++t) acc[t] = f32x4{0.f, 0.f, 0.f, 0.f};
  asm volatile("s_waitcnt vmcnt(0)" ::: "memory");
#pragma unroll 1
  for (int ks = 0; ks < nks; ++ks) {
    const unsigned va = ks < 5 ? vo[0] : ks < 10 ? vo[1] : ks < 15 ? vo[2] : vo[3];
    u32x4 av;
#pragma unroll 1
    for (unsigned sp = 0; sp < SPIN_CAP; ++sp) {
      av = ld_sc1(ra, va, soff + ks * 1024);
      if (!__any(umax3(max(av[0], av[1]), av[2], av[3]) == SENT)) break;
      asm volatile("s_sleep 8" ::: "memory");
    }
#pragma unroll
    for (int t = 0; t < 5; ++t) acc[t] = MFMA16(__builtin_bit_cast(bf16x8, ld_sc1(rw, woff, (ks * 5 + t) * 1024)), __builtin_bit_cast(bf16x8, av), acc[t]);
  }
  static_for<20>([&](auto I_) AI { vset<228 + I_.value>(__float_as_uint(acc[I_.value >> 2][I_.value & 3])); });
  if (RELOAD) { ld_unit(srd_a, vo, soff); asm volatile("s_waitcnt vmcnt(0)" ::: "memory"); }
}

#define EP_PACK(d, r) "v_cvt_pk_bf16_f32 v[" #d "], v[" #r "], v[" #r "+1]\n\tv_cvt_pk_bf16_f32 v[" #d "+1], v[" #r "+2], v[" #r "+3]\n\t"
#define EP_GUARD(d) "v_min_u32 v[" #d "], -2, v[" #d "]\n\tv_min_u32 v[" #d "+1], -2, v[" #d "+1]\n\t"
#define EP_GELU(R, NP)                                                                                                                       \
  ".set i_, 0\n\t.rept " #NP "\n\tv_pk_mul_f32 v[116+2*i_:117+2*i_], v[" #R "+2*i_:" #R "+1+2*i_], v[" #R "+2*i_:" #R "+1+2*i_]\n\t.set i_, i_+1\n\t.endr\n\t" \
  ".set i_, 0\n\t.rept " #NP "\n\tv_pk_fma_f32 v[116+2*i_:117+2*i_], v[116+2*i_:117+2*i_], %[B], %[A]\n\t.set i_, i_+1\n\t.endr\n\t"          \
  ".set i_, 0\n\t.rept " #NP "\n\tv_pk_mul_f32 v[116+2*i_:117+2*i_], v[116+2*i_:117+2*i_], v[" #R "+2*i_:" #R "+1+2*i_]\n\t.set i_, i_+1\n\t.endr\n\t" \
  ".set i_, 0\n\t.rept 2*" #NP "\n\tv_exp_f32 v[116+i_], v[116+i_]\n\t.set i_, i_+1\n\t.endr\n\t"                                           \
  ".set i_, 0\n\t.rept " #NP "\n\tv_pk_fma_f32 v[116+2*i_:117+2*i_], v[116+2*i_:117+2*i_], %[I], %[I]\n\t.set i_, i_+1\n\t.endr\n\t"          \
  ".set i_, 0\n\t.rept 2*" #NP "\n\tv_rcp_f32 v[116+i_], v[116+i_]\n\t.set i_, i_+1\n\t.endr\n\t"                                           \
  ".set i_, 0\n\t.rept " #NP "\n\tv_pk_mul_f32 v[" #R "+2*i_:" #R "+1+2*i_], v[" #R "+2*i_:" #R "+1+2*i_], v[116+2*i_:117+2*i_]\n\t.set i_, i_+1\n\t.endr\n\t"
struct GeluK { f32x2 nm, A, B, I; };
DI GeluK gelu_consts(float mean, float r, float ir) {
  const float c0 = -2.f * 1.4426950408889634f * 0.7978845608028654f, c1 = c0 * 0.044715f, A = c0 * r, B = c1 * r * r * r;
  return GeluK{f32x2{-mean, -mean}, f32x2{A, A}, f32x2{B, B}, f32x2{ir, ir}};
}
template <int T, int NT, int D> DI void ep_gelu(const GeluK& k, f32x4 c0, f32x4 c1) {
  asm volatile(
      "v_pk_fma_f32 v[%c[r]:%c[r]+1], %[nm], %[ca], v[%c[r]:%c[r]+1]\n\tv_pk_fma_f32 v[%c[r]+2:%c[r]+3], %[nm], %[cb], v[%c[r]+2:%c[r]+3]\n\t"
      ".if %c[nt] == 2\n\tv_pk_fma_f32 v[%c[r]+4:%c[r]+5], %[nm], %[cc], v[%c[r]+4:%c[r]+5]\n\tv_pk_fma_f32 v[%c[r]+6:%c[r]+7], %[nm], %[cd], v[%c[r]+6:%c[r]+7]\n\t.endif\n\t"
      EP_GELU(%c[r], 2*%c[nt])
      EP_PACK(%c[d], %c[r]) ".if %c[nt] == 2\n\t" EP_PACK(%c[d]+2, %c[r]+4) ".endif\n\t"
      EP_GUARD(%c[d]) ".if %c[nt] == 2\n\t" EP_GUARD(%c[d]+2) ".endif"
      ::[nm] "v"(k.nm), [A] "v"(k.A), [B] "v"(k.B), [I] "v"(k.I), [ca] "v"(f32x2{c0[0], c0[1]}), [cb] "v"(f32x2{c0[2], c0[3]}), [cc] "v"(f32x2{c1[0], c1[1]}),
        [cd] "v"(f32x2{c1[2], c1[3]}), [r] "n"(228 + 4 * T), [nt] "n"(NT), [d] "n"(D));
}
template <int R> DI void st_frag(u32x4 srd, unsigned voff, unsigned soff) { asm volatile("buffer_store_dwordx4 v[%c3:%c3+3], %0, %1, %2 offen sc1\n\ts_nop 1" ::"v"(voff), "s"(srd), "s"(soff), "n"(R) : "memory"); }
template <int R> DI void st_single(u32x4 srd, unsigned vsg, unsigned soff) {
  asm volatile("v_mov_b32 v[%c3+2], v[%c3]\n\tv_mov_b32 v[%c3+3], v[%c3+1]\n\tv_mov_b32 v248, v[%c3]\n\tv_mov_b32 v249, v[%c3+1]\n\ts_nop 1\n\t"
               "v_permlane16_swap_b32 v248, v[%c3+2]\n\tv_permlane16_swap_b32 v249, v[%c3+3]\n\t"
               "buffer_store_dwordx4 v[%c3:%c3+3], %0, %1, %2 offen sc1\n\ts_nop 1" ::"v"(vsg), "s"(srd), "s"(soff), "n"(R) : "memory");
}
template <int R> DI void st_tile(u32x4 srd, unsigned lane16, unsigned vsg, unsigned sp, unsigned ssg) {
  asm volatile(EP_PACK(116, %c0) EP_PACK(118, %c0+4) EP_GUARD(116) EP_GUARD(118) EP_PACK(120, %c0+8) EP_PACK(122, %c0+12) ::"n"(R));
  st_frag<116>(srd, lane16, sp);
  asm volatile(EP_GUARD(120) EP_GUARD(122) EP_PACK(250, %c0+16) ::"n"(R));
  st_frag<120>(srd, lane16, sp + 1024);
  asm volatile(EP_GUARD(250));
  st_single<250>(srd, vsg, ssg);
}
template <int R> DI void st_stats(__amdgpu_buffer_rsrc_t rs, unsigned vst, unsigned off) {
  float s, ss;
  asm volatile("v_pk_add_f32 v[116:117], v[%c2:%c2+1], v[%c2+2:%c2+3]\n\tv_pk_mul_f32 v[118:119], v[%c2:%c2+1], v[%c2:%c2+1]\n\tv_pk_mul_f32 v[120:121], v[%c2+2:%c2+3], v[%c2+2:%c2+3]\n\t"
               ".set i_, 2\n\t.rept 8\n\tv_pk_add_f32 v[116:117], v[116:117], v[%c2+2*i_:%c2+2*i_+1]\n\t"
               "v_pk_fma_f32 v[118+2*(i_&1):119+2*(i_&1)], v[%c2+2*i_:%c2+2*i_+1], v[%c2+2*i_:%c2+2*i_+1], v[118+2*(i_&1):119+2*(i_&1)]\n\t.set i_, i_+1\n\t.endr\n\t"
               "v_pk_add_f32 v[118:119], v[118:119], v[120:121]\n\tv_add_f32 %0, v116, v117\n\tv_add_f32 %1, v118, v119"
               : "=v"(s), "=v"(ss) : "n"(R));
  s = sum_q(s); ss = sum_q(ss);
  st_sc1(u32x2{nosent(__float_as_uint(s)), nosent(__float_as_uint(ss))}, rs, vst, off);
}
DI void read_stats(__amdgpu_buffer_rsrc_t rs, unsigned voff, unsigned soff, float& mean, float& var) {
  u32x4 st = __builtin_bit_cast(u32x4, vget4<250>());
  if (__builtin_expect(__any(umax3(max(st[0], st[1]), st[2], st[3]) == SENT), 0)) {
#pragma unroll 1
    for (unsigned sp = 0; sp < SPIN_CAP; ++sp) {
      asm volatile("s_sleep 8" ::: "memory");
      st = ld_sc1(rs, voff, soff);
      if (!__any(umax3(max(st[0], st[1]), st[2], st[3]) == SENT)) break;
    }
  }
  const float s = sum_q(__uint_as_float(st[0]) + __uint_as_float(st[2])), ss = sum_q(__uint_as_float(st[1]) + __uint_as_float(st[3]));
  mean = s * (1.f / D_);
  var = fmaxf(ss * (1.f / D_) - mean * mean, 0.f);
}
template <unsigned long long MASK> DI void arrive(u32x4 srd, unsigned voff) {
  unsigned long long sv;
  asm volatile("s_mov_b64 %0, exec\n\ts_mov_b64 exec, %3\n\tv_mov_b32 v249, 1\n\tbuffer_atomic_add v249, %1, %2, 0 offen sc1\n\ts_mov_b64 exec, %0"
               : "=&s"(sv) : "v"(voff), "s"(srd), "n"(MASK) : "memory");
}
template <unsigned long long MASK> DI void await(u32x4 srd, unsigned voff, unsigned expect) {
  unsigned long long sv; unsigned n;
  asm volatile("s_mov_b64 %0, exec\n\ts_mov_b64 exec, %5\n\ts_mov_b32 %1, 0\n\tv_mov_b32 v126, 0\n\tv_mov_b32 v127, 0\n\t"
               "buffer_atomic_add v126, %2, %3, 0 offen sc0 sc1\n\ts_sleep 3\n\t"
               "1:\n\tbuffer_atomic_add v127, %2, %3, 0 offen sc0 sc1\n\ts_waitcnt vmcnt(1)\n\tv_cmp_ne_u32 vcc, %4, v126\n\ts_cbranch_vccz 2f\n\t"
               "v_mov_b32 v126, 0\n\ts_add_u32 %1, %1, 1\n\tbuffer_atomic_add v126, %2, %3, 0 offen sc0 sc1\n\ts_waitcnt vmcnt(1)\n\tv_cmp_ne_u32 vcc, %4, v127\n\ts_cbranch_vccz 2f\n\t"
               "v_mov_b32 v127, 0\n\ts_cmp_lt_u32 %1, 0x8000\n\ts_cbranch_scc1 1b\n\t"
               "2:\n\ts_mov_b64 exec, %0"
               : "=&s"(sv), "=&s"(n) : "v"(voff), "s"(srd), "v"(expect), "n"(MASK) : "memory", "scc", "vcc");
}

template <int R> DI void ld_cs(u32x4 srd, unsigned voff, unsigned step) {
  asm volatile(".set i_, 0\n\t.rept 20\n\tbuffer_load_dword v[%c3+i_], %0, %1, 0 offen sc1\n\tv_add_u32 %0, %0, %2\n\t.set i_, i_+1\n\t.endr" : "+v"(voff) : "s"(srd), "v"(step), "n"(R) : "memory");
}
template <int R> DI void all_flags(__amdgpu_buffer_rsrc_t rs, unsigned off, unsigned epoch, unsigned lane16) {
  u32x4 f = __builtin_bit_cast(u32x4, vget4<R>());
#pragma unroll 1
  for (unsigned sp = 0; sp < SPIN_CAP && !__all(f[0] == epoch && f[1] == epoch && f[2] == epoch && f[3] == epoch); ++sp) {
    asm volatile("s_sleep 8" ::: "memory");
    f = ld_sc1(rs, lane16, off);
  }
}
DI void peek(u32x4 srd, unsigned voff) {
  unsigned long long sv;
  asm volatile("s_mov_b64 %0, exec\n\ts_mov_b64 exec, 1\n\tv_mov_b32 v124, 0\n\tbuffer_atomic_add v124, %1, %2, 0 offen sc0 sc1\n\ts_mov_b64 exec, %0" : "=&s"(sv) : "v"(voff), "s"(srd) : "memory");
}
template <int VM> DI void peek_await(u32x4 srd, unsigned voff, unsigned expect) {
  unsigned c;
  asm volatile("s_waitcnt vmcnt(%c1)\n\tv_readfirstlane_b32 %0, v124" : "=s"(c) : "n"(VM) : "memory");
  if (__builtin_expect(c != expect, 0)) await<1ull>(srd, voff, expect);
}
template <int R> DI u32x4 tr_col(unsigned sel) {
  unsigned o0, o1, o2, o3;
  asm volatile("v_perm_b32 %0, v[%c5+4], v[%c5], %4\n\tv_perm_b32 %1, v[%c5+12], v[%c5+8], %4\n\tv_perm_b32 %2, v[%c5+20], v[%c5+16], %4\n\tv_perm_b32 %3, v[%c5+28], v[%c5+24], %4\n\ts_nop 1"
               : "=v"(o0), "=v"(o1), "=v"(o2), "=v"(o3) : "s"(sel), "n"(R));
  return u32x4{o0, o1, o2, o3};
}

#define AE(i) "v_exp_f32 v[%c[sc]+" #i "], v[%c[sc]+" #i "]\n\t"
#define AC(d, i) "v_cvt_pk_bf16_f32 v" #d ", v[%c[sc]+" #i "], v[%c[sc]+" #i "+1]\n\t"
template <int SC, int SN, int OV> DI void att_a(unsigned vA) {
  asm volatile(
      "s_waitcnt lgkmcnt(1)\n\t"
      "v_mfma_f32_32x32x16_bf16 v[144:159], v[242:245], v[228:231], v[144:159]\n\t"
      AE(0) AE(1) "ds_read_b128 v[242:245], %[va] offset:%c[ov]+704\n\t" AE(2) AE(3) AC(224, 0)
      "v_mfma_f32_32x32x4_2b_bf16 v[%c[sn]:%c[sn]+31], v[236:237], v[254:255], 0\n\t"
      AC(225, 2) AE(4) AE(5) AE(6) AE(7) AC(226, 4) AC(227, 6) AE(8) AE(9) AE(10) AE(11) AC(228, 8)
      "s_waitcnt lgkmcnt(1)\n\t"
      "v_mfma_f32_32x32x16_bf16 v[128:143], v[238:241], v[224:227], v[128:143]\n\t"
      AC(229, 10) AE(12) AE(13) AE(14) AE(15) AC(230, 12) AC(231, 14)
      ::[va] "v"(vA), [sc] "n"(SC), [sn] "n"(SN), [ov] "n"(OV) : "memory");
}
template <int SC, int SN, int OK> DI void att_b(unsigned kA, unsigned kB, unsigned vA) {
  asm volatile(
      AE(16) "s_waitcnt lgkmcnt(0)\n\t"
      "v_mfma_f32_32x32x16_bf16 v[128:143], v[242:245], v[228:231], v[128:143]\n\t"
      AE(17) AE(18) AE(19) AC(224, 16)
      "v_mfma_f32_32x32x16_bf16 v[%c[sn]:%c[sn]+15], v[232:235], v[246:249], v[%c[sn]:%c[sn]+15]\n\t"
      AC(225, 18) AE(20) AE(21) AE(22) AE(23) AC(226, 20) AC(227, 22)
      "v_mfma_f32_32x32x16_bf16 v[%c[sn]+16:%c[sn]+31], v[232:235], v[250:253], v[%c[sn]+16:%c[sn]+31]\n\t"
      AE(24) AE(25) "ds_read_b128 v[232:235], %[ka] offset:%c[ok]\n\tds_read_b64 v[236:237], %[kb] offset:%c[ok]\n\t" AE(26) AE(27) AC(228, 24)
      "v_mfma_f32_32x32x16_bf16 v[144:159], v[238:241], v[224:227], v[144:159]\n\t"
      AC(229, 26) AE(28) AE(29) AE(30) AE(31) AC(230, 28) AC(231, 30)
      "ds_read_b128 v[238:241], %[va] offset:%c[ok]"
      ::[ka] "v"(kA), [kb] "v"(kB), [va] "v"(vA), [sc] "n"(SC), [sn] "n"(SN), [ok] "n"(OK) : "memory");
}
#undef AE
#undef AC
template <int SEND, int KEEP> DI void att_merge(float (&O)[12], unsigned mine, unsigned theirs) {
  asm volatile("ds_write_b128 %0, v[%c2:%c2+3]\n\tds_write_b128 %0, v[%c2+4:%c2+7] offset:1024\n\tds_write_b128 %0, v[%c2+8:%c2+11] offset:2048\n\t"
               "s_waitcnt vmcnt(0) lgkmcnt(0)\n\ts_barrier\n\t"
               "ds_read_b128 v[160:163], %1\n\tds_read_b128 v[164:167], %1 offset:1024\n\tds_read_b128 v[168:171], %1 offset:2048\n\ts_waitcnt lgkmcnt(0)\n\ts_barrier"
               ::"v"(mine), "v"(theirs), "n"(SEND) : "memory");
  static_for<12>([&](auto I_) AI { O[I_.value] = vadd<KEEP + I_.value, 160 + I_.value>(); });
}

__global__ void __launch_bounds__(512, 2) __attribute__((amdgpu_num_vgpr(48))) k_main(const char* __restrict__ x, const char* __restrict__ wqkv, const char* __restrict__ wo, const char* __restrict__ w1, const char* __restrict__ w2,
    const char* __restrict__ w3, const char* __restrict__ w4, char* __restrict__ ws, char* __restrict__ out) {
  extern __shared__ __attribute__((aligned(16))) char smem[];
  lds_ptr lds = (lds_ptr)smem;
  OWN_ALL_VGPRS();
  asm volatile(".subsection 2\n\t.rept 96\n\ts_nop 0\n\t.endr\n\t.subsection 0");
  STAMP_DECL; STAMP(0);
  const unsigned ldsb = (unsigned)(size_t)lds;
  const int tid = threadIdx.x, lane = tid & 63, w = __builtin_amdgcn_readfirstlane(tid >> 6), q = lane >> 4, tok = lane & 15;
  const int bid = blockIdx.x, xcd = bid & 7, r = bid >> 3;
  const int j = r & 7, head = 8 * (xcd & 3) + j, qb = 4 * (xcd >> 2) + (r >> 3), rb = 4 * qb + (xcd & 3);
  const __amdgpu_buffer_rsrc_t rsw = mk_rsrc(ws, WS_END);
  const unsigned wfs = OFF_WF + j * NGRP * GRP_B;
  const unsigned lane16 = lane * 16;

  const int rb1 = 2 * xcd + (r & 1), g1 = ((r >> 1) + 15) & 15;
  P1Load L1;
  const bool loader = w >= 4 && g1 < 15;
  if (__builtin_expect(loader, 1)) L1 = p1_start(x, wqkv, rb1, g1, (g1 + 5 * (r & 1) + xcd) % 10, w - 4, lane);
  const u32x4 srd = mk_srd(ws, WS_END);
  unsigned epoch = 0;
  if (loader) {
    asm volatile("s_waitcnt vmcnt(%c0)\n\ts_barrier" ::"n"(8 * (P1_DEPTH - 2)) : "memory");
    p1_issue(L1, P1_DEPTH - 1);
#pragma unroll 1
    for (int p = 0; p < 10 - P1_DEPTH; ++p) {
      asm volatile("s_waitcnt vmcnt(%c0)\n\ts_barrier" ::"n"(8 * (P1_DEPTH - 2)) : "memory");
      p1_issue(L1, p + P1_DEPTH);
    }
    if (P1_DEPTH == 4) asm volatile("s_waitcnt vmcnt(16)\n\ts_barrier" ::: "memory");
    if (w == 4) {
      epoch = *(volatile __attribute__((address_space(3))) unsigned*)(lds + L_EPO);
      peek(srd, OFF_AXF + rb1 * 128);
      asm volatile("s_waitcnt vmcnt(9)\n\ts_barrier\n\ts_waitcnt vmcnt(0)" ::: "memory");
      const bool late = __builtin_amdgcn_readfirstlane(__float_as_uint(vget<124>())) != __builtin_amdgcn_readfirstlane(epoch);
      if (late) { asm volatile("s_barrier\n\ts_barrier" ::: "memory"); await<1ull>(srd, OFF_AXF + rb1 * 128, epoch); }
      const u32x4 xs = ld_sc1(rsw, lane16, OFF_XST + rb1 * 1024);
      if (!late) asm volatile("s_barrier\n\ts_barrier" ::: "memory");
      *(__attribute__((address_space(3))) u32x4*)(lds + 16384 + lane16) = xs;
      asm volatile("s_waitcnt lgkmcnt(0)\n\ts_barrier" ::: "memory");
    } else asm volatile("s_waitcnt vmcnt(8)\n\ts_barrier\n\ts_waitcnt vmcnt(0)\n\ts_barrier\n\ts_barrier\n\ts_barrier" ::: "memory");
  } else {
    if (w == 0) {
      epoch = __builtin_amdgcn_raw_buffer_load_b32(rsw, 0, OFF_EPO, AUX_SC1);
      st_sc1(u32x4{0u, 0u, 0u, 0u}, rsw, (lane >> 4) * 2048 + (lane & 15) * 16, OFF_CNT + (32 * rb + j) * 256);
      st_sc1(u32x4{0u, 0u, 0u, 0u}, rsw, lane < 16 ? lane16 : 0x80000000u, OFF_QC + (head * 8 + qb) * 256);
    }
    auto ready = [&]() AI {
      epoch = __builtin_amdgcn_readfirstlane(epoch) + 1u;
      __builtin_amdgcn_raw_buffer_store_b32(epoch, rsw, lane == 0 ? bid * 4 : 0x80000000u, OFF_RDY, AUX_SC1);
      *(__attribute__((address_space(3))) unsigned*)(lds + L_EPO) = epoch;
    };
    if (g1 == 15) {
      p1_stats(x, rsw, rb1, w, lane, [&]() AI { if (w == 0) ready(); });
      if (w == 0) __builtin_amdgcn_raw_buffer_store_b32(epoch, rsw, lane == 0 ? 0 : 0x80000000u, OFF_AXF + rb1 * 128, AUX_SC1);
    } else if (w == 0) {
      asm volatile("s_waitcnt vmcnt(0)" ::: "memory");
      ready();
    }
    STAMP(47);
    if (tid < 256) *(__attribute__((address_space(3))) unsigned*)(lds + L_CONS + tid * 4) = 0u;
    if (tid < 4) *(__attribute__((address_space(3))) unsigned*)(lds + L_FILL + tid * 4) = tid < 2 ? 4u + tid : 0u;
    if (g1 < 10) p1_compute<true>(srd, rsw, lds, rb1, g1, w, lane STAMP_ARG);
    else if (g1 < 15) p1_compute<false>(srd, rsw, lds, rb1, g1, w, lane STAMP_ARG);
    else if (w == 0) {
      asm volatile("s_sleep 64" ::: "memory");
      asm volatile("buffer_load_dwordx4 v[250:253], %0, %1, %2 offen sc1" ::"v"(lane16), "s"(srd), "s"(OFF_RDY) : "memory");
    }
  }
  STAMP(43);
  const int hi = lane >> 5, r32 = lane & 31, wq = w & 3, wk = w >> 2;
  unsigned kvs[3], kvd[3];
#pragma unroll
  for (int i = 0; i < 3; ++i) {
    const int pc = min(w + 8 * i, 21), h2 = pc >= 11;
    kvs[i] = kv_cell((pc - 11 * h2) * 64 + lane, h2, head);
    kvd[i] = ldsb + pc * 1024;
  }
  const int job = w * 256 + bid;
  const bool has_job = job < 850;
  int mat, ks, cb;
  if (job < 300) { mat = job / 100; const int e = job - 100 * mat; ks = e / 5; cb = e - 5 * ks; }
  else if (job < 580) { mat = 3; const int e = job - 300; ks = e / 14; cb = e - 14 * ks; }
  else { mat = 4; const int e = job - 580; ks = e / 5; cb = e - 5 * ks; }
  const char* src = mat == 0 ? wo : mat == 1 ? w1 : mat == 2 ? w2 : mat == 3 ? w3 : w4;
  const unsigned kvb = ldsb + L_KV0 + wk * KV_HALF;
  unsigned kA = kvb + hi * 512 + r32 * 16, kB = kvb + 1024 + tail_slot(swap23(r32)) * 8, vA = kvb + 1280 + hi * 352 + r32 * 16;
  const unsigned qo = OFF_QF + (head * 64 + 8 * qb + 2 * wq) * QF_TILE, qv0 = hi * 512 + r32 * 16, qv1 = hi * QF_TILE + tail_slot(r32) * 8;
  {
    asm volatile("" ::"v"(kvs[0]), "v"(kvs[1]), "v"(kvs[2]), "s"(kvd[0]), "s"(kvd[1]), "s"(kvd[2]), "s"(qo), "v"(qv0), "v"(qv1), "v"(kA), "v"(kB), "v"(vA));
    asm volatile("s_waitcnt vmcnt(0) lgkmcnt(0)\n\ts_barrier" ::: "memory");
    STAMP(44);
    asm volatile(".set i_, 0\n\t.rept 32\n\tv_mov_b32 v[128+i_], 0\n\t.set i_, i_+1\n\t.endr\n\t"
                 ".set i_, 0\n\t.rept 4\n\tv_mov_b32 v[228+i_], 0\n\tv_mov_b32 v[242+i_], 0\n\t.set i_, i_+1\n\t.endr");
    if (w == 0) {
      all_flags<250>(rsw, OFF_RDY, epoch, lane16);
      const int gt = g1 < 5 ? g1 : g1 < 10 ? g1 - 5 : g1 - 10, hh = (128 * gt) / 20 + (lane >> 3);
      if (g1 < 15 && hh <= (128 * gt + 127) / 20 && (g1 >= 5 || (lane & 7) == (rb1 >> 1)))
        __hip_atomic_fetch_add((unsigned*)(ws + OFF_QC + (hh * 8 + (lane & 7)) * 256 + (rb1 & 3) * 64), 1u, __ATOMIC_RELAXED, __HIP_MEMORY_SCOPE_AGENT);
      if (bid == 0) __builtin_amdgcn_raw_buffer_store_b32(epoch, rsw, lane == 0 ? 0 : 0x80000000u, OFF_EPO, AUX_SC1);
      const int span = 1 + ((20 * head + 19) >> 7) - ((20 * head) >> 7);
      await<0xfull>(srd, OFF_QC + (head * 8 + qb) * 256 + lane * 64, (8 + ((lane >> 1) == (qb & 1))) * span);
    }
    asm volatile("s_barrier" ::: "memory");
    STAMP(45);
  }

  {
    asm volatile("buffer_load_dwordx4 v[246:249], %0, %2, %3 offen sc1\n\tbuffer_load_dwordx4 v[250:253], %0, %2, %3 offen offset:1280 sc1\n\t"
                 "buffer_load_dwordx2 v[254:255], %1, %2, %3 offen offset:1024 sc1" ::"v"(qv0), "v"(qv1), "s"(srd), "s"(qo) : "memory");
  }
  auto kv_piece = [&](int c, int i) AI { bdma_sc1(srd, kvs[i], OFF_KV + c * (4 * KV_BLK), kvd[i] + kv_at(c)); };
  kv_piece(0, 0); kv_piece(0, 1); kv_piece(0, 2);
  kv_piece(1, 0); kv_piece(1, 1); kv_piece(1, 2);
  asm volatile(
      "s_waitcnt vmcnt(3) lgkmcnt(0)\n\ts_barrier\n\t"
      "ds_read_b64 v[236:237], %1\n\tds_read_b128 v[232:235], %0\n\ts_waitcnt lgkmcnt(1)\n\t"
      "v_mfma_f32_32x32x4_2b_bf16 v[160:191], v[236:237], v[254:255], 0\n\t"
      "s_nop 15\n\ts_nop 1\n\ts_waitcnt lgkmcnt(0)\n\t"
      "v_mfma_f32_32x32x16_bf16 v[160:175], v[232:235], v[246:249], v[160:175]\n\t"
      "v_mfma_f32_32x32x16_bf16 v[176:191], v[232:235], v[250:253], v[176:191]\n\t"
      "ds_read_b128 v[232:235], %0 offset:%c3\n\tds_read_b64 v[236:237], %1 offset:%c3\n\tds_read_b128 v[238:241], %2 offset:%c3\n\t"
      "s_nop 7"
      ::"v"(kA), "v"(kB), "v"(vA), "n"(KV_REC) : "memory");
  STAMP(2);
  unsigned jvo[2], jcs;
  int kpos = ks;
  if (mat == 4) { const int half = ks >= 27, k27 = ks - 27 * half, j4 = min(k27 / 7, 3), jj = 4 * half + j4, pi = k27 - 7 * j4; kpos = pi < 2 ? 2 * jj + pi : pi < 5 ? 14 + 3 * jj + pi : 35 + 2 * jj - (jj > 3) + pi; }
  const unsigned jso = OFF_WF + ((kpos / 5) * 25 + (kpos % 5) * 5) * 1024;
  auto job_addr = [&]() AI {
    int ln = lane; asm volatile("" : "+v"(ln));
    const int lq = ln >> 4, lt = ln & 15, kk = 4 * cb + (lt >> 2);
    const bool ok = mat != 3 || kk < 54;
#pragma unroll
    for (int h = 0; h < 2; ++h) {
      int jj, t, pos, G;
      if (mat == 3) {
        const int half = kk >= 27, k27 = kk - 27 * half, j4 = min((k27 * 37) >> 8, 3);
        jj = 4 * half + j4; t = 2 * (k27 - 7 * j4) + h; pos = lt & 3;
        const int pass = (t * 13) >> 6; t -= 5 * pass; G = 12 + 4 * pass;
      } else {
        const int per5 = (kk * 13) >> 6, m5 = kk - 5 * per5;
        const bool single = m5 == 2;
        jj = 2 * per5 + (single ? ((lt >> 1) & 1) : (m5 > 2)); t = single ? 4 : 2 * (m5 > 2 ? m5 - 3 : m5) + h;
        pos = single ? 2 * (lt & 1) + h : lt & 3;
        G = mat == 4 ? 24 : 4 * mat;
      }
      jvo[h] = ok ? (unsigned)((jj * NGRP + G) * 25 + t) * 1024 + (lq * 16 + pos) * 16 : 0x80000000u;
    }
    jcs = lt < 2 ? lt * 64 + lq * 16 : 0x80000000u;
  };
  auto job_cols = [&](auto S_) AI {
    constexpr int sl = S_.value;
    const u32x4 o0 = tr_col<96 + sl>(0x05040100u), o1 = tr_col<96 + sl>(0x07060302u);
    st_sc1(o0, rsw, jvo[sl >> 1] + (sl & 1) * 128, jso);
    st_sc1(o1, rsw, jvo[sl >> 1] + (sl & 1) * 128 + 64, jso);
    if (mat == 1 || mat == 3) {
      const bf16x8 ones = {0x3f80, 0x3f80, 0x3f80, 0x3f80, 0x3f80, 0x3f80, 0x3f80, 0x3f80};
      const f32x4 z = {0.f, 0.f, 0.f, 0.f};
      const f32x4 c0 = MFMA16(__builtin_bit_cast(bf16x8, o0), ones, z), c1 = MFMA16(__builtin_bit_cast(bf16x8, o1), ones, z);
      st_sc1(__builtin_bit_cast(u32x4, (lane & 1) ? c1 : c0), rsw, jcs, (mat == 1 ? OFF_CS1 + (ks * 5 + cb) * 512 : OFF_CS3 + (ks * 14 + cb) * 512) + sl * 128);
    }
  };
  const unsigned rt = 4 * rb + (w & 3);
  const unsigned kp = 5 * (j >> 1) + 3 * (j & 1), ksg = 5 * (j >> 1) + 2;
  const unsigned rowoff = ((16 * rt + tok) * D_) * 2;
  const unsigned colp = (32 * kp + 8 * q) * 2, cols = (32 * ksg + 16 * (j & 1) + 4 * q) * 2;
  auto fill = [&](auto I_) AI {
    constexpr int it = I_.value;
    const u32x4 sent = {SENT, SENT, SENT, SENT};
    if constexpr (it < 5) st_sc1(sent, rsw, lane16, OFF_O + (bid * 40 + w * 5 + it) * 1024);
    else if (w == 7) st_sc1(sent, rsw, lane16, OFF_ST1 + bid * 1024);
  };
  auto side = [&](auto C_, auto S_) AI {
    constexpr int c = C_.value, sl = S_.value, t = 7 * c + sl;
    if constexpr (c == 0) {
      if constexpr (sl < 6) { kv_piece(2 + 2 * sl / 3, 2 * sl % 3); kv_piece(2 + (2 * sl + 1) / 3, (2 * sl + 1) % 3); }
      else { const int i = min(w, 5), h2 = i >= 3; bdma_sc1(srd, kv_cell((i - 3 * h2) * 64 + lane, h2, head), OFF_KV, ldsb + L_WRAP + i * 1024); }
    } else if constexpr (c == 1) {
      if constexpr (sl < 2) if (has_job) {
        int ln = lane; asm volatile("" : "+v"(ln));
        const int lq = ln >> 4, N = mat == 3 ? F_ : D_, n0 = 128 * cb + 8 * (ln & 15);
        int k0 = 32 * ks + 8 * lq + 4 * sl;
        if (mat == 0) { const int o = 4 * ks + lq, i = (o * 205) >> 10, oo = o - 5 * i; k0 = 40 * i + (oo < 3 ? 8 * oo : 8 * oo - 4) + 4 * sl + ((sl == 1 && oo == 2) ? 16 : 0); }
        const unsigned off = (unsigned)(k0 * N + (n0 < N ? n0 : 0)) * 2;
        gload4<96 + 16 * sl, 0>(src, off); gload4<100 + 16 * sl, 0>(src + N * 2, off);
        gload4<104 + 16 * sl, 0>(src + N * 4, off); gload4<108 + 16 * sl, 0>(src + N * 6, off);
      }
    } else if constexpr (c == 2) {
      if constexpr (sl >= 5) if (has_job) {
        if (sl == 5) { job_addr(); asm volatile("s_waitcnt vmcnt(0)" ::: "memory"); }
        job_cols(IC<sl - 5>{});
      }
    } else if constexpr (c == 3) {
      if constexpr (sl < 2) { if (has_job) job_cols(IC<sl + 2>{}); }
      else fill(IC<sl - 2>{});
    } else if constexpr (c == 4) {
      if constexpr (sl == 0) fill(IC<5>{});
      if constexpr (sl >= 1 && sl <= 3) { kv_piece(6, sl - 1); kv_piece(7, sl - 1); }
      if constexpr (sl == 4) if (tid == 0) __hip_atomic_store((unsigned*)(ws + OFF_FLAG) + bid, epoch, __ATOMIC_RELAXED, __HIP_MEMORY_SCOPE_AGENT);
      if constexpr (sl == 6) if (w < 4) {
        gload4<116, 0>(x, rowoff + colp); gload4<120, 64>(x, rowoff + colp);
        asm volatile("global_load_dwordx2 v[124:125], %0, %1" ::"v"(rowoff + cols), "s"(x) : "memory");
      }
    } else if constexpr (t < 39) {
      if constexpr (sl == 1) if (w == 0) asm volatile("buffer_load_dwordx4 v[96:99], %0, %1, %2 offen sc1" ::"v"(lane16), "s"(srd), "s"(OFF_FLAG) : "memory");
    } else if constexpr (t < 52) {
      const unsigned pc = (w + 8 * (t - 39)) * 1024;
      if (t < 51 || w < 4) bdma_sc1(srd, lane16, wfs + pc, ldsb + pc);
    }
  };
  static_for<8>([&](auto C_) AI {
    constexpr int c = C_.value;
    __builtin_amdgcn_s_setprio(2);
    att_a<160, 192, 1 * KV_REC>(vA); side(C_, IC<0>{}); att_b<160, 192, 2 * KV_REC>(kA, kB, vA); side(C_, IC<1>{});
    __builtin_amdgcn_s_setprio(1);
    att_a<192, 160, 2 * KV_REC>(vA); side(C_, IC<2>{}); att_b<192, 160, 3 * KV_REC>(kA, kB, vA); side(C_, IC<3>{});
    __builtin_amdgcn_s_setprio(0);
    att_a<160, 192, 3 * KV_REC>(vA);
    if constexpr (c == 5) if (w == 0) {
      asm volatile("s_waitcnt vmcnt(0)" ::: "memory");
      all_flags<96>(rsw, OFF_FLAG, epoch, lane16);
    }
    if constexpr (c == 0) asm volatile("s_waitcnt vmcnt(8) lgkmcnt(0)\n\ts_barrier" ::: "memory");
    else if constexpr (c == 1) { if (has_job) asm volatile("s_waitcnt vmcnt(8) lgkmcnt(0)\n\ts_barrier" ::: "memory"); else asm volatile("s_waitcnt vmcnt(0) lgkmcnt(0)\n\ts_barrier" ::: "memory"); }
    else if constexpr (c == 4) asm volatile("s_waitcnt vmcnt(6) lgkmcnt(0)\n\ts_barrier" ::: "memory");
    else if constexpr (c == 5) asm volatile("s_waitcnt vmcnt(0) lgkmcnt(0)\n\ts_barrier" ::: "memory");
    else if constexpr (c == 6) asm volatile("s_waitcnt lgkmcnt(0)\n\ts_barrier" ::: "memory");
    __builtin_amdgcn_s_setprio(3);
    { const int d = c < 7 ? kv_at(c + 1) - kv_at(c) : L_WRAP - kv_at(7) + wk * (3072 - KV_HALF); kA += d; kB += d; vA += d; }
    att_b<160, 192, 0>(kA, kB, vA); side(C_, IC<4>{});
    att_a<192, 160, 0>(vA); side(C_, IC<5>{});
    att_b<192, 160, KV_REC>(kA, kB, vA); side(C_, IC<6>{});
  });
  const int qt = 8 * qb + 2 * wq + wk;
  {
    asm volatile("s_waitcnt lgkmcnt(0)\n\tv_mfma_f32_32x32x16_bf16 v[144:159], v[242:245], v[228:231], v[144:159]\n\ts_nop 11" ::: "memory");
    STAMP(3);
    float O[12];
    const unsigned mine = ldsb + (w < 6 ? L_KV0 + w * 3072 : L_SCR2 + (w - 6) * 3072) + lane16;
    const unsigned theirs = ldsb + ((w ^ 4) < 6 ? L_KV0 + (w ^ 4) * 3072 : L_SCR2 + ((w ^ 4) - 6) * 3072) + lane16;
    if (wk == 0) att_merge<144, 128>(O, mine, theirs); else att_merge<128, 144>(O, mine, theirs);
    auto rr = __builtin_amdgcn_permlane32_swap(__float_as_uint(O[8]), __float_as_uint(O[8]), false, false);
    float l = __uint_as_float(rr[1]);
    bool fin = true;
#pragma unroll
    for (int i = 0; i < 12; ++i) fin = fin && (fabsf(O[i]) <= 3.0e38f || (hi && i > 8));
    const bool bad = !(l >= 8.6736174e-19f && l <= 1.2676506e30f && fin);
    if (__builtin_expect(__any(bad), 0)) {
      const f32x16 Os = attn_safe(rsw, OFF_QF + (head * 64 + qt) * QF_TILE, head, lane);
#pragma unroll
      for (int i = 0; i < 12; ++i) O[i] = Os[i];
      auto r2 = __builtin_amdgcn_permlane32_swap(__float_as_uint(O[8]), __float_as_uint(O[8]), false, false);
      l = __uint_as_float(r2[1]);
    }
    const float il = __builtin_amdgcn_rcpf(l);
    const unsigned rto = OFF_O + (2 * qt + (r32 >> 4)) * FM_RT + (r32 & 15) * 16;
    const int o1 = 5 * (head >> 1) + 3 * (head & 1) + hi, o2 = 5 * (head >> 1) + 2;
    u32x4 o = {nosent(pack2(O[0] * il, O[1] * il)), nosent(pack2(O[2] * il, O[3] * il)), nosent(pack2(O[4] * il, O[5] * il)), nosent(pack2(O[6] * il, O[7] * il))};
    st_sc1(o, rsw, rto + o1 * 256, 0);
    if (!hi) st_sc1(u32x2{nosent(pack2(O[8] * il, O[9] * il)), nosent(pack2(O[10] * il, O[11] * il))}, rsw, rto + o2 * 256 + (head & 1) * 8, 0);
    arrive<0xffffull>(srd, OFF_CNT + (16 * qt + lane) * 256 + (head >> 3) * 64);
  }
  if (w >= 6) {
    static_for<3>([&](auto R_) AI {
      const int id = min(tid - 384 + 128 * R_.value, 319);
      const bool is1 = id < 80;
      const int i = is1 ? id : id - 80, t = i >> 4, row = i & 15;
      int n;
      if (is1) n = t < 4 ? 32 * (kp + (t >> 1)) + 8 * (row >> 2) + 4 * (t & 1) + (row & 3) : 32 * ksg + 16 * (j & 1) + row;
      else n = min(32 * (27 * (j >> 2) + 7 * (j & 3) + (t >> 1)) + 8 * (row >> 2) + 4 * (t & 1) + (row & 3), F_ - 1);
      ld_cs<128 + 20 * R_.value>(srd, (is1 ? OFF_CS1 : OFF_CS3) + ((n >> 7) * 128 + (n & 7) * 16 + ((n >> 3) & 15)) * 4, (is1 ? 640 : 1792) * 4);
    });
    asm volatile("s_waitcnt vmcnt(0)" ::: "memory");
    static_for<3>([&](auto R_) AI {
      float sacc = 0.f;
      static_for<20>([&](auto K_) AI { sacc += vget<128 + 20 * R_.value + K_.value>(); });
      *(__attribute__((address_space(3))) float*)(lds + L_CS1 + min(tid - 384 + 128 * R_.value, 319) * 4) = sacc;
    });
    asm volatile("s_waitcnt lgkmcnt(0)" ::: "memory");
    *(volatile __attribute__((address_space(3))) unsigned*)(lds + L_CSF + (w - 6) * 4) = 1u;
    return;
  }

  if (w >= 4) {
    auto issue = [&](int G) AI {
      const unsigned so = wfs + G * GRP_B, d = ldsb + (G % 6) * GRP_B;
      if (G >= 20 && (G < 24 || G == 34)) {
        const unsigned st = G == 34 ? 4096 : 5120;
#pragma unroll
        for (int i = 0; i < 5; ++i) bdma4_sc1(srd, lane16, so + i * st, d + i * st);
        asm volatile("s_waitcnt vmcnt(20)" ::: "memory");
      } else {
#pragma unroll
        for (int i = 0; i < 6; ++i) bdma4_sc1(srd, lane16, so + i * 4096, d + i * 4096);
        bdma_sc1(srd, lane16, so + 24576, d + 24576);
        asm volatile("s_waitcnt vmcnt(25)" ::: "memory");
      }
    };
    volatile __attribute__((address_space(3))) unsigned* fillw = (volatile __attribute__((address_space(3))) unsigned*)(lds + L_FILL + (w - 4) * 4);
    volatile __attribute__((address_space(3))) unsigned* consw = (volatile __attribute__((address_space(3))) unsigned*)(lds + L_CONS + (lane & 3) * 256);
    __builtin_amdgcn_s_setprio(PRIO_L);
    int have = 0;
    auto min_cons = [&]() AI { const unsigned c = *consw; return (int)min(min(__builtin_amdgcn_readlane(c, 0), __builtin_amdgcn_readlane(c, 1)), min(__builtin_amdgcn_readlane(c, 2), __builtin_amdgcn_readlane(c, 3))); };
#pragma unroll 1
    for (int G = w; G < NGRP; G += 2) {
      const int need = G - 5;
      if (have < need && (have = min_cons()) < need) {
        asm volatile("s_waitcnt vmcnt(0)" ::: "memory");
        *fillw = G;
#pragma unroll 1
        for (unsigned sp = 0; sp < LDS_SPIN && (have = min_cons()) < need; ++sp) asm volatile("s_sleep 2" ::: "memory");
      }
      issue(G);
      *fillw = G;
    }
    asm volatile("s_waitcnt vmcnt(0)" ::: "memory");
    *fillw = NGRP;
    return;
  }

  __builtin_amdgcn_s_setprio(PRIO_C);
  const unsigned wl = ldsb + (q * 16 + 4 * (tok & 3) + (tok >> 2)) * 16;
  const unsigned wb[5] = {wl, wl + 2 * GRP_B, wl + 4 * GRP_B, ldsb + L_FILL, ldsb + L_CONS + w * 256 + lane * 4};
  const unsigned vo0[4] = {lane16, lane16, lane16, lane16};
  asm volatile(".set i_, 0\n\t.rept 5\n\tds_read_b128 v[208+4*i_:211+4*i_], %0 offset:1024*i_\n\t.set i_, i_+1\n\t.endr\n\ts_waitcnt lgkmcnt(0)" ::"v"(wb[0]) : "memory");
  const unsigned cown = OFF_CNT + (rt * 8 + j) * 256, cgrp = OFF_CNT + (rt * 8 + lane) * 256;
  const unsigned vsg = (q & 1) ? 0x80000000u : (2 * (j & 1) + (q >> 1)) * 256 + tok * 16;
  const unsigned vst = lane < 16 ? (j >> 1) * 256 + lane16 + (j & 1) * 8 : 0x80000000u;
  const unsigned wfl = wfs + (wl - ldsb);
  typedef const __attribute__((address_space(3))) f32x4* lcs;
  asm volatile(".set i_, 0\n\t.rept 10\n\tv_lshlrev_b32 v[96+2*i_], 16, v[116+i_]\n\tv_and_b32 v[97+2*i_], 0xffff0000, v[116+i_]\n\t.set i_, i_+1\n\t.endr");
  STAMP(4);
  await<0xfull>(srd, cown + lane * 64, 8);
  STAMP(5);
  ld_unit(srd, vo0, OFF_O + rt * FM_RT);
  if (__builtin_expect(run_unit<0, 4, 0, 20, vmw(63, 10, 5, 0), 1, 0, -1, 0>(wb, srd, 0, 0, 0, 0 STAMP_ARG) != 0, 0)) redo_unit<false>(rsw, vo0, OFF_O + rt * FM_RT, 20, rsw, wfl, srd);
  STAMP(6);
  asm volatile(".set i_, 0\n\t.rept 10\n\tv_pk_add_f32 v[96+2*i_:97+2*i_], v[96+2*i_:97+2*i_], v[228+2*i_:229+2*i_]\n\t.set i_, i_+1\n\t.endr");
  st_tile<96>(srd, lane16, vsg, OFF_H + rt * FM_RT + kp * 1024, OFF_H + rt * FM_RT + ksg * 1024);
  arrive<0xffull>(srd, cgrp + 4);
  st_stats<96>(rsw, vst, OFF_ST1 + rt * 1024);
  STAMP(7);
#pragma unroll 1
  for (unsigned sp = 0; sp < LDS_SPIN && !__all(*(volatile __attribute__((address_space(3))) unsigned*)(lds + L_CSF + (lane & 1) * 4) != 0u); ++sp) asm volatile("s_sleep 1" ::: "memory");
  f32x4 cs[5];
#pragma unroll
  for (int t = 0; t < 5; ++t) cs[t] = *(lcs)(lds + L_CS1 + t * 64 + q * 16);
  await<1ull>(srd, cown + 4, 8);
  STAMP(8);
  ld_unit(srd, vo0, OFF_H + rt * FM_RT);
  if (__builtin_expect(run_unit<4, 4, 0, 20, vmw(63, 10, 6, 1), 1, 0, 1, 1>(wb, srd, lane16, OFF_ST1 + rt * 1024, 0, 0 STAMP_ARG) != 0, 0)) redo_unit<false>(rsw, vo0, OFF_H + rt * FM_RT, 20, rsw, wfl + 4 * GRP_B, srd);
  STAMP(9);
  float mean1, r1;
  {
    float var;
    read_stats(rsw, lane16, OFF_ST1 + rt * 1024, mean1, var);
    r1 = __builtin_amdgcn_rsqf(var + LN_EPS);
    const float v2 = var * r1 * r1 + LN_EPS, r2 = r1 * __builtin_amdgcn_rsqf(v2);
    const GeluK gk = gelu_consts(mean1, r2, __builtin_amdgcn_sqrtf((var + LN_EPS) * v2));
    const unsigned o = OFF_H1 + rt * FM_RT;
    ep_gelu<0, 2, 228>(gk, cs[0], cs[1]); st_frag<228>(srd, lane16, o + kp * 1024);
    ep_gelu<2, 2, 236>(gk, cs[2], cs[3]); st_frag<236>(srd, lane16, o + kp * 1024 + 1024);
    ep_gelu<4, 1, 244>(gk, cs[4], cs[4]); st_single<244>(srd, vsg, o + ksg * 1024);
    const int rn = (j & 2) ? (int)rt - 1 : (int)rt + 1;
    if (rn >= 0 && rn < 128) arrive<0xffffull>(srd, (lane < 8 ? cgrp : OFF_CNT + (rn * 8 + lane - 8) * 256) + 68);
    else arrive<0xffull>(srd, cgrp + 68);
  }
  {
    const u32x4 srd1 = mk_srd(ws + OFF_H1, SZ_FM);
    unsigned vo[4];
#pragma unroll
    for (int g = 0; g < 4; ++g) {
      const int T = 16 * (int)rt + tok + (g == 0 ? -1 : g == 1 ? 1 : g == 2 ? -2 : 2);
      vo[g] = (T >= 0 && T < S_) ? (unsigned)((T >> 4) * FM_RT + q * 256 + (T & 15) * 16) : 0x80000000u;
    }
    const f32x2 rr = {r1, r1}, nmr = {-mean1 * r1, -mean1 * r1};
    STAMP(10);
    await<1ull>(srd, cown + 68, (rt == 0 || rt == 127) ? 12 : 16);
    ld_unit(srd1, vo, 0);
    if (__builtin_expect(run_unit<8, 4, 0, 20, vmw(63, 10, 5, 0), 1, 0, -1, 0>(wb, srd, 0, 0, 0, 0 STAMP_ARG) != 0, 0)) redo_unit<false>(mk_rsrc(ws + OFF_H1, SZ_FM), vo, 0, 20, rsw, wfl + 8 * GRP_B, srd1);
    STAMP(11);
    asm volatile(".set i_, 0\n\t.rept 10\n\tv_pk_fma_f32 v[228+2*i_:229+2*i_], v[96+2*i_:97+2*i_], %0, v[228+2*i_:229+2*i_]\n\t.set i_, i_+1\n\t.endr\n\t"
                 ".set i_, 0\n\t.rept 10\n\tv_pk_add_f32 v[228+2*i_:229+2*i_], v[228+2*i_:229+2*i_], %1\n\t.set i_, i_+1\n\t.endr" ::"v"(rr), "v"(nmr));
    st_tile<228>(srd, lane16, vsg, OFF_X1 + rt * FM_RT + kp * 1024, OFF_X1 + rt * FM_RT + ksg * 1024);
    arrive<0xffull>(srd, cgrp + 132);
    st_stats<228>(rsw, vst, OFF_ST2 + rt * 1024);
    asm volatile(".set i_, 0\n\t.rept 10\n\tv_pk_add_f32 v[96+2*i_:97+2*i_], v[96+2*i_:97+2*i_], v[228+2*i_:229+2*i_]\n\t.set i_, i_+1\n\t.endr");
  }
  STAMP(12);
#pragma unroll
  for (int t = 0; t < 5; ++t) cs[t] = *(lcs)(lds + L_CS3 + t * 64 + q * 16);
  await<1ull>(srd, cown + 132, 8);
  ld_unit(srd, vo0, OFF_X1 + rt * FM_RT);
  {
    const unsigned gs = OFF_G + rt * G_RT;
    const unsigned gl = gs;
    if (__builtin_expect(run_unit<12, 4, 0, 20, vmw(63, 10, 6, 1), 1, 0, 1, 1>(wb, srd, lane16, OFF_ST2 + rt * 1024, 0, 0 STAMP_ARG) != 0, 0)) redo_unit<true>(rsw, vo0, OFF_X1 + rt * FM_RT, 20, rsw, wfl + 12 * GRP_B, srd);
    STAMP(13);
    float mean, var;
    read_stats(rsw, lane16, OFF_ST2 + rt * 1024, mean, var);
    const GeluK gk = gelu_consts(mean, __builtin_amdgcn_rsqf(var + LN_EPS), __builtin_amdgcn_sqrtf(var + LN_EPS));
    ep_gelu<0, 2, 228>(gk, cs[0], cs[1]); st_frag<228>(srd, lane16, gs + 2 * j * 1024);
    ep_gelu<2, 2, 236>(gk, cs[2], cs[3]); st_frag<236>(srd, lane16, gs + (2 * j + 1) * 1024);
    ep_gelu<4, 1, 250>(gk, cs[4], cs[4]);
#pragma unroll
    for (int t = 0; t < 5; ++t) cs[t] = *(lcs)(lds + L_CS3 + (5 + t) * 64 + q * 16);
    STAMP(14);
    run_unit<16, 4, 0, 20, vmw(63), 0, 0, -1, 0, 0>(wb, srd, 0, 0, 0, cgrp + 196 STAMP_ARG);
    peek(srd, cown + 196);
    ep_gelu<0, 1, 252>(gk, cs[0], cs[0]); st_frag<250>(srd, lane16, gs + (16 + 3 * j) * 1024);
    ep_gelu<1, 2, 232>(gk, cs[1], cs[2]); st_frag<232>(srd, lane16, gs + (17 + 3 * j) * 1024);
    ep_gelu<3, 2, 240>(gk, cs[3], cs[4]); st_frag<240>(srd, lane16, gs + (18 + 3 * j) * 1024);
#pragma unroll
    for (int t = 0; t < 4; ++t) cs[t] = *(lcs)(lds + L_CS3 + (10 + t) * 64 + q * 16);
    peek_await<3>(srd, cown + 196, 8);
    run_unit<20, 4, 0, 20, vmw(63), 0, 16, -1, 0, 5>(wb, srd, lane16, gl, 0, cgrp + 8 STAMP_ARG);
    peek(srd, cown + 8);
    const unsigned g3 = gs + (40 + 2 * j - (j > 3)) * 1024;
    ep_gelu<0, 2, 228>(gk, cs[0], cs[1]); st_frag<228>(srd, lane16, g3);
    if ((j & 3) != 3) { ep_gelu<2, 2, 236>(gk, cs[2], cs[3]); st_frag<236>(srd, lane16, g3 + 1024); }
    STAMP(15);
    if ((j & 3) != 3) peek_await<2>(srd, cown + 8, 8); else peek_await<1>(srd, cown + 8, 8);
    STAMP(16);
    ld_frags<16, 4>(srd, lane16, gl + 16 * 1024);
    run_unit<24, 6, 0, 30, vmw(63, 63, 63, 16, 17, 11), 0, 20, 3, 2, 9>(wb, srd, lane16, gl + 20 * 1024, cown + 72, cgrp + 72 STAMP_ARG);
    peek_await<0>(srd, cown + 72, 8);
    ld_frags<0, 5>(srd, lane16, gl + 40 * 1024); ld_frags<5, 5>(srd, lane16, gl + 45 * 1024);
    run_unit<30, 5, 30, 54, vmw(63, 63, 9, 4, 0), 0, 4, -1, 0>(wb, srd, lane16, gl + 50 * 1024, 0, 0 STAMP_ARG);
    STAMP(17);
    asm volatile(".set i_, 0\n\t.rept 10\n\tv_pk_add_f32 v[228+2*i_:229+2*i_], v[96+2*i_:97+2*i_], v[228+2*i_:229+2*i_]\n\t.set i_, i_+1\n\t.endr\n\t"
                 EP_PACK(116, 228) EP_PACK(118, 232) EP_PACK(120, 236) EP_PACK(122, 240) EP_PACK(124, 244)
                 "global_store_dwordx4 %0, v[116:119], %2\n\tglobal_store_dwordx4 %0, v[120:123], %2 offset:64\n\tglobal_store_dwordx2 %1, v[124:125], %2\n\ts_waitcnt lgkmcnt(0)"
                 ::"v"(rowoff + colp), "v"(rowoff + cols), "s"(out) : "memory");
  }
  STAMP(18);
  STAMP_OUT(1);
}

extern "C" void kernel_launch(void* const* d_in, const int* in_sizes, int n_in, void* d_out, int out_size, void* d_ws, size_t ws_size, hipStream_t stream) {
  (void)in_sizes; (void)n_in; (void)out_size; (void)ws_size;
  (void)hipFuncSetAttribute((const void*)k_main, hipFuncAttributeMaxDynamicSharedMemorySize, K2_LDS + 1024);
  k_main<<<256, 512, K2_LDS + 1024, stream>>>((const char*)d_in[0], (const char*)d_in[1], (const char*)d_in[2], (const char*)d_in[4], (const char*)d_in[5], (const char*)d_in[6],
                                              (const char*)d_in[7], (char*)d_ws, (char*)d_out);
}
```

```cpp
#include <hip/hip_runtime.h>
#include <hip/hip_bf16.h>

typedef __attribute__((ext_vector_type(8))) short bf16x8;
typedef __attribute__((ext_vector_type(4))) short s16x4;
typedef __attribute__((ext_vector_type(2))) __bf16 bf16x2;
typedef __attribute__((ext_vector_type(2))) float f32x2;
typedef __attribute__((ext_vector_type(4))) float f32x4;
typedef __attribute__((ext_vector_type(16))) float f32x16;
typedef __attribute__((ext_vector_type(4))) unsigned u32x4;
typedef __attribute__((ext_vector_type(2))) unsigned u32x2;
typedef __attribute__((address_space(3))) const char* lds_cptr;
typedef __attribute__((address_space(3))) char* lds_ptr;
#define DI __device__ __forceinline__
#define AI __attribute__((always_inline))
#define SBAR() __builtin_amdgcn_sched_barrier(0)
#define PIN(x) asm volatile("" : "+v"(x))
#define MFMA16(a, b, c) __builtin_amdgcn_mfma_f32_16x16x32_bf16((a), (b), (c), 0, 0, 0)
#define MFMA32(a, b, c) __builtin_amdgcn_mfma_f32_32x32x16_bf16((a), (b), (c), 0, 0, 0)

constexpr int S_ = 2048, D_ = 640, F_ = 1728;
constexpr float LN_EPS = 1e-5f;
constexpr float QSCALE = 0.22360679774997896f * 1.4426950408889634f;
constexpr unsigned SENT = 0xFFFFFFFFu;

constexpr unsigned QF_TILE = 1280, KV_REC = 2688, FM_RT = 20 * 1024, G_RT = 54 * 1024;
constexpr unsigned SZ_FM = 128 * FM_RT;
constexpr unsigned OFF_QF = 0;
constexpr unsigned OFF_KV = OFF_QF + 32 * 64 * QF_TILE;
constexpr unsigned KV_BLK = 40960 + 40960 + 128;
constexpr unsigned OFF_O = OFF_KV + 64 * KV_BLK;
constexpr unsigned OFF_H = OFF_O + SZ_FM;
constexpr unsigned OFF_H1 = OFF_H + SZ_FM;
constexpr unsigned OFF_X1 = OFF_H1 + SZ_FM;
constexpr unsigned OFF_G = OFF_X1 + SZ_FM;
constexpr unsigned OFF_ST1 = OFF_G + 128 * G_RT;
constexpr unsigned OFF_ST2 = OFF_ST1 + 128 * 1024;
constexpr int NGRP = 35;
constexpr unsigned GRP_B = 25 * 1024;
constexpr unsigned OFF_WF = OFF_ST2 + 128 * 1024;
constexpr unsigned OFF_CS1 = OFF_WF + 8 * NGRP * GRP_B;
constexpr unsigned OFF_CS3 = OFF_CS1 + 20 * 640 * 4;
constexpr unsigned OFF_FLAG = OFF_CS3 + 20 * 1792 * 4;
constexpr unsigned OFF_CNT = OFF_FLAG + 1024;
constexpr unsigned OFF_QC = OFF_CNT + 1024 * 256;
constexpr unsigned OFF_RDY = OFF_QC + 256 * 256;
constexpr unsigned OFF_AXF = OFF_RDY + 1024;
constexpr unsigned OFF_XST = OFF_AXF + 16 * 128;
constexpr unsigned OFF_EPO = OFF_XST + 16 * 1024;
constexpr unsigned WS_END = OFF_EPO + 256;

#define STAMP_DECL
#define STAMP_ARG
#define STAMP_PAR
#define STAMP(i)
#define STAMPF(i)
#define STAMP_OUT(k)
DI unsigned pack2(float lo, float hi) { f32x2 v = {lo, hi}; return __builtin_bit_cast(unsigned, __builtin_convertvector(v, bf16x2)); }
DI float bflo(unsigned u) { return __uint_as_float(u << 16); }
DI float bfhi(unsigned u) { return __uint_as_float(u & 0xffff0000u); }
DI float dot2(unsigned a, unsigned b, float c) { return __builtin_amdgcn_fdot2_f32_bf16(__builtin_bit_cast(bf16x2, a), __builtin_bit_cast(bf16x2, b), c, false); }
DI float sum_q(float x) {
  auto s = __builtin_amdgcn_permlane16_swap(__float_as_uint(x), __float_as_uint(x), false, false);
  x = __uint_as_float(s[0]) + __uint_as_float(s[1]);
  auto t = __builtin_amdgcn_permlane32_swap(__float_as_uint(x), __float_as_uint(x), false, false);
  return __uint_as_float(t[0]) + __uint_as_float(t[1]);
}
DI float sum8(float x) {
  x += __builtin_bit_cast(float, __builtin_amdgcn_update_dpp(0, __builtin_bit_cast(int, x), 0xB1, 0xf, 0xf, true));
  x += __builtin_bit_cast(float, __builtin_amdgcn_update_dpp(0, __builtin_bit_cast(int, x), 0x4E, 0xf, 0xf, true));
  x += __builtin_bit_cast(float, __builtin_amdgcn_update_dpp(0, __builtin_bit_cast(int, x), 0x141, 0xf, 0xf, true));
  return x;
}
template <int N> struct IC { static constexpr int value = N; };
template <int N, int I = 0, class Fn> DI void static_for(Fn&& f) { if constexpr (I < N) { f(IC<I>{}); static_for<N, I + 1>(f); } }
DI int swap23(int r) { return (r & ~12) | ((r & 4) << 1) | ((r & 8) >> 1); }
DI bf16x8 lds16(lds_cptr p) { return *(const __attribute__((address_space(3))) bf16x8*)p; }
typedef short v4i16_t __attribute__((ext_vector_type(4)));
DI s16x4 ldstr(lds_cptr p) { return __builtin_bit_cast(s16x4, __builtin_amdgcn_ds_read_tr16_b64_v4i16((__attribute__((address_space(3))) v4i16_t*)p)); }

#define OWN_ALL_VGPRS() asm volatile("" ::: "v255")
template <int R> DI float vget() { float r; asm volatile("v_mov_b32 %0, v[%c1]" : "=v"(r) : "n"(R)); return r; }
template <int R0, int R1> DI float vadd() { float r; asm volatile("v_add_f32 %0, v[%c1], v[%c2]" : "=v"(r) : "n"(R0), "n"(R1)); return r; }
template <int R> DI f32x4 vget4() { return f32x4{vget<R>(), vget<R + 1>(), vget<R + 2>(), vget<R + 3>()}; }
template <int R> DI void vset(unsigned v) { asm volatile("v_mov_b32 v[%c1], %0" ::"v"(v), "n"(R)); }
template <int R, int OFF> DI void gload4(const char* sbase, unsigned voff) {
  asm volatile("global_load_dwordx4 v[%c2:%c2+3], %0, %1 offset:%c3" ::"v"(voff), "s"(sbase), "n"(R), "n"(OFF) : "memory");
}
template <int R> DI f32x4 acc_sub(float m, f32x4 c) {
  float a0, a1, a2, a3;
  asm volatile("v_fma_f32 %0, -%4, %5, v[%c9]\n\tv_fma_f32 %1, -%4, %6, v[%c9+1]\n\tv_fma_f32 %2, -%4, %7, v[%c9+2]\n\tv_fma_f32 %3, -%4, %8, v[%c9+3]"
               : "=&v"(a0), "=&v"(a1), "=&v"(a2), "=&v"(a3) : "v"(m), "v"(c[0]), "v"(c[1]), "v"(c[2]), "v"(c[3]), "n"(R));
  return f32x4{a0, a1, a2, a3};
}
constexpr int AUX_SC1 = 16;
DI __amdgpu_buffer_rsrc_t mk_rsrc(const char* p, unsigned bytes) { return __builtin_amdgcn_make_buffer_rsrc((void*)p, 0, bytes, 0x00020000); }
DI void st_sc1(u32x4 v, __amdgpu_buffer_rsrc_t rs, unsigned voff, unsigned soff) { __builtin_amdgcn_raw_buffer_store_b128(v, rs, voff, soff, AUX_SC1); }
DI void st_sc1(u32x2 v, __amdgpu_buffer_rsrc_t rs, unsigned voff, unsigned soff) { __builtin_amdgcn_raw_buffer_store_b64(v, rs, voff, soff, AUX_SC1); }

DI u32x4 mk_srd(const char* p, unsigned bytes) { const unsigned long long v = (unsigned long long)p; return u32x4{(unsigned)v, (unsigned)(v >> 32) & 0xffffu, bytes, 0x00020000u}; }
DI void bdma_sc1(u32x4 srd, unsigned voff, unsigned soff, unsigned ldsdst) {
  asm volatile("s_mov_b32 m0, %3\n\ts_nop 0\n\tbuffer_load_dwordx4 %0, %1, %2 offen sc1 lds" ::"v"(voff), "s"(srd), "s"(soff), "s"(ldsdst) : "memory");
}
DI void bdma4_sc1(u32x4 srd, unsigned voff, unsigned soff, unsigned ldsdst) {
  asm volatile("s_mov_b32 m0, %3\n\ts_nop 0\n\tbuffer_load_dwordx4 %0, %1, %2 offen sc1 lds\n\tbuffer_load_dwordx4 %0, %1, %2 offen offset:1024 sc1 lds\n\t"
               "buffer_load_dwordx4 %0, %1, %2 offen offset:2048 sc1 lds\n\tbuffer_load_dwordx4 %0, %1, %2 offen offset:3072 sc1 lds" ::"v"(voff), "s"(srd), "s"(soff), "s"(ldsdst) : "memory");
}
DI void bdma(u32x4 srd, unsigned voff, unsigned soff, unsigned ldsdst) {
  asm volatile("s_mov_b32 m0, %3\n\ts_nop 0\n\tbuffer_load_dwordx4 %0, %1, %2 offen lds" ::"v"(voff), "s"(srd), "s"(soff), "s"(ldsdst) : "memory");
}

#define P1_RD(n, NXT, S2N, OFF)                                                                                                    \
  ".if " #n " < 4\n\tds_read_b64_tr_b16 v[" #NXT "+16+4*" #n ":" #NXT "+17+4*" #n "], v[246+" #n "] offset:" #OFF "+8192*" #S2N "\n\t"    \
  "ds_read_b64_tr_b16 v[" #NXT "+18+4*" #n ":" #NXT "+19+4*" #n "], v[246+" #n "] offset:" #OFF "+8192*" #S2N "+1024\n\t"                \
  ".elseif " #n " < 8\n\tds_read_b128 v[" #NXT "+4*(" #n "-4):" #NXT "+4*(" #n "-4)+3], v[244+" #S2N "] offset:" #OFF "+4096*((" #n "-4)/2)+512*(" #n "&1)\n\t.endif\n\t"
#define P1_STEP(SET, NXT, S2N, OFF, S2)                                                                                            \
  ".set n_, 0\n\t.rept 16\n\t.set m_, n_/4\n\t.set e_, n_&3\n\t"                                                                    \
  ".if %c[tf]\n\tv_mfma_f32_16x16x32_bf16 v[96+4*n_:99+4*n_], v[" #SET "+16+4*e_:" #SET "+19+4*e_], v[" #SET "+4*m_:" #SET "+3+4*m_], v[96+4*n_:99+4*n_]\n\t" \
  ".else\n\tv_mfma_f32_16x16x32_bf16 v[96+4*n_:99+4*n_], v[" #SET "+4*m_:" #SET "+3+4*m_], v[" #SET "+16+4*e_:" #SET "+19+4*e_], v[96+4*n_:99+4*n_]\n\t.endif\n\t" \
  P1_RD(n_, NXT, S2N, OFF)                                                                                                         \
  ".set n_, n_+1\n\t.endr\n\t"                                                                                                     \
  "s_cmp_lg_u32 %[wm], " #S2 "\n\ts_cbranch_scc1 1f\n\t.set e_, 0\n\t.rept 4\n\t"                                                   \
  ".if %c[tf]\n\tv_mfma_f32_16x16x32_bf16 v[160+4*e_:163+4*e_], v[" #SET "+16+4*e_:" #SET "+19+4*e_], v[240:243], v[160+4*e_:163+4*e_]\n\t" \
  ".else\n\tv_mfma_f32_16x16x32_bf16 v[160+4*e_:163+4*e_], v[240:243], v[" #SET "+16+4*e_:" #SET "+19+4*e_], v[160+4*e_:163+4*e_]\n\t.endif\n\t" \
  ".set e_, e_+1\n\t.endr\n\t1:\n\t"
template <bool TF> DI void p1_periods(int wm, unsigned n2) {
  asm volatile(
      "2:\n\t"
      P1_STEP(176, 208, 1, 0, 0) "s_waitcnt lgkmcnt(0)\n\ts_barrier\n\t"
      P1_STEP(208, 176, 0, 32768, 1) "s_waitcnt lgkmcnt(0)\n\t"
      P1_STEP(176, 208, 1, 32768, 0) "s_waitcnt lgkmcnt(0)\n\ts_barrier\n\t"
      ".set i_, 244\n\t.rept 6\n\tv_xor_b32 v[i_], 0x10000, v[i_]\n\t.set i_, i_+1\n\t.endr\n\t"
      P1_STEP(208, 176, 0, 0, 1)
      "s_sub_u32 %[cnt], %[cnt], 1\n\ts_cmp_lg_u32 %[cnt], 0\n\ts_waitcnt lgkmcnt(0)\n\ts_cbranch_scc1 2b\n\t"
      "s_nop 7"
      : [cnt] "+s"(n2) : [wm] "s"(wm), [tf] "n"(TF ? 1 : 0) : "memory", "scc");
}
typedef __attribute__((address_space(3))) f32x4 lf32x4;
typedef __attribute__((address_space(3))) f32x2 lf32x2;

template <bool TF>
DI void p1_compute(u32x4 srd, __amdgpu_buffer_rsrc_t rsw, lds_ptr lds, int rb, int g, int w, int lane STAMP_PAR) {
  const int q = lane >> 4, i16 = lane & 15, wm = w >> 1, wn = w & 1;
  {
    const unsigned xa = 16384 + (64 * wm + 8 * (i16 >> 2) + (i16 & 3)) * 128 + ((q ^ (i16 >> 1)) * 16);
    const int p4 = lane & 3, qq = (lane >> 2) & 3;
    const unsigned wa = (8 * q + qq) * 256 + (((8 * wn + p4) ^ (2 * (qq + 4 * (q & 1)))) * 16) + 8 * (p4 >> 1);
    vset<244>(xa); vset<245>(xa ^ 64); vset<246>(wa); vset<247>(wa ^ 8); vset<248>(wa ^ 64); vset<249>(wa ^ 72);
  }
  asm volatile(".set i_, 96\n\t.rept 80\n\tv_mov_b32 v[i_], 0\n\t.set i_, i_+1\n\t.endr\n\t"
               "v_mov_b32 v240, 0x3f803f80\n\tv_mov_b32 v241, v240\n\tv_mov_b32 v242, v240\n\tv_mov_b32 v243, v240\n\t"
               "s_waitcnt lgkmcnt(0)\n\ts_barrier\n\t.set n_, 0\n\t.rept 8\n\t" P1_RD(n_, 176, 0, 0) ".set n_, n_+1\n\t.endr\n\ts_waitcnt lgkmcnt(0)" ::: "memory");
  STAMP(41);
  p1_periods<TF>(wm, 3);
  if (w == 0) asm volatile("buffer_load_dwordx4 v[250:253], %0, %1, %2 offen sc1" ::"v"(lane * 16), "s"(srd), "s"(OFF_RDY) : "memory");
  p1_periods<TF>(wm, 2);
  STAMP(42);
  f32x4 c[4];
  static_for<4>([&](auto E_) AI { c[E_.value] = vget4<160 + 4 * E_.value>(); *(lf32x4*)(lds + w * 4096 + E_.value * 1024 + lane * 16) = c[E_.value]; });
  asm volatile("s_waitcnt lgkmcnt(0)\n\ts_barrier" ::: "memory");
  STAMP(46);
#pragma unroll
  for (int e = 0; e < 4; ++e) c[e] += *(const lf32x4*)(lds + (w ^ 2) * 4096 + e * 1024 + lane * 16);
  const int kt0 = 4 * rb + 2 * wm;
  if (TF) {
    const bool isq = g < 5;
    const unsigned rec = isq ? QF_TILE : KV_BLK, hst = isq ? 64 * QF_TILE : 1280;
    const int r0 = 8 * (i16 >> 2) + (i16 & 3);
    f32x2 st[4];
#pragma unroll
    for (int mi = 0; mi < 4; ++mi) { st[mi] = *(const lf32x2*)(lds + 16384 + (4 * wm + mi) * 128 + i16 * 8); if (isq) st[mi][1] *= QSCALE; }
    static_for<2>([&](auto P_) AI {
      constexpr int P = P_.value;
      const int cq = 16 * (isq ? g : g - 5) + 8 * wn + 4 * P + q, i5 = (cq * 205) >> 10, m5 = cq - 5 * i5;
      const int hd = 2 * i5 + (m5 > 2);
      const unsigned base = (isq ? OFF_QF : OFF_KV) + hd * hst + kt0 * rec;
      const unsigned oct = base + (m5 > 2 ? m5 - 3 : m5) * 512, tail = base + 1024 + (8 * (i16 >> 2) + 2 * (i16 & 3)) * 8;
      const unsigned ad0 = m5 == 2 ? tail : oct + (isq ? r0 : swap23(r0)) * 16, ad1 = m5 == 2 ? tail + hst : oct + (isq ? r0 + 4 : swap23(r0 + 4)) * 16;
      static_for<2>([&](auto A_) AI {
        constexpr int a = A_.value;
        u32x4 o[2];
        static_for<2>([&](auto B_) AI {
          constexpr int mi = 2 * a + B_.value;
          const f32x4 a0 = acc_sub<96 + 16 * mi + 8 * P>(st[mi][0], c[2 * P]) * st[mi][1], a1 = acc_sub<100 + 16 * mi + 8 * P>(st[mi][0], c[2 * P + 1]) * st[mi][1];
          const unsigned p0 = pack2(a0[0], a0[1]), p1 = pack2(a0[2], a0[3]), p2 = pack2(a1[0], a1[1]), p3 = pack2(a1[2], a1[3]);
          o[B_.value] = q < 2 ? u32x4{p0, p1, p2, p3} : u32x4{p2, p3, p0, p1};
        });
        const bool tl = m5 == 2;
        st_sc1(tl ? u32x4{o[0][0], o[0][1], o[1][0], o[1][1]} : o[0], rsw, ad0, a * rec);
        st_sc1(tl ? u32x4{o[0][2], o[0][3], o[1][2], o[1][3]} : o[1], rsw, ad1, a * rec);
      });
    });
  } else {
    const int p4 = i16 >> 2, cl = 64 * wn + 8 * p4 + 4 * (p4 >> 1) + (i16 & 3), cd = p4 < 2 ? 4 : -4;
    static_for<2>([&](auto A_) AI {
      constexpr int a = A_.value;
      lds_cptr sp = (lds_cptr)lds + 16384 + (4 * wm + 2 * a) * 128 + q * 32;
      const f32x4 s0 = *(const lf32x4*)(sp), s1 = *(const lf32x4*)(sp + 16), s2 = *(const lf32x4*)(sp + 128), s3 = *(const lf32x4*)(sp + 144);
      const f32x4 mA = {s0[0], s0[2], s1[0], s1[2]}, rA = {s0[1], s0[3], s1[1], s1[3]}, mB = {s2[0], s2[2], s3[0], s3[2]}, rB = {s2[1], s2[3], s3[1], s3[3]};
      static_for<4>([&](auto E_) AI {
        constexpr int e = E_.value;
        const float cs = c[e][0];
        const f32x4 a0 = acc_sub<96 + 32 * a + 4 * e>(cs, mA) * rA, a1 = acc_sub<112 + 32 * a + 4 * e>(cs, mB) * rB;
        st_sc1(u32x4{pack2(a0[0], a0[1]), pack2(a0[2], a0[3]), pack2(a1[0], a1[1]), pack2(a1[2], a1[3])}, rsw, q * 10240 + (cl + 32 * (e >> 1) + ((e & 1) ? cd : 0)) * 16,
               OFF_KV + ((kt0 & 32) | ((kt0 + a + 1) & 31)) * KV_BLK + 40960 + (g - 10) * 2048);
      });
    });
  }
}

#define P1_DEPTH 4
struct P1Load { u32x4 srd; unsigned vo[4], pstep, hstep, dst; int rot; };
DI void p1_issue(const P1Load& L, int p) {
  int pp = p + L.rot; pp -= pp >= 10 ? 10 : 0;
  const unsigned so = pp * L.pstep, d = (p & 3) * 32768 + L.dst;
#pragma unroll
  for (int i = 0; i < 8; ++i) bdma(L.srd, L.vo[i & 3], so + (i >> 2) * L.hstep, d + i * 1024);
}
DI P1Load p1_start(const char* x, const char* wqkv, int rb, int g, int rot, int lw, int lane) {
  const int q = lane >> 4, i16 = lane & 15, isx = lw >> 1, hf = lw & 1;
  P1Load L;
  L.srd = isx ? mk_srd(x, S_ * D_ * 2) : mk_srd(wqkv, D_ * 3 * D_ * 2);
#pragma unroll
  for (int i = 0; i < 4; ++i)
    L.vo[i] = isx ? (unsigned)((128 * rb + 64 * hf + 8 * i + (lane >> 3)) * 1280 + 16 * ((lane & 7) ^ (2 * i + ((lane >> 4) & 1))))
                  : (unsigned)((32 * hf + 4 * i + q) * 3840 + 256 * g + 16 * (i16 ^ (2 * (q + 4 * (i >> 1)))));
  L.pstep = isx ? 128 : 64 * 3840; L.hstep = isx ? 32 * 1280 : 16 * 3840; L.dst = lw * 8192; L.rot = rot;
  p1_issue(L, 0); p1_issue(L, 1);
  if (P1_DEPTH == 4) p1_issue(L, 2);
  return L;
}
template <class Fn> DI void p1_stats(const char* x, __amdgpu_buffer_rsrc_t rsw, int rb, int w, int lane, Fn landed) {
  const char* p = x + (size_t)(128 * rb + 16 * w + (lane >> 3)) * 1280 + (lane & 7) * 16;
  u32x4 v[2][10];
#pragma unroll
  for (int h = 0; h < 2; ++h)
#pragma unroll
    for (int k = 0; k < 10; ++k) v[h][k] = *(const u32x4*)(p + h * 8 * 1280 + k * 128);
  asm volatile("s_waitcnt vmcnt(20)" ::: "memory");
  landed();
#pragma unroll
  for (int h = 0; h < 2; ++h) {
    f32x4 sm4 = {0.f, 0.f, 0.f, 0.f}, sq4 = sm4;
#pragma unroll
    for (int k = 0; k < 10; ++k)
#pragma unroll
      for (int d = 0; d < 4; ++d) { sm4[d] = dot2(v[h][k][d], 0x3f803f80u, sm4[d]); sq4[d] = dot2(v[h][k][d], v[h][k][d], sq4[d]); }
    float sm = (sm4[0] + sm4[1]) + (sm4[2] + sm4[3]), sq = (sq4[0] + sq4[1]) + (sq4[2] + sq4[3]);
    sm = sum8(sm); sq = sum8(sq);
    const float mean = sm * (1.f / D_);
    const float rstd = __builtin_amdgcn_rsqf(fmaxf(sq * (1.f / D_) - mean * mean, 0.f) + LN_EPS);
    const int t = 16 * w + 8 * h + (lane >> 3);
    st_sc1(u32x2{__float_as_uint(mean), __float_as_uint(rstd)}, rsw, (lane & 7) == 0 ? (2 * (t >> 5) + ((t >> 2) & 1)) * 128 + (4 * ((t >> 3) & 3) + (t & 3)) * 8 : 0x80000000u, OFF_XST + rb * 1024);
  }
  st_sc1(u32x4{0x3f803f80u, 0x3f803f80u, 0x3f803f80u, 0x3f803f80u}, rsw, (lane == 0 && w < 4) ? 81920 : 0x80000000u, OFF_KV + (4 * rb + (w & 3)) * KV_BLK);
  asm volatile("s_waitcnt vmcnt(0) lgkmcnt(0)\n\ts_barrier" ::: "memory");
}

constexpr int KV_HALF = 11 * 1024, KV_SLOT = 2 * KV_HALF;
constexpr int L_KV0 = 4 * GRP_B, L_WRAP = 144 * 1024, L_CS1 = 6 * GRP_B, L_CS3 = L_CS1 + 320, L_FILL = L_CS3 + 960, L_CSF = L_FILL + 4, L_EPO = L_FILL + 12,
              L_OWN = L_FILL + 16, L_CONS = L_FILL + 64, L_SCR2 = L_CONS + 256, K2_LDS = L_SCR2 + 6144 + 512;
constexpr int kv_at(int c) { return (c < 2 || c > 5) ? L_KV0 + (c & 1) * KV_SLOT : (c - 2) * KV_SLOT; }
#define PRIO_H 0
#define PACE 0
#define PSL 3
#define DLY16 0
#define POSTP 14
#define STASH 0
constexpr unsigned SPIN_CAP = 20000, LDS_SPIN = 0x20000;

DI u32x4 ld_sc1(__amdgpu_buffer_rsrc_t rs, unsigned voff, unsigned soff) { return __builtin_amdgcn_raw_buffer_load_b128(rs, voff, soff, AUX_SC1); }
DI unsigned nosent(unsigned u) { return min(u, SENT - 1u); }
DI unsigned umax3(unsigned a, unsigned b, unsigned c) { return max(max(a, b), c); }
DI int tail_slot(int tokn) { return (tokn & 24) | ((tokn & 3) << 1) | ((tokn >> 2) & 1); }
DI unsigned kv_src(int n, int head) {
  const int pl = ((n - 80) * 745) >> 14, row = n - 80 - 22 * pl;
  return n < 80 ? head * 1280 + n * 16 : row < 20 ? 40960 + pl * 10240 + (20 * head + (row < 16 ? swap23(row) : row)) * 16 : 81920;
}
DI unsigned kv_cell(int n, int half, int head) { const int rec = (n * 1561) >> 18; return (32 * half + min(rec, 3)) * KV_BLK + kv_src(n - 168 * rec, head); }
DI f32x16 attn_safe(__amdgpu_buffer_rsrc_t rs, unsigned qp, int head, int lane) {
  const int hi = lane >> 5, r32 = lane & 31;
  const bf16x8 q1 = __builtin_bit_cast(bf16x8, ld_sc1(rs, hi * 512 + r32 * 16, qp));
  const u32x2 q2l = __builtin_amdgcn_raw_buffer_load_b64(rs, 1024 + tail_slot(r32) * 8, qp, AUX_SC1);
  const bf16x8 q2 = __builtin_bit_cast(bf16x8, u32x4{hi ? 0u : q2l[0], hi ? 0u : q2l[1], 0u, 0u});
  f32x16 O = {};
  float m = -3.0e38f;
#pragma unroll 1
  for (int t = 0; t < 64; ++t) {
    const unsigned kb = OFF_KV + t * KV_BLK + head * 1280, vb = OFF_KV + ((t & 32) | ((t + 1) & 31)) * KV_BLK;
    const bf16x8 k1 = __builtin_bit_cast(bf16x8, ld_sc1(rs, hi * 512 + r32 * 16, kb));
    const u32x2 k2l = __builtin_amdgcn_raw_buffer_load_b64(rs, 1024 + tail_slot(swap23(r32)) * 8, kb, AUX_SC1);
    const bf16x8 v0 = __builtin_bit_cast(bf16x8, ld_sc1(rs, kv_src(80 + 22 * hi + min(r32, 21), head), vb)),
                 v1 = __builtin_bit_cast(bf16x8, ld_sc1(rs, kv_src(124 + 22 * hi + min(r32, 21), head), vb));
    f32x16 sc = MFMA32(k1, q1, f32x16{});
    sc = MFMA32(__builtin_bit_cast(bf16x8, u32x4{k2l[0], k2l[1], 0u, 0u}), q2, sc);
    float tm = sc[0];
#pragma unroll
    for (int i = 1; i < 16; ++i) tm = fmaxf(tm, sc[i]);
    { auto rr = __builtin_amdgcn_permlane32_swap(__float_as_uint(tm), __float_as_uint(tm), false, false); tm = fmaxf(__uint_as_float(rr[0]), __uint_as_float(rr[1])); }
    const float mn = fmaxf(m, tm), al = __builtin_amdgcn_exp2f(m - mn);
    m = mn;
    u32x4 p0, p1;
#pragma unroll
    for (int i = 0; i < 4; ++i) {
      p0[i] = pack2(__builtin_amdgcn_exp2f(sc[2 * i] - mn), __builtin_amdgcn_exp2f(sc[2 * i + 1] - mn));
      p1[i] = pack2(__builtin_amdgcn_exp2f(sc[8 + 2 * i] - mn), __builtin_amdgcn_exp2f(sc[9 + 2 * i] - mn));
    }
#pragma unroll
    for (int i = 0; i < 16; ++i) O[i] *= al;
    O = MFMA32(v0, __builtin_bit_cast(bf16x8, p0), O);
    O = MFMA32(v1, __builtin_bit_cast(bf16x8, p1), O);
  }
  return O;
}

constexpr int R_A = 128;
template <int A0, int N> DI void ld_frags(u32x4 srd, unsigned voff, unsigned soff) {
  unsigned t;
  asm volatile("s_add_u32 %0, %3, 4096\n\t.set i_, 0\n\t.rept %c5\n\t"
               ".if i_ < 4\n\tbuffer_load_dwordx4 v[%c4+4*i_:%c4+4*i_+3], %1, %2, %3 offen offset:1024*i_ sc1\n\t"
               ".else\n\tbuffer_load_dwordx4 v[%c4+4*i_:%c4+4*i_+3], %1, %2, %0 offen sc1\n\t.endif\n\t.set i_, i_+1\n\t.endr"
               : "=&s"(t) : "v"(voff), "s"(srd), "s"(soff), "n"(R_A + 4 * A0), "n"(N) : "memory", "scc");
}
DI void ld_unit(u32x4 srd, const unsigned (&vo)[4], unsigned soff) {
  ld_frags<0, 5>(srd, vo[0], soff); ld_frags<5, 5>(srd, vo[1], soff + 5120); ld_frags<10, 5>(srd, vo[2], soff + 10240); ld_frags<15, 5>(srd, vo[3], soff + 15360);
}
DI void ld_lead(u32x4 srd, const unsigned (&vo)[4], unsigned soff) { ld_frags<0, 5>(srd, vo[0], soff); ld_frags<5, 3>(srd, vo[1], soff + 5120); }
template <int G> DI void w_prime(const unsigned (&wb)[6]) {
  unsigned u;
  asm volatile("s_mov_b32 %0, 0\n\t1:\n\tds_read_b32 v254, %2\n\ts_add_u32 %0, %0, 1\n\ts_cmp_gt_u32 %0, 0x20000\n\ts_waitcnt lgkmcnt(0)\n\tv_cmp_gt_u32 vcc, %c3+1, v254\n\t"
               "s_cbranch_scc1 2f\n\ts_cbranch_vccnz 1b\n\t2:\n\t"
               ".set i_, 0\n\t.rept 5\n\tds_read_b128 v[208+4*i_:211+4*i_], %1 offset:%c4+1024*i_\n\t.set i_, i_+1\n\t.endr\n\ts_waitcnt lgkmcnt(0)"
               : "=&s"(u) : "v"(wb[(G % 6) >> 1]), "v"(wb[3]), "n"(G), "n"(((G % 6) & 1) * GRP_B) : "memory", "scc", "vcc");
}
#define GST_A
#define GST_B
#define GST_CL
constexpr unsigned long long vmw(int a, int b = 63, int c = 63, int d = 63, int e = 63, int f = 63) {
  return (unsigned long long)a | (unsigned long long)b << 6 | (unsigned long long)c << 12 | (unsigned long long)d << 18 | (unsigned long long)e << 24 | (unsigned long long)f << 30;
}
template <int G0, int NG, int KS0, int KSE, unsigned long long VMW, int LEAD, int NRL, int XG, int XOP, int AVM = -1, bool TAIL = true>
DI unsigned long long run_unit(const unsigned (&wb)[6], u32x4 srd, unsigned voff, unsigned soff, unsigned xoff, unsigned aoff,
                               u32x4 asrd, const unsigned (&vq)[4], unsigned aso STAMP_PAR) {
  unsigned long long m; unsigned u, so;
#define DBG_INC
#define DBG_OP
  asm volatile(
      "s_mov_b32 %[so], %[soff]\n\t"
      ".set gi_, 0\n\t.rept %c[ng]\n\t"
      ".set g_, %c[g0]+gi_\n\t.set gn_, g_+1\n\t.if gn_ > 34\n\t.set gn_, 34\n\t.endif\n\t"
      ".set sc_, g_-6*(g_/6)\n\t.set sn_, gn_-6*(gn_/6)\n\t.set nk_, %c[kse]-%c[ks0]-5*gi_\n\t.if nk_ > 5\n\t.set nk_, 5\n\t.endif\n\t"
      "ds_read_b32 v254, %[fl]\n\t" GST_A
      ".set vw_, (%c[vmw]>>(6*gi_))&63\n\t.if vw_ < 63\n\ts_waitcnt vmcnt(vw_)\n\t.endif\n\t"
      GST_B ".set k_, 0\n\t.rept nk_\n\t"
      ".set kr_, 5*gi_+k_\n\t.set ks_, %c[ks0]+kr_\n\t.set a_, 128+4*(ks_-20*(ks_/20))\n\t"
      ".if %c[lead]\n\t.set vw_, 19-ks_\n\t.if vw_ > %c[lead]-1\n\t.set vw_, %c[lead]-1\n\t.endif\n\t.if %c[xop] == 1 && gi_ > %c[xg]\n\t.set vw_, vw_+1\n\t.endif\n\ts_waitcnt vmcnt(vw_)\n\t.endif\n\t"
      ".set last_, %c[lead] && ks_ == 19 && g_ < 12\n\t"
      ".if k_ == nk_-1 && !last_\n\t"
      "s_cbranch_vccnz 3f\n\t2:\n\t.subsection 1\n\t3:\n\ts_mov_b32 %[u], 0\n\t"
      "1:\n\t" DBG_INC "s_sleep 1\n\tds_read_b32 v254, %[fl]\n\ts_add_u32 %[u], %[u], 1\n\ts_cmp_gt_u32 %[u], 0x20000\n\ts_waitcnt lgkmcnt(0)\n\tv_cmp_gt_u32 vcc, gn_+1, v254\n\t"
      "s_cbranch_scc1 2b\n\ts_cbranch_vccnz 1b\n\ts_branch 2b\n\t.subsection 0\n\t"
      ".endif\n\t"
      ".set t_, 0\n\t.rept 5\n\t"
      ".set f_, 5*k_+t_\n\t"
      ".if last_ && t_ >= 2\n\t.if (t_ & 1) == 0\n\ts_waitcnt lgkmcnt((4-t_)/2)\n\t.endif\n\t"
      ".elseif f_ == 0 || f_ == 2\n\ts_waitcnt lgkmcnt(5)\n\t.elseif (f_ & 1) == 0\n\ts_waitcnt lgkmcnt(3)\n\t.endif\n\t"
      ".if ks_ == 0\n\tv_mfma_f32_16x16x32_bf16 v[228+4*t_:231+4*t_], v[208+4*t_:211+4*t_], v[a_:a_+3], 0\n\t"
      ".else\n\tv_mfma_f32_16x16x32_bf16 v[228+4*t_:231+4*t_], v[208+4*t_:211+4*t_], v[a_:a_+3], v[228+4*t_:231+4*t_]\n\t.endif\n\t"
      ".set s_, sc_\n\t.set o_, 5120*(k_+1)+1024*t_\n\t.if k_ == nk_-1\n\t.set s_, sn_\n\t.set o_, 1024*t_\n\t.endif\n\t"
      ".if last_\n\t.elseif s_ < 2\n\tds_read_b128 v[208+4*t_:211+4*t_], %[w0] offset:25600*s_+o_\n\t"
      ".elseif s_ < 4\n\tds_read_b128 v[208+4*t_:211+4*t_], %[w1] offset:25600*(s_-2)+o_\n\t"
      ".else\n\tds_read_b128 v[208+4*t_:211+4*t_], %[w2] offset:25600*(s_-4)+o_\n\t.endif\n\t"
      ".if k_ == nk_-2 && t_ == 3\n\tv_cmp_gt_u32 vcc, gn_+1, v254\n\t.endif\n\t"
      ".set t_, t_+1\n\t.endr\n\t"
      ".set fn_, ks_+%c[lead]\n\t.if %c[lead] && fn_ < 20\n\t"
      ".if fn_ < 10\n\tbuffer_load_dwordx4 v[128+4*fn_:131+4*fn_], %[q1], %[asrd], %[aso] offen offset:1024*(fn_&3) sc1\n\t"
      ".elseif fn_ < 15\n\tbuffer_load_dwordx4 v[128+4*fn_:131+4*fn_], %[q2], %[asrd], %[aso] offen offset:1024*(fn_&3) sc1\n\t"
      ".else\n\tbuffer_load_dwordx4 v[128+4*fn_:131+4*fn_], %[q3], %[asrd], %[aso] offen offset:1024*(fn_&3) sc1\n\t.endif\n\t"
      ".if (fn_&3) == 3\n\ts_addk_i32 %[aso], 4096\n\t.endif\n\t.endif\n\t"
      ".if kr_ < %c[nrl]\n\t"
      "buffer_load_dwordx4 v[a_:a_+3], %[voff], %[srd], %[so] offen offset:1024*(kr_&3) sc1\n\t"
      ".if (kr_&3) == 3\n\ts_addk_i32 %[so], 4096\n\t.endif\n\t"
      ".endif\n\t"
      ".set k_, k_+1\n\t.endr\n\t"
      "ds_add_u32 %[cs], %[one]\n\t"
      ".if gi_ == 0 && %c[avm] >= 0\n\ts_waitcnt vmcnt(%c[avm])\n\ts_mov_b64 %[m], exec\n\ts_mov_b64 exec, 0xff\n\tv_mov_b32 v125, 1\n\tbuffer_atomic_add v125, %[ao], %[srd], 0 offen sc1\n\ts_mov_b64 exec, %[m]\n\t.endif\n\t"
      ".if gi_ == %c[xg] && %c[xop] == 1\n\tbuffer_load_dwordx4 v[250:253], %[voff], %[srd], %[so] offen sc1\n\t.endif\n\t"
      ".if gi_ == %c[xg] && %c[xop] == 2\n\ts_mov_b64 %[m], exec\n\ts_mov_b64 exec, 1\n\tv_mov_b32 v124, 0\n\tbuffer_atomic_add v124, %[xo], %[srd], 0 offen sc0 sc1\n\ts_mov_b64 exec, %[m]\n\t.endif\n\t"
      ".set gi_, gi_+1\n\t.endr\n\t"
      ".if %c[xop] > 0\n\ts_waitcnt vmcnt(0)\n\t.endif\n\t"
      ".if %c[tail]\n\ts_nop 4\n\tv_cmp_u_f32 %[m], v228, v228\n\t.endif"
      : [m] "=&s"(m), [u] "=&s"(u), [so] "=&s"(so), [aso] "+s"(aso) DBG_OP
      : [w0] "v"(wb[0]), [w1] "v"(wb[1]), [w2] "v"(wb[2]), [fl] "v"(wb[3]), [cs] "v"(wb[4]), [one] "v"(wb[5]), [voff] "v"(voff), [xo] "v"(xoff), [ao] "v"(aoff), [avm] "n"(AVM), [tail] "n"(TAIL ? 1 : 0), [srd] "s"(srd), [soff] "s"(soff),
        [g0] "n"(G0), [ng] "n"(NG), [ks0] "n"(KS0), [kse] "n"(KSE), [vmw] "n"(VMW), [lead] "n"(LEAD), [q1] "v"(vq[1]), [q2] "v"(vq[2]), [q3] "v"(vq[3]), [asrd] "s"(asrd), [nrl] "n"(NRL), [xg] "n"(XG), [xop] "n"(XOP)
      : "memory", "scc", "vcc" GST_CL);
  return m;
}
template <bool RELOAD> DI void redo_unit(__amdgpu_buffer_rsrc_t ra, const unsigned (&vo)[4], unsigned soff, int nks, __amdgpu_buffer_rsrc_t rw, unsigned woff, u32x4 srd_a) {
  f32x4 acc[5];
#pragma unroll
  for (int t = 0; t < 5; ++t) acc[t] = f32x4{0.f, 0.f, 0.f, 0.f};
  asm volatile("s_waitcnt vmcnt(0)" ::: "memory");
#pragma unroll 1
  for (int ks = 0; ks < nks; ++ks) {
    const unsigned va = ks < 5 ? vo[0] : ks < 10 ? vo[1] : ks < 15 ? vo[2] : vo[3];
    u32x4 av;
#pragma unroll 1
    for (unsigned sp = 0; sp < SPIN_CAP; ++sp) {
      av = ld_sc1(ra, va, soff + ks * 1024);
      if (!__any(umax3(max(av[0], av[1]), av[2], av[3]) == SENT)) break;
      asm volatile("s_sleep 8" ::: "memory");
    }
#pragma unroll
    for (int t = 0; t < 5; ++t) acc[t] = MFMA16(__builtin_bit_cast(bf16x8, ld_sc1(rw, woff, (ks * 5 + t) * 1024)), __builtin_bit_cast(bf16x8, av), acc[t]);
  }
  static_for<20>([&](auto I_) AI { vset<228 + I_.value>(__float_as_uint(acc[I_.value >> 2][I_.value & 3])); });
  if (RELOAD) { ld_unit(srd_a, vo, soff); asm volatile("s_waitcnt vmcnt(0)" ::: "memory"); }
}

#define EP_PACK(d, r) "v_cvt_pk_bf16_f32 v[" #d "], v[" #r "], v[" #r "+1]\n\tv_cvt_pk_bf16_f32 v[" #d "+1], v[" #r "+2], v[" #r "+3]\n\t"
#define EP_GUARD(d) "v_min_u32 v[" #d "], -2, v[" #d "]\n\tv_min_u32 v[" #d "+1], -2, v[" #d "+1]\n\t"
#define EP_GELU(R, NP)                                                                                                                       \
  ".set i_, 0\n\t.rept " #NP "\n\tv_pk_mul_f32 v[116+2*i_:117+2*i_], v[" #R "+2*i_:" #R "+1+2*i_], v[" #R "+2*i_:" #R "+1+2*i_]\n\t.set i_, i_+1\n\t.endr\n\t" \
  ".set i_, 0\n\t.rept " #NP "\n\tv_pk_fma_f32 v[116+2*i_:117+2*i_], v[116+2*i_:117+2*i_], %[B], %[A]\n\t.set i_, i_+1\n\t.endr\n\t"          \
  ".set i_, 0\n\t.rept " #NP "\n\tv_pk_mul_f32 v[116+2*i_:117+2*i_], v[116+2*i_:117+2*i_], v[" #R "+2*i_:" #R "+1+2*i_]\n\t.set i_, i_+1\n\t.endr\n\t" \
  ".set i_, 0\n\t.rept 2*" #NP "\n\tv_exp_f32 v[116+i_], v[116+i_]\n\t.set i_, i_+1\n\t.endr\n\t"                                           \
  ".set i_, 0\n\t.rept " #NP "\n\tv_pk_fma_f32 v[116+2*i_:117+2*i_], v[116+2*i_:117+2*i_], %[I], %[I]\n\t.set i_, i_+1\n\t.endr\n\t"          \
  ".set i_, 0\n\t.rept 2*" #NP "\n\tv_rcp_f32 v[116+i_], v[116+i_]\n\t.set i_, i_+1\n\t.endr\n\t"                                           \
  ".set i_, 0\n\t.rept " #NP "\n\tv_pk_mul_f32 v[" #R "+2*i_:" #R "+1+2*i_], v[" #R "+2*i_:" #R "+1+2*i_], v[116+2*i_:117+2*i_]\n\t.set i_, i_+1\n\t.endr\n\t"
struct GeluK { f32x2 nm, A, B, I; };
DI GeluK gelu_consts(float mean, float r, float ir) {
  const float c0 = -2.f * 1.4426950408889634f * 0.7978845608028654f, c1 = c0 * 0.044715f, A = c0 * r, B = c1 * r * r * r;
  return GeluK{f32x2{-mean, -mean}, f32x2{A, A}, f32x2{B, B}, f32x2{ir, ir}};
}
template <int T, int NT, int D> DI void ep_gelu(const GeluK& k, f32x4 c0, f32x4 c1) {
  asm volatile(
      "v_pk_fma_f32 v[%c[r]:%c[r]+1], %[nm], %[ca], v[%c[r]:%c[r]+1]\n\tv_pk_fma_f32 v[%c[r]+2:%c[r]+3], %[nm], %[cb], v[%c[r]+2:%c[r]+3]\n\t"
      ".if %c[nt] == 2\n\tv_pk_fma_f32 v[%c[r]+4:%c[r]+5], %[nm], %[cc], v[%c[r]+4:%c[r]+5]\n\tv_pk_fma_f32 v[%c[r]+6:%c[r]+7], %[nm], %[cd], v[%c[r]+6:%c[r]+7]\n\t.endif\n\t"
      EP_GELU(%c[r], 2*%c[nt])
      EP_PACK(%c[d], %c[r]) ".if %c[nt] == 2\n\t" EP_PACK(%c[d]+2, %c[r]+4) ".endif\n\t"
      EP_GUARD(%c[d]) ".if %c[nt] == 2\n\t" EP_GUARD(%c[d]+2) ".endif"
      ::[nm] "v"(k.nm), [A] "v"(k.A), [B] "v"(k.B), [I] "v"(k.I), [ca] "v"(f32x2{c0[0], c0[1]}), [cb] "v"(f32x2{c0[2], c0[3]}), [cc] "v"(f32x2{c1[0], c1[1]}),
        [cd] "v"(f32x2{c1[2], c1[3]}), [r] "n"(228 + 4 * T), [nt] "n"(NT), [d] "n"(D));
}
template <int R> DI void st_frag(u32x4 srd, unsigned voff, unsigned soff) { asm volatile("buffer_store_dwordx4 v[%c3:%c3+3], %0, %1, %2 offen sc1\n\ts_nop 1" ::"v"(voff), "s"(srd), "s"(soff), "n"(R) : "memory"); }
template <int R> DI void st_single(u32x4 srd, unsigned vsg, unsigned soff) {
  asm volatile("v_mov_b32 v[%c3+2], v[%c3]\n\tv_mov_b32 v[%c3+3], v[%c3+1]\n\tv_mov_b32 v248, v[%c3]\n\tv_mov_b32 v249, v[%c3+1]\n\ts_nop 1\n\t"
               "v_permlane16_swap_b32 v248, v[%c3+2]\n\tv_permlane16_swap_b32 v249, v[%c3+3]\n\t"
               "buffer_store_dwordx4 v[%c3:%c3+3], %0, %1, %2 offen sc1\n\ts_nop 1" ::"v"(vsg), "s"(srd), "s"(soff), "n"(R) : "memory");
}
template <int R> DI void st_tile(u32x4 srd, unsigned lane16, unsigned vsg, unsigned sp, unsigned ssg) {
  asm volatile(EP_PACK(116, %c0) EP_PACK(118, %c0+4) EP_GUARD(116) EP_GUARD(118) EP_PACK(120, %c0+8) EP_PACK(122, %c0+12) ::"n"(R));
  st_frag<116>(srd, lane16, sp);
  asm volatile(EP_GUARD(120) EP_GUARD(122) EP_PACK(250, %c0+16) ::"n"(R));
  st_frag<120>(srd, lane16, sp + 1024);
  asm volatile(EP_GUARD(250));
  st_single<250>(srd, vsg, ssg);
}
template <int R> DI void st_stats(__amdgpu_buffer_rsrc_t rs, unsigned vst, unsigned off) {
  float s, ss;
  asm volatile("v_pk_add_f32 v[116:117], v[%c2:%c2+1], v[%c2+2:%c2+3]\n\tv_pk_mul_f32 v[118:119], v[%c2:%c2+1], v[%c2:%c2+1]\n\tv_pk_mul_f32 v[120:121], v[%c2+2:%c2+3], v[%c2+2:%c2+3]\n\t"
               ".set i_, 2\n\t.rept 8\n\tv_pk_add_f32 v[116:117], v[116:117], v[%c2+2*i_:%c2+2*i_+1]\n\t"
               "v_pk_fma_f32 v[118+2*(i_&1):119+2*(i_&1)], v[%c2+2*i_:%c2+2*i_+1], v[%c2+2*i_:%c2+2*i_+1], v[118+2*(i_&1):119+2*(i_&1)]\n\t.set i_, i_+1\n\t.endr\n\t"
               "v_pk_add_f32 v[118:119], v[118:119], v[120:121]\n\tv_add_f32 %0, v116, v117\n\tv_add_f32 %1, v118, v119"
               : "=v"(s), "=v"(ss) : "n"(R));
  s = sum_q(s); ss = sum_q(ss);
  st_sc1(u32x2{nosent(__float_as_uint(s)), nosent(__float_as_uint(ss))}, rs, vst, off);
}
DI void read_stats(__amdgpu_buffer_rsrc_t rs, unsigned voff, unsigned soff, float& mean, float& var) {
  u32x4 st = __builtin_bit_cast(u32x4, vget4<250>());
  if (__builtin_expect(__any(umax3(max(st[0], st[1]), st[2], st[3]) == SENT), 0)) {
#pragma unroll 1
    for (unsigned sp = 0; sp < SPIN_CAP; ++sp) {
      asm volatile("s_sleep 8" ::: "memory");
      st = ld_sc1(rs, voff, soff);
      if (!__any(umax3(max(st[0], st[1]), st[2], st[3]) == SENT)) break;
    }
  }
  const float s = sum_q(__uint_as_float(st[0]) + __uint_as_float(st[2])), ss = sum_q(__uint_as_float(st[1]) + __uint_as_float(st[3]));
  mean = s * (1.f / D_);
  var = fmaxf(ss * (1.f / D_) - mean * mean, 0.f);
}
template <unsigned long long MASK> DI void arrive(u32x4 srd, unsigned voff) {
  unsigned long long sv;
  asm volatile("s_mov_b64 %0, exec\n\ts_mov_b64 exec, %3\n\tv_mov_b32 v249, 1\n\tbuffer_atomic_add v249, %1, %2, 0 offen sc1\n\ts_mov_b64 exec, %0"
               : "=&s"(sv) : "v"(voff), "s"(srd), "n"(MASK) : "memory");
}
template <unsigned long long MASK> DI void await(u32x4 srd, unsigned voff, unsigned expect) {
  unsigned long long sv; unsigned n;
#define POLL(R) "s_waitcnt vmcnt(2)\n\tv_cmp_ne_u32 vcc, %4, " #R "\n\ts_cbranch_vccz 2f\n\tv_mov_b32 " #R ", 0\n\tbuffer_atomic_add " #R ", %2, %3, 0 offen sc0 sc1\n\t"
  asm volatile("s_mov_b64 %0, exec\n\ts_mov_b64 exec, %5\n\ts_mov_b32 %1, 0\n\tv_mov_b32 v126, 0\n\tv_mov_b32 v127, 0\n\tv_mov_b32 v248, 0\n\t"
               "buffer_atomic_add v126, %2, %3, 0 offen sc0 sc1\n\ts_sleep %c6\n\tbuffer_atomic_add v127, %2, %3, 0 offen sc0 sc1\n\ts_sleep %c6\n\tbuffer_atomic_add v248, %2, %3, 0 offen sc0 sc1\n\t"
               "1:\n\t" POLL(v126) POLL(v127) POLL(v248)
               "s_add_u32 %1, %1, 1\n\ts_cmp_lt_u32 %1, 0x8000\n\ts_cbranch_scc1 1b\n\t"
               "2:\n\ts_mov_b64 exec, %0"
               : "=&s"(sv), "=&s"(n) : "v"(voff), "s"(srd), "v"(expect), "n"(MASK), "n"(PSL) : "memory", "scc", "vcc");
#undef POLL
}

template <int R> DI void warm_code(int part, int lane) {
  unsigned long long pc; unsigned len;
  asm volatile("s_getpc_b64 %0\n\t1:\n\ts_mov_b32 %1, .Lk_code_end-1b" : "=s"(pc), "=s"(len));
  asm volatile("global_load_dword v[%c2], %0, %1" ::"v"(min((unsigned)(part * 64 + lane) * 128u, len - 4u)), "s"(pc), "n"(R) : "memory");
}
DI void ld_group(u32x4 srd, unsigned voff, unsigned soff) {
  asm volatile(".set i_, 0\n\t.rept 25\n\tbuffer_load_dwordx4 v[128+4*i_:131+4*i_], %1, %2, %0 offen offset:1024*(i_&3) sc1\n\t.if (i_&3) == 3\n\ts_addk_i32 %0, 4096\n\t.endif\n\t.set i_, i_+1\n\t.endr"
               : "+s"(soff) : "v"(voff), "s"(srd) : "memory", "scc");
}
DI void st_group(unsigned a) {
  asm volatile(".set i_, 0\n\t.rept 25\n\tds_write_b128 %0, v[128+4*i_:131+4*i_] offset:1024*i_\n\t.set i_, i_+1\n\t.endr\n\ts_waitcnt lgkmcnt(0)" ::"v"(a) : "memory");
}
template <int R> DI void ld_cs(u32x4 srd, unsigned voff, unsigned step) {
  asm volatile(".set i_, 0\n\t.rept 20\n\tbuffer_load_dwordx4 v[%c3+4*i_:%c3+4*i_+3], %0, %1, 0 offen sc1\n\tv_add_u32 %0, %0, %2\n\t.set i_, i_+1\n\t.endr" : "+v"(voff) : "s"(srd), "v"(step), "n"(R) : "memory");
}
template <int R> DI void all_flags(__amdgpu_buffer_rsrc_t rs, unsigned off, unsigned epoch, unsigned lane16) {
  u32x4 f = __builtin_bit_cast(u32x4, vget4<R>());
#pragma unroll 1
  for (unsigned sp = 0; sp < SPIN_CAP && !__all(f[0] == epoch && f[1] == epoch && f[2] == epoch && f[3] == epoch); ++sp) {
    asm volatile("s_sleep 8" ::: "memory");
    f = ld_sc1(rs, lane16, off);
  }
}
DI void peek(u32x4 srd, unsigned voff) {
  unsigned long long sv;
  asm volatile("s_mov_b64 %0, exec\n\ts_mov_b64 exec, 1\n\tv_mov_b32 v124, 0\n\tbuffer_atomic_add v124, %1, %2, 0 offen sc0 sc1\n\ts_mov_b64 exec, %0" : "=&s"(sv) : "v"(voff), "s"(srd) : "memory");
}
template <int VM> DI void peek_await(u32x4 srd, unsigned voff, unsigned expect) {
  unsigned c;
  asm volatile("s_waitcnt vmcnt(%c1)\n\tv_readfirstlane_b32 %0, v124" : "=s"(c) : "n"(VM) : "memory");
  if (__builtin_expect(c != expect, 0)) await<1ull>(srd, voff, expect);
}
template <int R> DI u32x4 tr_col(unsigned sel) {
  unsigned o0, o1, o2, o3;
  asm volatile("v_perm_b32 %0, v[%c5+4], v[%c5], %4\n\tv_perm_b32 %1, v[%c5+12], v[%c5+8], %4\n\tv_perm_b32 %2, v[%c5+20], v[%c5+16], %4\n\tv_perm_b32 %3, v[%c5+28], v[%c5+24], %4\n\ts_nop 1"
               : "=v"(o0), "=v"(o1), "=v"(o2), "=v"(o3) : "s"(sel), "n"(R));
  return u32x4{o0, o1, o2, o3};
}

#define AE(i) "v_exp_f32 v[%c[sc]+" #i "], v[%c[sc]+" #i "]\n\t"
#define AC(d, i) "v_cvt_pk_bf16_f32 v" #d ", v[%c[sc]+" #i "], v[%c[sc]+" #i "+1]\n\t"
template <int SC, int SN, int OV> DI void att_a(unsigned vA) {
  asm volatile(
      "s_waitcnt lgkmcnt(1)\n\t"
      "v_mfma_f32_32x32x16_bf16 v[144:159], v[242:245], v[228:231], v[144:159]\n\t"
      AE(0) AE(1) "ds_read_b128 v[242:245], %[va] offset:%c[ov]+704\n\t" AE(2) AE(3) AC(224, 0)
      "v_mfma_f32_32x32x4_2b_bf16 v[%c[sn]:%c[sn]+31], v[236:237], v[254:255], 0\n\t"
      AC(225, 2) AE(4) AE(5) AE(6) AE(7) AC(226, 4) AC(227, 6) AE(8) AE(9) AE(10) AE(11) AC(228, 8)
      "s_waitcnt lgkmcnt(1)\n\t"
      "v_mfma_f32_32x32x16_bf16 v[128:143], v[238:241], v[224:227], v[128:143]\n\t"
      AC(229, 10) AE(12) AE(13) AE(14) AE(15) AC(230, 12) AC(231, 14)
      ::[va] "v"(vA), [sc] "n"(SC), [sn] "n"(SN), [ov] "n"(OV) : "memory");
}
template <int SC, int SN, int OK> DI void att_b(unsigned kA, unsigned kB, unsigned vA) {
  asm volatile(
      AE(16) "s_waitcnt lgkmcnt(0)\n\t"
      "v_mfma_f32_32x32x16_bf16 v[128:143], v[242:245], v[228:231], v[128:143]\n\t"
      AE(17) AE(18) AE(19) AC(224, 16)
      "v_mfma_f32_32x32x16_bf16 v[%c[sn]:%c[sn]+15], v[232:235], v[246:249], v[%c[sn]:%c[sn]+15]\n\t"
      AC(225, 18) AE(20) AE(21) AE(22) AE(23) AC(226, 20) AC(227, 22)
      "v_mfma_f32_32x32x16_bf16 v[%c[sn]+16:%c[sn]+31], v[232:235], v[250:253], v[%c[sn]+16:%c[sn]+31]\n\t"
      AE(24) AE(25) "ds_read_b128 v[232:235], %[ka] offset:%c[ok]\n\tds_read_b64 v[236:237], %[kb] offset:%c[ok]\n\t" AE(26) AE(27) AC(228, 24)
      "v_mfma_f32_32x32x16_bf16 v[144:159], v[238:241], v[224:227], v[144:159]\n\t"
      AC(229, 26) AE(28) AE(29) AE(30) AE(31) AC(230, 28) AC(231, 30)
      "ds_read_b128 v[238:241], %[va] offset:%c[ok]"
      ::[ka] "v"(kA), [kb] "v"(kB), [va] "v"(vA), [sc] "n"(SC), [sn] "n"(SN), [ok] "n"(OK) : "memory");
}
#undef AE
#undef AC
template <int SEND, int KEEP> DI unsigned long long att_merge(unsigned mine, unsigned theirs) {
  unsigned long long m;
  asm volatile("ds_write_b128 %1, v[%c3:%c3+3]\n\tds_write_b128 %1, v[%c3+4:%c3+7] offset:1024\n\tds_write_b128 %1, v[%c3+8:%c3+11] offset:2048\n\t"
               "s_waitcnt vmcnt(0) lgkmcnt(0)\n\ts_barrier\n\t"
               "ds_read_b128 v[168:171], %2 offset:2048\n\tds_read_b128 v[160:163], %2\n\tds_read_b128 v[164:167], %2 offset:1024\n\ts_waitcnt lgkmcnt(2)\n\t"
               "v_pk_add_f32 v[168:169], v[168:169], v[%c4+8:%c4+9]\n\tv_pk_add_f32 v[170:171], v[170:171], v[%c4+10:%c4+11]\n\t"
               "v_mov_b32 v172, v168\n\tv_mov_b32 v173, v168\n\ts_waitcnt lgkmcnt(0)\n\t"
               ".set i_, 0\n\t.rept 4\n\tv_pk_add_f32 v[160+2*i_:161+2*i_], v[160+2*i_:161+2*i_], v[%c4+2*i_:%c4+2*i_+1]\n\t.set i_, i_+1\n\t.endr\n\t"
               "v_permlane32_swap_b32 v172, v173\n\t"
               "v_pk_mul_f32 v[174:175], v[168:169], 0\n\tv_pk_mul_f32 v[178:179], v[170:171], 0\n\tv_rcp_f32 v176, v173\n\t"
               "v_pk_fma_f32 v[174:175], v[160:161], 0, v[174:175]\n\tv_pk_fma_f32 v[178:179], v[162:163], 0, v[178:179]\n\t"
               "v_pk_fma_f32 v[174:175], v[164:165], 0, v[174:175]\n\tv_pk_fma_f32 v[178:179], v[166:167], 0, v[178:179]\n\t"
               "v_pk_add_f32 v[174:175], v[174:175], v[178:179]\n\tv_add_f32 v173, v173, v174\n\tv_add_f32 v173, v173, v175\n\t"
               "v_cmp_nle_f32 vcc, 0x21800000, v173\n\ts_mov_b64 %0, vcc\n\tv_cmp_nge_f32 vcc, 0x71800000, v173\n\ts_or_b64 %0, %0, vcc"
               : "=&s"(m) : "v"(mine), "v"(theirs), "n"(SEND), "n"(KEEP) : "memory", "vcc", "scc");
  return m;
}

__global__ void __launch_bounds__(512, 2) __attribute__((amdgpu_num_vgpr(48))) k_main(const char* __restrict__ x, const char* __restrict__ wqkv, const char* __restrict__ wo, const char* __restrict__ w1, const char* __restrict__ w2,
    const char* __restrict__ w3, const char* __restrict__ w4, char* __restrict__ ws, char* __restrict__ out) {
  extern __shared__ __attribute__((aligned(16))) char smem[];
  lds_ptr lds = (lds_ptr)smem;
  OWN_ALL_VGPRS();
  asm volatile(".subsection 2\n\t.rept 96\n\ts_nop 0\n\t.endr\n\t.Lk_code_end:\n\t.subsection 0");
  STAMP_DECL; STAMP(0);
  const unsigned ldsb = (unsigned)(size_t)lds;
  const int tid = threadIdx.x, lane = tid & 63, w = __builtin_amdgcn_readfirstlane(tid >> 6), q = lane >> 4, tok = lane & 15;
  const int bid = blockIdx.x, xcd = bid & 7, r = bid >> 3;
  const int j = r & 7, head = 8 * (xcd & 3) + j, qb = 4 * (xcd >> 2) + (r >> 3), rb = 4 * qb + (xcd & 3);
  const __amdgpu_buffer_rsrc_t rsw = mk_rsrc(ws, WS_END);
  const unsigned wfs = OFF_WF + j * NGRP * GRP_B;
  const unsigned lane16 = lane * 16;

  const int rb1 = 2 * xcd + (r & 1), g1 = ((r >> 1) + 15) & 15;
  P1Load L1;
  const bool loader = w >= 4 && g1 < 15;
  if (__builtin_expect(loader, 1)) L1 = p1_start(x, wqkv, rb1, g1, (g1 + 5 * (r & 1) + xcd) % 10, w - 4, lane);
  const u32x4 srd = mk_srd(ws, WS_END);
  unsigned epoch = 0;
  if (loader) {
    asm volatile("s_waitcnt vmcnt(%c0)\n\ts_barrier" ::"n"(8 * (P1_DEPTH - 2)) : "memory");
    p1_issue(L1, P1_DEPTH - 1);
#pragma unroll 1
    for (int p = 0; p < 10 - P1_DEPTH; ++p) {
      asm volatile("s_waitcnt vmcnt(%c0)\n\ts_barrier" ::"n"(8 * (P1_DEPTH - 2)) : "memory");
      p1_issue(L1, p + P1_DEPTH);
    }
    if (P1_DEPTH == 4) asm volatile("s_waitcnt vmcnt(16)\n\ts_barrier" ::: "memory");
    if (w == 4) {
      epoch = *(volatile __attribute__((address_space(3))) unsigned*)(lds + L_EPO);
      peek(srd, OFF_AXF + rb1 * 128);
      asm volatile("s_waitcnt vmcnt(9)\n\ts_barrier\n\ts_waitcnt vmcnt(0)" ::: "memory");
      const bool late = __builtin_amdgcn_readfirstlane(__float_as_uint(vget<124>())) != __builtin_amdgcn_readfirstlane(epoch);
      if (late) { asm volatile("s_barrier\n\ts_barrier" ::: "memory"); await<1ull>(srd, OFF_AXF + rb1 * 128, epoch); }
      const u32x4 xs = ld_sc1(rsw, lane16, OFF_XST + rb1 * 1024);
      if (!late) asm volatile("s_barrier\n\ts_barrier" ::: "memory");
      *(__attribute__((address_space(3))) u32x4*)(lds + 16384 + lane16) = xs;
      asm volatile("s_waitcnt lgkmcnt(0)\n\ts_barrier" ::: "memory");
    } else asm volatile("s_waitcnt vmcnt(8)\n\ts_barrier\n\ts_waitcnt vmcnt(0)\n\ts_barrier\n\ts_barrier\n\ts_barrier" ::: "memory");
  } else {
    if (w == 0) {
      epoch = __builtin_amdgcn_raw_buffer_load_b32(rsw, 0, OFF_EPO, AUX_SC1);
      st_sc1(u32x4{0u, 0u, 0u, 0u}, rsw, (lane >> 4) * 2048 + (lane & 15) * 16, OFF_CNT + (32 * rb + j) * 256);
      st_sc1(u32x4{0u, 0u, 0u, 0u}, rsw, lane < 16 ? lane16 : 0x80000000u, OFF_QC + (head * 8 + qb) * 256);
    }
    if (w == 1) warm_code<255>(r & 7, lane);
    auto ready = [&]() AI {
      epoch = __builtin_amdgcn_readfirstlane(epoch) + 1u;
      __builtin_amdgcn_raw_buffer_store_b32(epoch, rsw, lane == 0 ? bid * 4 : 0x80000000u, OFF_RDY, AUX_SC1);
      *(__attribute__((address_space(3))) unsigned*)(lds + L_EPO) = epoch;
    };
    if (g1 == 15) {
      p1_stats(x, rsw, rb1, w, lane, [&]() AI { if (w == 0) ready(); });
      if (w == 0) __builtin_amdgcn_raw_buffer_store_b32(epoch, rsw, lane == 0 ? 0 : 0x80000000u, OFF_AXF + rb1 * 128, AUX_SC1);
    } else if (w == 0) {
      asm volatile("s_waitcnt vmcnt(0)" ::: "memory");
      ready();
    }
    STAMP(47);
    if (tid < 80 && tid != 3) *(__attribute__((address_space(3))) unsigned*)(lds + L_FILL + tid * 4) = tid == 0 ? 4u : (tid >> 2) != 1 ? 0u : tid < 6 ? tid : 24u + tid;
    if (g1 < 10) p1_compute<true>(srd, rsw, lds, rb1, g1, w, lane STAMP_ARG);
    else if (g1 < 15) p1_compute<false>(srd, rsw, lds, rb1, g1, w, lane STAMP_ARG);
    else if (w == 0) {
      asm volatile("s_sleep 64" ::: "memory");
      asm volatile("buffer_load_dwordx4 v[250:253], %0, %1, %2 offen sc1" ::"v"(lane16), "s"(srd), "s"(OFF_RDY) : "memory");
    }
  }
  STAMP(43);
  const int hi = lane >> 5, r32 = lane & 31, wq = w & 3, wk = w >> 2;
  unsigned kvs[3], kvd[3];
#pragma unroll
  for (int i = 0; i < 3; ++i) {
    const int pc = min(w + 8 * i, 21), h2 = pc >= 11;
    kvs[i] = kv_cell((pc - 11 * h2) * 64 + lane, h2, head);
    kvd[i] = ldsb + pc * 1024;
  }
  const int job = w * 256 + bid;
  const bool has_job = job < 850;
  int mat, ks, cb;
  if (job < 300) { mat = job / 100; const int e = job - 100 * mat; ks = e / 5; cb = e - 5 * ks; }
  else if (job < 580) { mat = 3; const int e = job - 300; ks = e / 14; cb = e - 14 * ks; }
  else { mat = 4; const int e = job - 580; ks = e / 5; cb = e - 5 * ks; }
  const char* src = mat == 0 ? wo : mat == 1 ? w1 : mat == 2 ? w2 : mat == 3 ? w3 : w4;
  const unsigned kvb = ldsb + L_KV0 + wk * KV_HALF;
  unsigned kA = kvb + hi * 512 + r32 * 16, kB = kvb + 1024 + tail_slot(swap23(r32)) * 8, vA = kvb + 1280 + hi * 352 + r32 * 16;
  const unsigned qo = OFF_QF + (head * 64 + 8 * qb + 2 * wq) * QF_TILE, qv0 = hi * 512 + r32 * 16, qv1 = hi * QF_TILE + tail_slot(r32) * 8;
  {
    asm volatile("" ::"v"(kvs[0]), "v"(kvs[1]), "v"(kvs[2]), "s"(kvd[0]), "s"(kvd[1]), "s"(kvd[2]), "s"(qo), "v"(qv0), "v"(qv1), "v"(kA), "v"(kB), "v"(vA));
    asm volatile("s_waitcnt vmcnt(0) lgkmcnt(0)\n\ts_barrier" ::: "memory");
    STAMP(44);
    asm volatile(".set i_, 0\n\t.rept 32\n\tv_mov_b32 v[128+i_], 0\n\t.set i_, i_+1\n\t.endr\n\t"
                 ".set i_, 0\n\t.rept 4\n\tv_mov_b32 v[228+i_], 0\n\tv_mov_b32 v[242+i_], 0\n\t.set i_, i_+1\n\t.endr");
    if (w == 0) {
      all_flags<250>(rsw, OFF_RDY, epoch, lane16);
      const int gt = g1 < 5 ? g1 : g1 < 10 ? g1 - 5 : g1 - 10, hh = (128 * gt) / 20 + (lane >> 3);
      if (g1 < 15 && hh <= (128 * gt + 127) / 20 && (g1 >= 5 || (lane & 7) == (rb1 >> 1)))
        __hip_atomic_fetch_add((unsigned*)(ws + OFF_QC + (hh * 8 + (lane & 7)) * 256 + (rb1 & 3) * 64), 1u, __ATOMIC_RELAXED, __HIP_MEMORY_SCOPE_AGENT);
      if (bid == 0) __builtin_amdgcn_raw_buffer_store_b32(epoch, rsw, lane == 0 ? 0 : 0x80000000u, OFF_EPO, AUX_SC1);
      const int span = 1 + ((20 * head + 19) >> 7) - ((20 * head) >> 7);
      await<0xfull>(srd, OFF_QC + (head * 8 + qb) * 256 + lane * 64, (8 + ((lane >> 1) == (qb & 1))) * span);
    }
    asm volatile("s_barrier" ::: "memory");
    STAMP(45);
  }

  {
    asm volatile("buffer_load_dwordx4 v[246:249], %0, %2, %3 offen sc1\n\tbuffer_load_dwordx4 v[250:253], %0, %2, %3 offen offset:1280 sc1\n\t"
                 "buffer_load_dwordx2 v[254:255], %1, %2, %3 offen offset:1024 sc1" ::"v"(qv0), "v"(qv1), "s"(srd), "s"(qo) : "memory");
  }
  auto kv_piece = [&](int c, int i) AI { bdma_sc1(srd, kvs[i], OFF_KV + c * (4 * KV_BLK), kvd[i] + kv_at(c)); };
  kv_piece(0, 0); kv_piece(0, 1); kv_piece(0, 2);
  kv_piece(1, 0); kv_piece(1, 1); kv_piece(1, 2);
  asm volatile(
      "s_waitcnt vmcnt(3) lgkmcnt(0)\n\ts_barrier\n\t"
      "ds_read_b64 v[236:237], %1\n\tds_read_b128 v[232:235], %0\n\ts_waitcnt lgkmcnt(1)\n\t"
      "v_mfma_f32_32x32x4_2b_bf16 v[160:191], v[236:237], v[254:255], 0\n\t"
      "s_nop 15\n\ts_nop 1\n\ts_waitcnt lgkmcnt(0)\n\t"
      "v_mfma_f32_32x32x16_bf16 v[160:175], v[232:235], v[246:249], v[160:175]\n\t"
      "v_mfma_f32_32x32x16_bf16 v[176:191], v[232:235], v[250:253], v[176:191]\n\t"
      "ds_read_b128 v[232:235], %0 offset:%c3\n\tds_read_b64 v[236:237], %1 offset:%c3\n\tds_read_b128 v[238:241], %2 offset:%c3\n\t"
      "s_nop 7"
      ::"v"(kA), "v"(kB), "v"(vA), "n"(KV_REC) : "memory");
  STAMP(2);
  unsigned jvo[2], jcs;
  int kpos = ks;
  if (mat == 4) { const int half = ks >= 27, k27 = ks - 27 * half, j4 = min(k27 / 7, 3), jj = 4 * half + j4, pi = k27 - 7 * j4; kpos = pi < 2 ? 2 * jj + pi : pi < 5 ? 14 + 3 * jj + pi : 35 + 2 * jj - (jj > 3) + pi; }
  const unsigned jso = OFF_WF + ((kpos / 5) * 25 + (kpos % 5) * 5) * 1024;
  auto job_addr = [&]() AI {
    int ln = lane; asm volatile("" : "+v"(ln));
    const int lq = ln >> 4, lt = ln & 15, kk = 4 * cb + (lt >> 2);
    const bool ok = mat != 3 || kk < 54;
#pragma unroll
    for (int h = 0; h < 2; ++h) {
      int jj, t, pos, G;
      if (mat == 3) {
        const int half = kk >= 27, k27 = kk - 27 * half, j4 = min((k27 * 37) >> 8, 3);
        jj = 4 * half + j4; t = 2 * (k27 - 7 * j4) + h; pos = lt & 3;
        const int pass = (t * 13) >> 6; t -= 5 * pass; G = 12 + 4 * pass;
      } else {
        const int per5 = (kk * 13) >> 6, m5 = kk - 5 * per5;
        const bool single = m5 == 2;
        jj = 2 * per5 + (single ? ((lt >> 1) & 1) : (m5 > 2)); t = single ? 4 : 2 * (m5 > 2 ? m5 - 3 : m5) + h;
        pos = single ? 2 * (lt & 1) + h : lt & 3;
        G = mat == 4 ? 24 : 4 * mat;
      }
      jvo[h] = ok ? (unsigned)((jj * NGRP + G) * 25 + t) * 1024 + (lq * 16 + pos) * 16 : 0x80000000u;
    }
    jcs = lt < 2 ? lt * 64 + lq * 16 : 0x80000000u;
  };
  auto job_cols = [&](auto S_) AI {
    constexpr int sl = S_.value;
    const u32x4 o0 = tr_col<96 + sl>(0x05040100u), o1 = tr_col<96 + sl>(0x07060302u);
    st_sc1(o0, rsw, jvo[sl >> 1] + (sl & 1) * 128, jso);
    st_sc1(o1, rsw, jvo[sl >> 1] + (sl & 1) * 128 + 64, jso);
    if (mat == 1 || mat == 3) {
      const bf16x8 ones = {0x3f80, 0x3f80, 0x3f80, 0x3f80, 0x3f80, 0x3f80, 0x3f80, 0x3f80};
      const f32x4 z = {0.f, 0.f, 0.f, 0.f};
      const f32x4 c0 = MFMA16(__builtin_bit_cast(bf16x8, o0), ones, z), c1 = MFMA16(__builtin_bit_cast(bf16x8, o1), ones, z);
      st_sc1(__builtin_bit_cast(u32x4, (lane & 1) ? c1 : c0), rsw, jcs, (mat == 1 ? OFF_CS1 + (ks * 5 + cb) * 512 : OFF_CS3 + (ks * 14 + cb) * 512) + sl * 128);
    }
  };
  const unsigned rt = 4 * rb + (w & 3);
  const unsigned kp = 5 * (j >> 1) + 3 * (j & 1), ksg = 5 * (j >> 1) + 2;
  const unsigned rowoff = ((16 * rt + tok) * D_) * 2;
  const unsigned colp = (32 * kp + 8 * q) * 2, cols = (32 * ksg + 16 * (j & 1) + 4 * q) * 2;
  auto fill = [&](auto I_) AI {
    constexpr int it = I_.value;
    const u32x4 sent = {SENT, SENT, SENT, SENT};
    if constexpr (it < 5) st_sc1(sent, rsw, lane16, OFF_O + (bid * 40 + w * 5 + it) * 1024);
    else if (w == 7) st_sc1(sent, rsw, lane16, OFF_ST1 + bid * 1024);
  };
  auto side = [&](auto C_, auto S_) AI {
    constexpr int c = C_.value, sl = S_.value, t = 7 * c + sl;
    if constexpr (c == 0) {
      if constexpr (sl < 6) { kv_piece(2 + 2 * sl / 3, 2 * sl % 3); kv_piece(2 + (2 * sl + 1) / 3, (2 * sl + 1) % 3); }
      else { const int i = min(w, 5), h2 = i >= 3; bdma_sc1(srd, kv_cell((i - 3 * h2) * 64 + lane, h2, head), OFF_KV, ldsb + L_WRAP + i * 1024); }
    } else if constexpr (c == 1) {
      if constexpr (sl < 2) if (has_job) {
        int ln = lane; asm volatile("" : "+v"(ln));
        const int lq = ln >> 4, N = mat == 3 ? F_ : D_, n0 = 128 * cb + 8 * (ln & 15);
        int k0 = 32 * ks + 8 * lq + 4 * sl;
        if (mat == 0) { const int o = 4 * ks + lq, i = (o * 205) >> 10, oo = o - 5 * i; k0 = 40 * i + (oo < 3 ? 8 * oo : 8 * oo - 4) + 4 * sl + ((sl == 1 && oo == 2) ? 16 : 0); }
        const unsigned off = (unsigned)(k0 * N + (n0 < N ? n0 : 0)) * 2;
        gload4<96 + 16 * sl, 0>(src, off); gload4<100 + 16 * sl, 0>(src + N * 2, off);
        gload4<104 + 16 * sl, 0>(src + N * 4, off); gload4<108 + 16 * sl, 0>(src + N * 6, off);
      }
      if constexpr (sl == 2) if (w == 7) warm_code<126>(r & 7, lane);
    } else if constexpr (c == 2) {
      if constexpr (sl >= 5) if (has_job) {
        if (sl == 5) { job_addr(); asm volatile("s_waitcnt vmcnt(0)" ::: "memory"); }
        job_cols(IC<sl - 5>{});
      }
    } else if constexpr (c == 3) {
      if constexpr (sl < 2) { if (has_job) job_cols(IC<sl + 2>{}); }
      else fill(IC<sl - 2>{});
    } else if constexpr (c == 4) {
      if constexpr (sl == 0) fill(IC<5>{});
      if constexpr (sl >= 1 && sl <= 3) { kv_piece(6, sl - 1); kv_piece(7, sl - 1); }
      if constexpr (sl == 4) if (tid == 0) __hip_atomic_store((unsigned*)(ws + OFF_FLAG) + bid, epoch, __ATOMIC_RELAXED, __HIP_MEMORY_SCOPE_AGENT);
      if constexpr (sl == 6) if (w < 4) {
        gload4<116, 0>(x, rowoff + colp); gload4<120, 64>(x, rowoff + colp);
        asm volatile("global_load_dwordx2 v[124:125], %0, %1" ::"v"(rowoff + cols), "s"(x) : "memory");
      }
    } else if constexpr (t < 39) {
      if constexpr (sl == 1) if (w == 0) asm volatile("buffer_load_dwordx4 v[96:99], %0, %1, %2 offen sc1" ::"v"(lane16), "s"(srd), "s"(OFF_FLAG) : "memory");
    } else if constexpr (t < 52) {
      const unsigned pc = (w + 8 * (t - 39)) * 1024;
      if (t < 51 || w < 4) bdma_sc1(srd, lane16, wfs + pc, ldsb + pc);
    }
  };
  const int qt = 8 * qb + 2 * wq + wk;
  const unsigned mine = ldsb + (w < 6 ? L_KV0 + w * 3072 : L_SCR2 + (w - 6) * 3072) + lane16;
  const unsigned theirs = ldsb + ((w ^ 4) < 6 ? L_KV0 + (w ^ 4) * 3072 : L_SCR2 + ((w ^ 4) - 6) * 3072) + lane16;
  const unsigned rto = OFF_O + (2 * qt + (r32 >> 4)) * FM_RT + (r32 & 15) * 16;
  const unsigned ov0 = rto + (5 * (head >> 1) + 3 * (head & 1) + hi) * 256, ov1 = hi ? 0x80000000u : rto + (5 * (head >> 1) + 2) * 256 + (head & 1) * 8;
  const unsigned ov2 = OFF_CNT + (16 * qt + lane) * 256 + (head >> 3) * 64;
  static_for<8>([&](auto C_) AI {
    constexpr int c = C_.value;
    if constexpr (c == 7) asm volatile("" ::"v"(mine), "v"(theirs), "v"(ov0), "v"(ov1), "v"(ov2));
    __builtin_amdgcn_s_setprio(2);
    att_a<160, 192, 1 * KV_REC>(vA); side(C_, IC<0>{}); att_b<160, 192, 2 * KV_REC>(kA, kB, vA); side(C_, IC<1>{});
    __builtin_amdgcn_s_setprio(1);
    att_a<192, 160, 2 * KV_REC>(vA); side(C_, IC<2>{}); att_b<192, 160, 3 * KV_REC>(kA, kB, vA); side(C_, IC<3>{});
    __builtin_amdgcn_s_setprio(0);
    att_a<160, 192, 3 * KV_REC>(vA);
    if constexpr (c == 5) if (w == 0) {
      asm volatile("s_waitcnt vmcnt(0)" ::: "memory");
      all_flags<96>(rsw, OFF_FLAG, epoch, lane16);
    }
    if constexpr (c == 0) asm volatile("s_waitcnt vmcnt(8) lgkmcnt(0)\n\ts_barrier" ::: "memory");
    else if constexpr (c == 1) { if (has_job) asm volatile("s_waitcnt vmcnt(8) lgkmcnt(0)\n\ts_barrier" ::: "memory"); else if (w == 7) asm volatile("s_waitcnt vmcnt(1) lgkmcnt(0)\n\ts_barrier" ::: "memory"); else asm volatile("s_waitcnt vmcnt(0) lgkmcnt(0)\n\ts_barrier" ::: "memory"); }
    else if constexpr (c == 4) asm volatile("s_waitcnt vmcnt(6) lgkmcnt(0)\n\ts_barrier" ::: "memory");
    else if constexpr (c == 5) asm volatile("s_waitcnt vmcnt(0) lgkmcnt(0)\n\ts_barrier" ::: "memory");
    else if constexpr (c == 6) asm volatile("s_waitcnt lgkmcnt(0)\n\ts_barrier" ::: "memory");
    __builtin_amdgcn_s_setprio(3);
    { const int d = c < 7 ? kv_at(c + 1) - kv_at(c) : L_WRAP - kv_at(7) + wk * (3072 - KV_HALF); kA += d; kB += d; vA += d; }
    att_b<160, 192, 0>(kA, kB, vA); side(C_, IC<4>{});
    att_a<192, 160, 0>(vA); side(C_, IC<5>{});
    att_b<192, 160, KV_REC>(kA, kB, vA); side(C_, IC<6>{});
  });
  {
    asm volatile("s_waitcnt lgkmcnt(0)\n\tv_mfma_f32_32x32x16_bf16 v[144:159], v[242:245], v[228:231], v[144:159]\n\ts_nop 11" ::: "memory");
    STAMP(3);
    if (__builtin_expect((wk == 0 ? att_merge<144, 128>(mine, theirs) : att_merge<128, 144>(mine, theirs)) != 0, 0)) {
      const f32x16 Os = attn_safe(rsw, OFF_QF + (head * 64 + qt) * QF_TILE, head, lane);
      static_for<12>([&](auto I_) AI { vset<160 + I_.value>(__float_as_uint(Os[I_.value])); });
      auto r2 = __builtin_amdgcn_permlane32_swap(__float_as_uint(Os[8]), __float_as_uint(Os[8]), false, false);
      vset<176>(__float_as_uint(__builtin_amdgcn_rcpf(__uint_as_float(r2[1]))));
    }
    asm volatile(".set i_, 0\n\t.rept 6\n\tv_pk_mul_f32 v[160+2*i_:161+2*i_], v[160+2*i_:161+2*i_], v[176:177] op_sel_hi:[1,0]\n\t.set i_, i_+1\n\t.endr\n\t"
                 EP_PACK(180, 160) EP_PACK(182, 164) EP_PACK(184, 168) EP_GUARD(180) EP_GUARD(182) EP_GUARD(184)
                 "buffer_store_dwordx4 v[180:183], %0, %2, 0 offen sc1\n\tbuffer_store_dwordx2 v[184:185], %1, %2, 0 offen sc1" ::"v"(ov0), "v"(ov1), "s"(srd) : "memory");
    arrive<0xffffull>(srd, ov2);
    asm volatile("s_barrier" ::: "memory");
  }
  if (w >= 4) {
    typedef volatile __attribute__((address_space(3))) unsigned* lword;
    __builtin_amdgcn_s_setprio(PRIO_H);
    auto min_cons = [&]() AI {
      const unsigned c = *(lword)(lds + L_CONS + (lane & 3) * 64);
      return (int)(min(min(__builtin_amdgcn_readlane(c, 0), __builtin_amdgcn_readlane(c, 1)), min(__builtin_amdgcn_readlane(c, 2), __builtin_amdgcn_readlane(c, 3))) >> 2);
    };
    auto wait_cons = [&](int need) AI {
      int have;
#pragma unroll 1
      for (unsigned sp = 0; (have = min_cons()) < need && sp < LDS_SPIN; ++sp) asm volatile("s_sleep 2" ::: "memory");
      return have;
    };
    const int gst = w < 6 ? 22 + w : 24 + w;
    auto publish = [&](unsigned g) AI {
      unsigned long long sv;
      asm volatile("s_mov_b64 %0, exec\n\ts_mov_b64 exec, 1\n\tds_write_b32 %1, %3\n\tds_read_b128 v[252:255], %2 offset:16\n\ts_waitcnt lgkmcnt(0)\n\t"
                   "v_min3_u32 v252, v252, v253, v254\n\tv_min_u32 v252, v252, v255\n\tds_max_u32 %2, v252\n\ts_mov_b64 exec, %0"
                   : "=&s"(sv) : "v"(ldsb + L_OWN + (w - 4) * 4), "v"(ldsb + L_FILL), "v"(g) : "memory");
    };
    if (w >= 6) {
      if (w == 7) wait_cons(1);
      {
        const bool is1 = w == 6;
        const int c0 = is1 ? 10 * j : 4 * (27 * (j >> 2) + 7 * (j & 3)), nc = is1 ? 10 : 28;
        const int i = lane & 7, cq = (c0 & ~3) + 4 * (lane >> 3);
        ld_cs<128>(srd, (is1 ? OFF_CS1 : OFF_CS3) + ((cq >> 4) * 128 + i * 16 + (cq & 15)) * 4, (is1 ? 640 : 1792) * 4);
        asm volatile("s_waitcnt vmcnt(0)\n\t.set i_, 1\n\t.rept 19\n\tv_pk_add_f32 v[128:129], v[128:129], v[128+4*i_:129+4*i_]\n\t"
                     "v_pk_add_f32 v[130:131], v[130:131], v[130+4*i_:131+4*i_]\n\t.set i_, i_+1\n\t.endr" ::: "memory");
        static_for<4>([&](auto E_) AI {
          const float sacc = vget<128 + E_.value>();
          const int c = cq + E_.value - c0;
          int slot;
          if (is1) {
            const int cp = (j & 1) ? c - 2 : c;
            slot = (cp >= 0 && cp < 8) ? (2 * (cp >> 2) + (i >> 2)) * 16 + 4 * (cp & 3) + (i & 3) : 64 + 8 * (c & 1) + i;
          } else slot = 80 + (2 * (c >> 2) + (i >> 2)) * 16 + 4 * (c & 3) + (i & 3);
          if (c >= 0 && c < nc) *(__attribute__((address_space(3))) float*)(lds + L_CS1 + slot * 4) = sacc;
        });
      }
      *(lword)(lds + L_CSF + (w - 6) * 4) = 1u;
      if (STASH) {
        wait_cons(w == 6 ? 4 : 12);
        asm volatile("s_sleep 16" ::: "memory");
        ld_group(srd, lane16, wfs + gst * GRP_B);
        asm volatile("s_waitcnt vmcnt(0)" ::: "memory");
        wait_cons(gst - 5);
        st_group(ldsb + (gst % 6) * GRP_B + lane16);
      }
      publish(NGRP);
      return;
    }
    auto issue = [&](int G) AI {
      const unsigned so = __builtin_amdgcn_readfirstlane(wfs + G * GRP_B), d = __builtin_amdgcn_readfirstlane(ldsb + (G % 6) * GRP_B);
      if (G >= 20 && (G < 24 || G == 34)) {
        const unsigned st = G == 34 ? 4096 : 5120;
#pragma unroll
        for (int i = 0; i < 5; ++i) { bdma4_sc1(srd, lane16, so + i * st, d + i * st); if (PACE) asm volatile("s_sleep %c0" ::"n"(PACE)); }
        asm volatile("s_waitcnt vmcnt(20)" ::: "memory");
      } else {
#pragma unroll
        for (int i = 0; i < 6; ++i) { bdma4_sc1(srd, lane16, so + i * 4096, d + i * 4096); if (PACE) asm volatile("s_sleep %c0" ::"n"(PACE)); }
        bdma_sc1(srd, lane16, so + 24576, d + 24576);
        asm volatile("s_waitcnt vmcnt(25)" ::: "memory");
      }
    };
    int have = 0;
    bool got = false;
#pragma unroll 1
    for (int G = w; G < NGRP; G += 2) {
      if (STASH && G == gst + 4) continue;
      const int need = (G >= 8 && G < POSTP && (G & 2) == 0) ? (G & ~3) - 3 : G - 5;
      if (have < need && (have = min_cons()) < need) {
        asm volatile("s_waitcnt vmcnt(0)" ::: "memory");
        publish(G);
        if (STASH && G == (w == 4 ? 10 : 15)) { ld_group(srd, lane16, wfs + gst * GRP_B); got = true; }
        have = wait_cons(need);
      }
      if (DLY16 && (G == 16 || G == 17)) { wait_cons(12); asm volatile("s_sleep %c0" ::"n"(DLY16)); }
      if (G == gst && got) { st_group(ldsb + (gst % 6) * GRP_B + lane16); asm volatile("s_waitcnt vmcnt(0)" ::: "memory"); publish(G + 2); }
      else { issue(G); publish(G); }
    }
    asm volatile("s_waitcnt vmcnt(0)" ::: "memory");
    publish(NGRP);
    return;
  }

  __builtin_amdgcn_s_setprio(3);
  const unsigned wl = ldsb + (q * 16 + 4 * (tok & 3) + (tok >> 2)) * 16;
  const unsigned wb[6] = {wl, wl + 2 * GRP_B, wl + 4 * GRP_B, ldsb + L_FILL, ldsb + L_CONS + w * 64 + tok * 4, 1u};
  const unsigned vo0[4] = {lane16, lane16, lane16, lane16};
  w_prime<0>(wb);
  const unsigned cown = OFF_CNT + (rt * 8 + j) * 256, cgrp = OFF_CNT + (rt * 8 + lane) * 256;
  const unsigned vsg = (q & 1) ? 0x80000000u : (2 * (j & 1) + (q >> 1)) * 256 + tok * 16;
  const unsigned vst = lane < 16 ? (j >> 1) * 256 + lane16 + (j & 1) * 8 : 0x80000000u;
  const unsigned wfl = wfs + (wl - ldsb);
  typedef const __attribute__((address_space(3))) f32x4* lcs;
  asm volatile(".set i_, 0\n\t.rept 10\n\tv_lshlrev_b32 v[96+2*i_], 16, v[116+i_]\n\tv_and_b32 v[97+2*i_], 0xffff0000, v[116+i_]\n\t.set i_, i_+1\n\t.endr");
  STAMP(4);
  await<0xfull>(srd, cown + lane * 64, 8);
  STAMP(5);
  ld_lead(srd, vo0, OFF_O + rt * FM_RT);
  if (__builtin_expect(run_unit<0, 4, 0, 20, vmw(63), 8, 0, -1, 0>(wb, srd, 0, 0, 0, 0, srd, vo0, OFF_O + rt * FM_RT + 8192 STAMP_ARG) != 0, 0)) redo_unit<false>(rsw, vo0, OFF_O + rt * FM_RT, 20, rsw, wfl, srd);
  STAMP(6);
  asm volatile(".set i_, 0\n\t.rept 10\n\tv_pk_add_f32 v[96+2*i_:97+2*i_], v[96+2*i_:97+2*i_], v[228+2*i_:229+2*i_]\n\t.set i_, i_+1\n\t.endr");
  st_tile<96>(srd, lane16, vsg, OFF_H + rt * FM_RT + kp * 1024, OFF_H + rt * FM_RT + ksg * 1024);
  arrive<0xffull>(srd, cgrp + 4);
  st_stats<96>(rsw, vst, OFF_ST1 + rt * 1024);
  STAMP(7);
  w_prime<4>(wb);
  await<1ull>(srd, cown + 4, 8);
  STAMP(8);
  ld_lead(srd, vo0, OFF_H + rt * FM_RT);
#pragma unroll 1
  for (unsigned sp = 0; sp < LDS_SPIN && !__builtin_amdgcn_readfirstlane(*(volatile __attribute__((address_space(3))) unsigned*)(lds + L_CSF)); ++sp) asm volatile("s_sleep 1" ::: "memory");
  f32x4 cs[5];
#pragma unroll
  for (int t = 0; t < 5; ++t) cs[t] = *(lcs)(lds + L_CS1 + t * 64 + q * 16);
  if (__builtin_expect(run_unit<4, 4, 0, 20, vmw(63), 8, 0, 2, 1>(wb, srd, lane16, OFF_ST1 + rt * 1024, 0, 0, srd, vo0, OFF_H + rt * FM_RT + 8192 STAMP_ARG) != 0, 0)) redo_unit<false>(rsw, vo0, OFF_H + rt * FM_RT, 20, rsw, wfl + 4 * GRP_B, srd);
  STAMP(9);
  float mean1, r1;
  {
    float var;
    read_stats(rsw, lane16, OFF_ST1 + rt * 1024, mean1, var);
    r1 = __builtin_amdgcn_rsqf(var + LN_EPS);
    const float v2 = var * r1 * r1 + LN_EPS, r2 = r1 * __builtin_amdgcn_rsqf(v2);
    const GeluK gk = gelu_consts(mean1, r2, __builtin_amdgcn_sqrtf((var + LN_EPS) * v2));
    const unsigned o = OFF_H1 + rt * FM_RT;
    ep_gelu<0, 2, 228>(gk, cs[0], cs[1]); st_frag<228>(srd, lane16, o + kp * 1024);
    ep_gelu<2, 2, 236>(gk, cs[2], cs[3]); st_frag<236>(srd, lane16, o + kp * 1024 + 1024);
    ep_gelu<4, 1, 244>(gk, cs[4], cs[4]); st_single<244>(srd, vsg, o + ksg * 1024);
    const int rn = (j & 2) ? (int)rt - 1 : (int)rt + 1;
    if (rn >= 0 && rn < 128) arrive<0xffffull>(srd, (lane < 8 ? cgrp : OFF_CNT + (rn * 8 + lane - 8) * 256) + 68);
    else arrive<0xffull>(srd, cgrp + 68);
  }
  {
    const u32x4 srd1 = mk_srd(ws + OFF_H1, SZ_FM);
    unsigned vo[4];
#pragma unroll
    for (int g = 0; g < 4; ++g) {
      const int T = 16 * (int)rt + tok + (g == 0 ? -1 : g == 1 ? 1 : g == 2 ? -2 : 2);
      vo[g] = (T >= 0 && T < S_) ? (unsigned)((T >> 4) * FM_RT + q * 256 + (T & 15) * 16) : 0x80000000u;
    }
    const f32x2 rr = {r1, r1}, nmr = {-mean1 * r1, -mean1 * r1};
    STAMP(10);
    w_prime<8>(wb);
    await<1ull>(srd, cown + 68, (rt == 0 || rt == 127) ? 12 : 16);
    STAMP(1);
    ld_lead(srd1, vo, 0);
    if (__builtin_expect(run_unit<8, 4, 0, 20, vmw(63), 8, 0, -1, 0>(wb, srd, 0, 0, 0, 0, srd1, vo, 8192 STAMP_ARG) != 0, 0)) redo_unit<false>(mk_rsrc(ws + OFF_H1, SZ_FM), vo, 0, 20, rsw, wfl + 8 * GRP_B, srd1);
    STAMP(11);
    asm volatile(".set i_, 0\n\t.rept 10\n\tv_pk_fma_f32 v[228+2*i_:229+2*i_], v[96+2*i_:97+2*i_], %0, v[228+2*i_:229+2*i_]\n\t.set i_, i_+1\n\t.endr\n\t"
                 ".set i_, 0\n\t.rept 10\n\tv_pk_add_f32 v[228+2*i_:229+2*i_], v[228+2*i_:229+2*i_], %1\n\t.set i_, i_+1\n\t.endr" ::"v"(rr), "v"(nmr));
    st_tile<228>(srd, lane16, vsg, OFF_X1 + rt * FM_RT + kp * 1024, OFF_X1 + rt * FM_RT + ksg * 1024);
    arrive<0xffull>(srd, cgrp + 132);
    st_stats<228>(rsw, vst, OFF_ST2 + rt * 1024);
    asm volatile(".set i_, 0\n\t.rept 10\n\tv_pk_add_f32 v[96+2*i_:97+2*i_], v[96+2*i_:97+2*i_], v[228+2*i_:229+2*i_]\n\t.set i_, i_+1\n\t.endr");
  }
  STAMP(12);
#pragma unroll 1
  for (unsigned sp = 0; sp < LDS_SPIN && !__builtin_amdgcn_readfirstlane(*(volatile __attribute__((address_space(3))) unsigned*)(lds + L_CSF + 4)); ++sp) asm volatile("s_sleep 1" ::: "memory");
#pragma unroll
  for (int t = 0; t < 5; ++t) cs[t] = *(lcs)(lds + L_CS3 + t * 64 + q * 16);
  w_prime<12>(wb);
  await<1ull>(srd, cown + 132, 8);
  STAMP(19);
  ld_lead(srd, vo0, OFF_X1 + rt * FM_RT);
  {
    const unsigned gs = OFF_G + rt * G_RT;
    const unsigned gl = gs;
    if (__builtin_expect(run_unit<12, 4, 0, 20, vmw(63), 8, 0, 2, 1>(wb, srd, lane16, OFF_ST2 + rt * 1024, 0, 0, srd, vo0, OFF_X1 + rt * FM_RT + 8192 STAMP_ARG) != 0, 0)) redo_unit<true>(rsw, vo0, OFF_X1 + rt * FM_RT, 20, rsw, wfl + 12 * GRP_B, srd);
    STAMP(13);
    float mean, var;
    read_stats(rsw, lane16, OFF_ST2 + rt * 1024, mean, var);
    const GeluK gk = gelu_consts(mean, __builtin_amdgcn_rsqf(var + LN_EPS), __builtin_amdgcn_sqrtf(var + LN_EPS));
    ep_gelu<0, 2, 228>(gk, cs[0], cs[1]); st_frag<228>(srd, lane16, gs + 2 * j * 1024);
    ep_gelu<2, 2, 236>(gk, cs[2], cs[3]); st_frag<236>(srd, lane16, gs + (2 * j + 1) * 1024);
    ep_gelu<4, 1, 250>(gk, cs[4], cs[4]);
#pragma unroll
    for (int t = 0; t < 5; ++t) cs[t] = *(lcs)(lds + L_CS3 + (5 + t) * 64 + q * 16);
    STAMP(14);
    run_unit<16, 4, 0, 20, vmw(63), 0, 0, -1, 0, 0>(wb, srd, 0, 0, 0, cgrp + 196, srd, vo0, 0 STAMP_ARG);
    peek(srd, cown + 196);
    ep_gelu<0, 1, 252>(gk, cs[0], cs[0]); st_frag<250>(srd, lane16, gs + (16 + 3 * j) * 1024);
    ep_gelu<1, 2, 232>(gk, cs[1], cs[2]); st_frag<232>(srd, lane16, gs + (17 + 3 * j) * 1024);
    ep_gelu<3, 2, 240>(gk, cs[3], cs[4]); st_frag<240>(srd, lane16, gs + (18 + 3 * j) * 1024);
#pragma unroll
    for (int t = 0; t < 4; ++t) cs[t] = *(lcs)(lds + L_CS3 + (10 + t) * 64 + q * 16);
    peek_await<3>(srd, cown + 196, 8);
    run_unit<20, 4, 0, 20, vmw(63), 0, 16, -1, 0, 5>(wb, srd, lane16, gl, 0, cgrp + 8, srd, vo0, 0 STAMP_ARG);
    peek(srd, cown + 8);
    const unsigned g3 = gs + (40 + 2 * j - (j > 3)) * 1024;
    ep_gelu<0, 2, 228>(gk, cs[0], cs[1]); st_frag<228>(srd, lane16, g3);
    if ((j & 3) != 3) { ep_gelu<2, 2, 236>(gk, cs[2], cs[3]); st_frag<236>(srd, lane16, g3 + 1024); }
    STAMP(15);
    if ((j & 3) != 3) peek_await<2>(srd, cown + 8, 8); else peek_await<1>(srd, cown + 8, 8);
    STAMP(16);
    ld_frags<16, 4>(srd, lane16, gl + 16 * 1024);
    run_unit<24, 6, 0, 30, vmw(63, 63, 63, 16, 17, 11), 0, 20, 3, 2, 9>(wb, srd, lane16, gl + 20 * 1024, cown + 72, cgrp + 72, srd, vo0, 0 STAMP_ARG);
    peek_await<0>(srd, cown + 72, 8);
    ld_frags<0, 5>(srd, lane16, gl + 40 * 1024); ld_frags<5, 5>(srd, lane16, gl + 45 * 1024);
    run_unit<30, 5, 30, 54, vmw(63, 63, 9, 4, 0), 0, 4, -1, 0>(wb, srd, lane16, gl + 50 * 1024, 0, 0, srd, vo0, 0 STAMP_ARG);
    STAMP(17);
    asm volatile(".set i_, 0\n\t.rept 10\n\tv_pk_add_f32 v[228+2*i_:229+2*i_], v[96+2*i_:97+2*i_], v[228+2*i_:229+2*i_]\n\t.set i_, i_+1\n\t.endr\n\t"
                 EP_PACK(116, 228) EP_PACK(118, 232) EP_PACK(120, 236) EP_PACK(122, 240) EP_PACK(124, 244)
                 "global_store_dwordx4 %0, v[116:119], %2\n\tglobal_store_dwordx4 %0, v[120:123], %2 offset:64\n\tglobal_store_dwordx2 %1, v[124:125], %2\n\ts_waitcnt lgkmcnt(0)"
                 ::"v"(rowoff + colp), "v"(rowoff + cols), "s"(out) : "memory");
  }
  STAMP(18);
  STAMP_OUT(1);
}

extern "C" void kernel_launch(void* const* d_in, const int* in_sizes, int n_in, void* d_out, int out_size, void* d_ws, size_t ws_size, hipStream_t stream) {
  (void)in_sizes; (void)n_in; (void)out_size; (void)ws_size;
  (void)hipFuncSetAttribute((const void*)k_main, hipFuncAttributeMaxDynamicSharedMemorySize, K2_LDS);
  k_main<<<256, 512, K2_LDS, stream>>>((const char*)d_in[0], (const char*)d_in[1], (const char*)d_in[2], (const char*)d_in[4], (const char*)d_in[5], (const char*)d_in[6],
                                              (const char*)d_in[7], (char*)d_ws, (char*)d_out);
}
```

```cpp
#include <hip/hip_runtime.h>
#include <hip/hip_bf16.h>

typedef __attribute__((ext_vector_type(8))) short bf16x8;
typedef __attribute__((ext_vector_type(4))) short s16x4;
typedef __attribute__((ext_vector_type(2))) __bf16 bf16x2;
typedef __attribute__((ext_vector_type(2))) float f32x2;
typedef __attribute__((ext_vector_type(4))) float f32x4;
typedef __attribute__((ext_vector_type(16))) float f32x16;
typedef __attribute__((ext_vector_type(4))) unsigned u32x4;
typedef __attribute__((ext_vector_type(2))) unsigned u32x2;
typedef __attribute__((address_space(3))) const char* lds_cptr;
typedef __attribute__((address_space(3))) char* lds_ptr;
#define DI __device__ __forceinline__
#define AI __attribute__((always_inline))
#define SBAR() __builtin_amdgcn_sched_barrier(0)
#define PIN(x) asm volatile("" : "+v"(x))
#define MFMA16(a, b, c) __builtin_amdgcn_mfma_f32_16x16x32_bf16((a), (b), (c), 0, 0, 0)
#define MFMA32(a, b, c) __builtin_amdgcn_mfma_f32_32x32x16_bf16((a), (b), (c), 0, 0, 0)

constexpr int S_ = 2048, D_ = 640, F_ = 1728;
constexpr float LN_EPS = 1e-5f;
constexpr float QSCALE = 0.22360679774997896f * 1.4426950408889634f;
constexpr unsigned SENT = 0xFFFFFFFFu;

constexpr unsigned QF_TILE = 1280, KV_REC = 2688, FM_RT = 20 * 1024, G_RT = 54 * 1024;
constexpr unsigned SZ_FM = 128 * FM_RT;
constexpr unsigned OFF_QF = 0;
constexpr unsigned OFF_KV = OFF_QF + 32 * 64 * QF_TILE;
constexpr unsigned KV_BLK = 40960 + 40960 + 128;
constexpr unsigned OFF_O = OFF_KV + 64 * KV_BLK;
constexpr unsigned OFF_H = OFF_O + SZ_FM;
constexpr unsigned OFF_H1 = OFF_H + SZ_FM;
constexpr unsigned OFF_X1 = OFF_H1 + SZ_FM;
constexpr unsigned OFF_G = OFF_X1 + SZ_FM;
constexpr unsigned OFF_ST1 = OFF_G + 128 * G_RT;
constexpr unsigned OFF_ST2 = OFF_ST1 + 128 * 1024;
constexpr int NGRP = 35;
constexpr unsigned GRP_B = 25 * 1024;
constexpr unsigned OFF_WF = OFF_ST2 + 128 * 1024;
constexpr unsigned OFF_CS1 = OFF_WF + 8 * NGRP * GRP_B;
constexpr unsigned OFF_CS3 = OFF_CS1 + 20 * 640 * 4;
constexpr unsigned OFF_FLAG = OFF_CS3 + 20 * 1792 * 4;
constexpr unsigned OFF_CNT = OFF_FLAG + 1024;
constexpr unsigned OFF_QC = OFF_CNT + 1024 * 256;
constexpr unsigned OFF_RDY = OFF_QC + 256 * 256;
constexpr unsigned OFF_AXF = OFF_RDY + 1024;
constexpr unsigned OFF_XST = OFF_AXF + 16 * 128;
constexpr unsigned OFF_EPO = OFF_XST + 16 * 1024;
constexpr unsigned WS_END = OFF_EPO + 256;

#define SPAN_BEGIN
#define SPAN_END
#define STAMP_DECL
#define STAMP_ARG
#define STAMP_PAR
#define STAMP(i)
#define STAMPF(i)
#define STAMP_OUT(k)
DI unsigned pack2(float lo, float hi) { f32x2 v = {lo, hi}; return __builtin_bit_cast(unsigned, __builtin_convertvector(v, bf16x2)); }
DI float bflo(unsigned u) { return __uint_as_float(u << 16); }
DI float bfhi(unsigned u) { return __uint_as_float(u & 0xffff0000u); }
DI float dot2(unsigned a, unsigned b, float c) { return __builtin_amdgcn_fdot2_f32_bf16(__builtin_bit_cast(bf16x2, a), __builtin_bit_cast(bf16x2, b), c, false); }
DI float sum_q(float x) {
  auto s = __builtin_amdgcn_permlane16_swap(__float_as_uint(x), __float_as_uint(x), false, false);
  x = __uint_as_float(s[0]) + __uint_as_float(s[1]);
  auto t = __builtin_amdgcn_permlane32_swap(__float_as_uint(x), __float_as_uint(x), false, false);
  return __uint_as_float(t[0]) + __uint_as_float(t[1]);
}
DI float sum8(float x) {
  x += __builtin_bit_cast(float, __builtin_amdgcn_update_dpp(0, __builtin_bit_cast(int, x), 0xB1, 0xf, 0xf, true));
  x += __builtin_bit_cast(float, __builtin_amdgcn_update_dpp(0, __builtin_bit_cast(int, x), 0x4E, 0xf, 0xf, true));
  x += __builtin_bit_cast(float, __builtin_amdgcn_update_dpp(0, __builtin_bit_cast(int, x), 0x141, 0xf, 0xf, true));
  return x;
}
template <int N> struct IC { static constexpr int value = N; };
template <int N, int I = 0, class Fn> DI void static_for(Fn&& f) { if constexpr (I < N) { f(IC<I>{}); static_for<N, I + 1>(f); } }
DI int swap23(int r) { return (r & ~12) | ((r & 4) << 1) | ((r & 8) >> 1); }
DI bf16x8 lds16(lds_cptr p) { return *(const __attribute__((address_space(3))) bf16x8*)p; }
typedef short v4i16_t __attribute__((ext_vector_type(4)));
DI s16x4 ldstr(lds_cptr p) { return __builtin_bit_cast(s16x4, __builtin_amdgcn_ds_read_tr16_b64_v4i16((__attribute__((address_space(3))) v4i16_t*)p)); }

#define OWN_ALL_VGPRS() asm volatile("" ::: "v255")
template <int R> DI float vget() { float r; asm volatile("v_mov_b32 %0, v[%c1]" : "=v"(r) : "n"(R)); return r; }
template <int R0, int R1> DI float vadd() { float r; asm volatile("v_add_f32 %0, v[%c1], v[%c2]" : "=v"(r) : "n"(R0), "n"(R1)); return r; }
template <int R> DI f32x4 vget4() { return f32x4{vget<R>(), vget<R + 1>(), vget<R + 2>(), vget<R + 3>()}; }
template <int R> DI void vset(unsigned v) { asm volatile("v_mov_b32 v[%c1], %0" ::"v"(v), "n"(R)); }
template <int R, int OFF> DI void gload4(const char* sbase, unsigned voff) {
  asm volatile("global_load_dwordx4 v[%c2:%c2+3], %0, %1 offset:%c3" ::"v"(voff), "s"(sbase), "n"(R), "n"(OFF) : "memory");
}
constexpr int AUX_SC1 = 16;
DI __amdgpu_buffer_rsrc_t mk_rsrc(const char* p, unsigned bytes) { return __builtin_amdgcn_make_buffer_rsrc((void*)p, 0, bytes, 0x00020000); }
DI void st_sc1(u32x4 v, __amdgpu_buffer_rsrc_t rs, unsigned voff, unsigned soff) { __builtin_amdgcn_raw_buffer_store_b128(v, rs, voff, soff, AUX_SC1); }
DI void st_sc1(u32x2 v, __amdgpu_buffer_rsrc_t rs, unsigned voff, unsigned soff) { __builtin_amdgcn_raw_buffer_store_b64(v, rs, voff, soff, AUX_SC1); }

DI u32x4 mk_srd(const char* p, unsigned bytes) { const unsigned long long v = (unsigned long long)p; return u32x4{(unsigned)v, (unsigned)(v >> 32) & 0xffffu, bytes, 0x00020000u}; }
DI void bdma_sc1(u32x4 srd, unsigned voff, unsigned soff, unsigned ldsdst) {
  asm volatile("s_mov_b32 m0, %3\n\ts_nop 0\n\tbuffer_load_dwordx4 %0, %1, %2 offen sc1 lds" ::"v"(voff), "s"(srd), "s"(soff), "s"(ldsdst) : "memory");
}
DI void bdma4_sc1(u32x4 srd, unsigned voff, unsigned soff, unsigned ldsdst) {
  asm volatile("s_mov_b32 m0, %3\n\ts_nop 0\n\tbuffer_load_dwordx4 %0, %1, %2 offen sc1 lds\n\tbuffer_load_dwordx4 %0, %1, %2 offen offset:1024 sc1 lds\n\t"
               "buffer_load_dwordx4 %0, %1, %2 offen offset:2048 sc1 lds\n\tbuffer_load_dwordx4 %0, %1, %2 offen offset:3072 sc1 lds" ::"v"(voff), "s"(srd), "s"(soff), "s"(ldsdst) : "memory");
}
DI void bdma(u32x4 srd, unsigned voff, unsigned soff, unsigned ldsdst) {
  asm volatile("s_mov_b32 m0, %3\n\ts_nop 0\n\tbuffer_load_dwordx4 %0, %1, %2 offen lds" ::"v"(voff), "s"(srd), "s"(soff), "s"(ldsdst) : "memory");
}

#define P1_RD(n, NXT, S2N, OFF)                                                                                                    \
  ".if " #n " < 4\n\tds_read_b64_tr_b16 v[" #NXT "+16+4*" #n ":" #NXT "+17+4*" #n "], v[246+" #n "] offset:" #OFF "+8192*" #S2N "\n\t"    \
  "ds_read_b64_tr_b16 v[" #NXT "+18+4*" #n ":" #NXT "+19+4*" #n "], v[246+" #n "] offset:" #OFF "+8192*" #S2N "+1024\n\t"                \
  ".elseif " #n " < 8\n\tds_read_b128 v[" #NXT "+4*(" #n "-4):" #NXT "+4*(" #n "-4)+3], v[244+" #S2N "] offset:" #OFF "+4096*((" #n "-4)/2)+512*(" #n "&1)\n\t.endif\n\t"
#define P1_STEP(SET, NXT, S2N, OFF, S2)                                                                                            \
  ".set n_, 0\n\t.rept 16\n\t.set m_, n_/4\n\t.set e_, n_&3\n\t"                                                                    \
  ".if %c[tf]\n\tv_mfma_f32_16x16x32_bf16 v[96+4*n_:99+4*n_], v[" #SET "+16+4*e_:" #SET "+19+4*e_], v[" #SET "+4*m_:" #SET "+3+4*m_], v[96+4*n_:99+4*n_]\n\t" \
  ".else\n\tv_mfma_f32_16x16x32_bf16 v[96+4*n_:99+4*n_], v[" #SET "+4*m_:" #SET "+3+4*m_], v[" #SET "+16+4*e_:" #SET "+19+4*e_], v[96+4*n_:99+4*n_]\n\t.endif\n\t" \
  P1_RD(n_, NXT, S2N, OFF)                                                                                                         \
  ".set n_, n_+1\n\t.endr\n\t"                                                                                                     \
  "s_cmp_lg_u32 %[wm], " #S2 "\n\ts_cbranch_scc1 1f\n\t.set e_, 0\n\t.rept 4\n\t"                                                   \
  ".if %c[tf]\n\tv_mfma_f32_16x16x32_bf16 v[160+4*e_:163+4*e_], v[" #SET "+16+4*e_:" #SET "+19+4*e_], v[240:243], v[160+4*e_:163+4*e_]\n\t" \
  ".else\n\tv_mfma_f32_16x16x32_bf16 v[160+4*e_:163+4*e_], v[240:243], v[" #SET "+16+4*e_:" #SET "+19+4*e_], v[160+4*e_:163+4*e_]\n\t.endif\n\t" \
  ".set e_, e_+1\n\t.endr\n\t1:\n\t"
template <bool TF> DI void p1_periods(int wm, unsigned n2) {
  asm volatile(
      "2:\n\t"
      P1_STEP(176, 208, 1, 0, 0) "s_waitcnt lgkmcnt(0)\n\ts_barrier\n\t"
      P1_STEP(208, 176, 0, 32768, 1) "s_waitcnt lgkmcnt(0)\n\t"
      P1_STEP(176, 208, 1, 32768, 0) "s_waitcnt lgkmcnt(0)\n\ts_barrier\n\t"
      ".set i_, 244\n\t.rept 6\n\tv_xor_b32 v[i_], 0x10000, v[i_]\n\t.set i_, i_+1\n\t.endr\n\t"
      P1_STEP(208, 176, 0, 0, 1)
      "s_sub_u32 %[cnt], %[cnt], 1\n\ts_cmp_lg_u32 %[cnt], 0\n\ts_waitcnt lgkmcnt(0)\n\ts_cbranch_scc1 2b\n\t"
      "s_nop 7"
      : [cnt] "+s"(n2) : [wm] "s"(wm), [tf] "n"(TF ? 1 : 0) : "memory", "scc");
}
typedef __attribute__((address_space(3))) f32x4 lf32x4;
typedef __attribute__((address_space(3))) f32x2 lf32x2;

template <int P> DI void p1_store_t(u32x4 srd, unsigned a0, unsigned a1, unsigned rec, unsigned long long tails) {
  unsigned long long sv;
  asm volatile(
      "s_mov_b64 %0, exec\n\t.set m_, 0\n\t.rept 4\n\t.set r_, 96+16*m_+8*%c6\n\t.set i_, 0\n\t.rept 4\n\t"
      "v_pk_fma_f32 v[r_+2*i_:r_+2*i_+1], v[192+2*m_:193+2*m_], v[160+8*%c6+2*i_:161+8*%c6+2*i_], v[r_+2*i_:r_+2*i_+1] op_sel_hi:[0,1,1] neg_lo:[1,0,0] neg_hi:[1,0,0]\n\t.set i_, i_+1\n\t.endr\n\t"
      ".set i_, 0\n\t.rept 4\n\tv_pk_mul_f32 v[r_+2*i_:r_+2*i_+1], v[r_+2*i_:r_+2*i_+1], v[192+2*m_:193+2*m_] op_sel:[0,1]\n\t.set i_, i_+1\n\t.endr\n\t"
      ".set i_, 0\n\t.rept 4\n\tv_cvt_pk_bf16_f32 v[r_+i_], v[r_+2*i_], v[r_+2*i_+1]\n\t.set i_, i_+1\n\t.endr\n\t"
      ".if m_ & 1\n\t"
      "s_mov_b32 exec_lo, 0\n\tv_swap_b32 v[r_-16], v[r_-14]\n\tv_swap_b32 v[r_-15], v[r_-13]\n\tv_swap_b32 v[r_], v[r_+2]\n\tv_swap_b32 v[r_+1], v[r_+3]\n\t"
      "s_mov_b64 exec, %5\n\tv_swap_b32 v[r_-14], v[r_]\n\tv_swap_b32 v[r_-13], v[r_+1]\n\ts_mov_b64 exec, %0\n\t"
      ".if m_ == 1\n\tbuffer_store_dwordx4 v[r_-16:r_-13], %1, %3, 0 offen sc1\n\tbuffer_store_dwordx4 v[r_:r_+3], %2, %3, 0 offen sc1\n\t"
      ".else\n\tbuffer_store_dwordx4 v[r_-16:r_-13], %1, %3, %4 offen sc1\n\tbuffer_store_dwordx4 v[r_:r_+3], %2, %3, %4 offen sc1\n\t.endif\n\t"
      ".endif\n\t.set m_, m_+1\n\t.endr"
      : "=&s"(sv) : "v"(a0), "v"(a1), "s"(srd), "s"(rec), "s"(tails), "n"(P) : "memory");
}
template <int A> DI void p1_store_n(u32x4 srd, unsigned a0, unsigned a1, unsigned so) {
  asm volatile(
      ".set e_, 0\n\t.rept 4\n\t.set b_, 0\n\t.rept 2\n\t.set r_, 96+32*%c4+16*b_+4*e_\n\t.set s_, 192+16*%c4+8*b_\n\t"
      "v_pk_fma_f32 v[r_:r_+1], v[160+4*e_:161+4*e_], v[s_:s_+1], v[r_:r_+1] neg_lo:[1,0,0] neg_hi:[1,0,0]\n\t"
      "v_pk_fma_f32 v[r_+2:r_+3], v[160+4*e_:161+4*e_], v[s_+2:s_+3], v[r_+2:r_+3] neg_lo:[1,0,0] neg_hi:[1,0,0]\n\t.set b_, b_+1\n\t.endr\n\t"
      ".set b_, 0\n\t.rept 2\n\t.set r_, 96+32*%c4+16*b_+4*e_\n\t.set s_, 192+16*%c4+8*b_\n\t"
      "v_pk_mul_f32 v[r_:r_+1], v[r_:r_+1], v[s_+4:s_+5]\n\tv_pk_mul_f32 v[r_+2:r_+3], v[r_+2:r_+3], v[s_+6:s_+7]\n\t.set b_, b_+1\n\t.endr\n\t"
      ".set r_, 96+32*%c4+4*e_\n\t"
      "v_cvt_pk_bf16_f32 v[r_], v[r_], v[r_+1]\n\tv_cvt_pk_bf16_f32 v[r_+1], v[r_+2], v[r_+3]\n\tv_cvt_pk_bf16_f32 v[r_+2], v[r_+16], v[r_+17]\n\tv_cvt_pk_bf16_f32 v[r_+3], v[r_+18], v[r_+19]\n\t"
      ".if e_ & 1\n\tbuffer_store_dwordx4 v[r_:r_+3], %1, %2, %3 offen offset:512*(e_/2) sc1\n\t.else\n\tbuffer_store_dwordx4 v[r_:r_+3], %0, %2, %3 offen offset:512*(e_/2) sc1\n\t.endif\n\t"
      ".set e_, e_+1\n\t.endr"
      ::"v"(a0), "v"(a1), "s"(srd), "s"(so), "n"(A) : "memory");
}
template <bool TF, class Fn>
DI void p1_compute(u32x4 srd, __amdgpu_buffer_rsrc_t rsw, lds_ptr lds, int rb, int g, int w, int lane, Fn started STAMP_PAR) {
  const int q = lane >> 4, i16 = lane & 15, wm = w >> 1, wn = w & 1;
  {
    const unsigned xa = 16384 + (64 * wm + 8 * (i16 >> 2) + (i16 & 3)) * 128 + ((q ^ (i16 >> 1)) * 16);
    const int p4 = lane & 3, qq = (lane >> 2) & 3;
    const unsigned wa = (8 * q + qq) * 256 + (((8 * wn + p4) ^ (2 * (qq + 4 * (q & 1)))) * 16) + 8 * (p4 >> 1);
    vset<244>(xa); vset<245>(xa ^ 64); vset<246>(wa); vset<247>(wa ^ 8); vset<248>(wa ^ 64); vset<249>(wa ^ 72);
  }
  const int kt0 = 4 * rb + 2 * wm;
  const unsigned rec = g < 5 ? QF_TILE : KV_BLK;
  unsigned ad[4], sta = 16384 + wm * 512 + (TF ? i16 * 4 : q * 16);
  unsigned long long tl[2];
  if (TF) {
    const bool isq = g < 5;
    const unsigned hst = isq ? 64 * QF_TILE : 1280;
    const int r0 = 8 * (i16 >> 2) + (i16 & 3);
#pragma unroll
    for (int P = 0; P < 2; ++P) {
      const int cq = 16 * (isq ? g : g - 5) + 8 * wn + 4 * P + q, i5 = (cq * 205) >> 10, m5 = cq - 5 * i5;
      const unsigned base = (isq ? OFF_QF : OFF_KV) + (2 * i5 + (m5 > 2)) * hst + kt0 * rec;
      const unsigned oct = base + (m5 > 2 ? m5 - 3 : m5) * 512, tail = base + 1024 + (8 * (i16 >> 2) + 2 * (i16 & 3)) * 8;
      ad[2 * P] = m5 == 2 ? tail : oct + (isq ? r0 : swap23(r0)) * 16;
      ad[2 * P + 1] = m5 == 2 ? tail + hst : oct + (isq ? r0 + 4 : swap23(r0 + 4)) * 16;
      tl[P] = __ballot(m5 == 2);
    }
  } else {
    const int p4 = i16 >> 2, cl = 64 * wn + 8 * p4 + (i16 & 3);
    ad[0] = q * 10240 + (cl + 4 * (p4 >> 1)) * 16; ad[1] = q * 10240 + (cl + 4 - 4 * (p4 >> 1)) * 16; ad[2] = ad[3] = 0; tl[0] = tl[1] = 0;
  }
  asm volatile("" : "+v"(ad[0]), "+v"(ad[1]), "+v"(ad[2]), "+v"(ad[3]), "+v"(sta));
  asm volatile(".set i_, 96\n\t.rept 80\n\tv_mov_b32 v[i_], 0\n\t.set i_, i_+1\n\t.endr\n\t"
               "v_mov_b32 v240, 0x3f803f80\n\tv_mov_b32 v241, v240\n\tv_mov_b32 v242, v240\n\tv_mov_b32 v243, v240\n\t"
               "s_waitcnt lgkmcnt(0)\n\ts_barrier\n\t.set n_, 0\n\t.rept 8\n\t" P1_RD(n_, 176, 0, 0) ".set n_, n_+1\n\t.endr\n\ts_waitcnt lgkmcnt(0)" ::: "memory");
  STAMP(41);
  started();
  p1_periods<TF>(wm, 3);
  if (w == 0) asm volatile("buffer_load_dwordx4 v[250:253], %0, %1, %2 offen sc1" ::"v"(lane * 16), "s"(srd), "s"(OFF_RDY) : "memory");
  p1_periods<TF>(wm, 2);
  STAMP(42);
  asm volatile(".set i_, 0\n\t.rept 4\n\tds_write_b128 %0, v[160+4*i_:163+4*i_] offset:1024*i_\n\t.set i_, i_+1\n\t.endr\n\ts_waitcnt lgkmcnt(0)\n\ts_barrier\n\t"
               ".set i_, 0\n\t.rept 4\n\tds_read_b128 v[176+4*i_:179+4*i_], %1 offset:1024*i_\n\t.set i_, i_+1\n\t.endr\n\t"
               ".set i_, 0\n\t.rept 4\n\t.if %c3\n\tds_read2_b32 v[192+2*i_:193+2*i_], %2 offset0:32*i_ offset1:32*i_+16\n\t"
               ".else\n\tds_read_b128 v[192+8*i_:195+8*i_], %2 offset:128*i_\n\tds_read_b128 v[196+8*i_:199+8*i_], %2 offset:128*i_+64\n\t.endif\n\t.set i_, i_+1\n\t.endr\n\t"
               "s_waitcnt lgkmcnt(0)\n\t.set i_, 0\n\t.rept 8\n\tv_pk_add_f32 v[160+2*i_:161+2*i_], v[160+2*i_:161+2*i_], v[176+2*i_:177+2*i_]\n\t.set i_, i_+1\n\t.endr"
               ::"v"(w * 4096 + lane * 16), "v"((w ^ 2) * 4096 + lane * 16), "v"(sta), "n"(TF ? 1 : 0) : "memory");
  STAMP(46);
  if (TF) {
    if (g < 5) asm volatile(".set i_, 0\n\t.rept 4\n\tv_mul_f32 v[193+2*i_], %0, v[193+2*i_]\n\t.set i_, i_+1\n\t.endr" ::"s"(QSCALE));
    static_for<2>([&](auto P_) AI { p1_store_t<P_.value>(srd, ad[2 * P_.value], ad[2 * P_.value + 1], rec, tl[P_.value]); });
  } else {
    static_for<2>([&](auto A_) AI { p1_store_n<A_.value>(srd, ad[0], ad[1], OFF_KV + ((kt0 & 32) | ((kt0 + A_.value + 1) & 31)) * KV_BLK + 40960 + (g - 10) * 2048); });
  }
}

struct P1Load { u32x4 srd; unsigned vo[4], pstep, hstep, dst; int rot; };
DI void p1_issue(const P1Load& L, int p) {
  int pp = p + L.rot; pp -= pp >= 10 ? 10 : 0;
  const unsigned so = pp * L.pstep, d = (p & 3) * 32768 + L.dst;
#pragma unroll
  for (int i = 0; i < 8; ++i) bdma(L.srd, L.vo[i & 3], so + (i >> 2) * L.hstep, d + i * 1024);
}
DI P1Load p1_start(const char* x, const char* wqkv, int rb, int g, int rot, int lw, int lane) {
  const bool isx = lw >= 2;
  const int hf = lw & 1, sh = isx ? 3 : 4;
  P1Load L;
  const unsigned stride = isx ? 1280 : 3840, r0 = isx ? 128 * rb + 64 * hf : 32 * hf, col = isx ? 0 : 256 * g;
  const unsigned base = (r0 + (lane >> sh)) * stride + col, ch = (lane & ((1 << sh) - 1)) ^ (((lane >> 4) & (isx ? 1 : 3)) << (isx ? 0 : 1));
#pragma unroll
  for (int i = 0; i < 4; ++i) L.vo[i] = base + i * (isx ? 10240 : 15360) + 16 * (ch ^ (isx ? 2 * i : 8 * (i >> 1)));
  asm volatile("" : "+v"(L.vo[0]), "+v"(L.vo[1]), "+v"(L.vo[2]), "+v"(L.vo[3]));
  asm volatile("" : "+s"(x), "+s"(wqkv));
  L.srd = mk_srd(isx ? x : wqkv, isx ? S_ * D_ * 2 : D_ * 3 * D_ * 2);
  L.pstep = isx ? 128 : 64 * 3840; L.hstep = isx ? 32 * 1280 : 16 * 3840; L.dst = lw * 8192; L.rot = rot;
  p1_issue(L, 0); p1_issue(L, 1); p1_issue(L, 2);
  return L;
}
template <class Fn> DI void p1_stats(const char* x, __amdgpu_buffer_rsrc_t rsw, int rb, int w, int lane, Fn landed) {
  const char* p = x + (size_t)(128 * rb + 16 * w + (lane >> 3)) * 1280 + (lane & 7) * 16;
  u32x4 v[2][10];
#pragma unroll
  for (int h = 0; h < 2; ++h)
#pragma unroll
    for (int k = 0; k < 10; ++k) v[h][k] = *(const u32x4*)(p + h * 8 * 1280 + k * 128);
  asm volatile("s_waitcnt vmcnt(20)" ::: "memory");
  landed();
#pragma unroll
  for (int h = 0; h < 2; ++h) {
    f32x4 sm4 = {0.f, 0.f, 0.f, 0.f}, sq4 = sm4;
#pragma unroll
    for (int k = 0; k < 10; ++k)
#pragma unroll
      for (int d = 0; d < 4; ++d) { sm4[d] = dot2(v[h][k][d], 0x3f803f80u, sm4[d]); sq4[d] = dot2(v[h][k][d], v[h][k][d], sq4[d]); }
    float sm = (sm4[0] + sm4[1]) + (sm4[2] + sm4[3]), sq = (sq4[0] + sq4[1]) + (sq4[2] + sq4[3]);
    sm = sum8(sm); sq = sum8(sq);
    const float mean = sm * (1.f / D_);
    const float rstd = __builtin_amdgcn_rsqf(fmaxf(sq * (1.f / D_) - mean * mean, 0.f) + LN_EPS);
    const int t = 16 * w + 8 * h + (lane >> 3);
    __builtin_amdgcn_raw_buffer_store_b32(__float_as_uint((lane & 1) ? rstd : mean), rsw,
        (lane & 6) == 0 ? (2 * (t >> 5) + ((t >> 2) & 1)) * 128 + (lane & 1) * 64 + (4 * ((t >> 3) & 3) + (t & 3)) * 4 : 0x80000000u, OFF_XST + rb * 1024, AUX_SC1);
  }
  { const unsigned c = lane ? 0u : 0x3f803f80u; st_sc1(u32x4{c, c, c, c}, rsw, (lane < 2 && w < 4) ? 81920 + lane * 16 : 0x80000000u, OFF_KV + (4 * rb + (w & 3)) * KV_BLK); }
  asm volatile("s_waitcnt vmcnt(0) lgkmcnt(0)\n\ts_barrier" ::: "memory");
}

constexpr int KV_HALF = 11 * 1024, KV_SLOT = 2 * KV_HALF;
constexpr int L_KV0 = 4 * GRP_B, L_WRAP = 144 * 1024, L_CS1 = 6 * GRP_B, L_CS3 = L_CS1 + 320, L_FILL = L_CS3 + 960, L_CSF = L_FILL + 4, L_EPO = L_FILL + 12,
              L_OWN = L_FILL + 16, L_CONS = L_FILL + 64, L_SCR2 = L_CONS + 256, K2_LDS = L_SCR2 + 6144 + 512;
constexpr int kv_at(int c) { return (c < 2 || c > 5) ? L_KV0 + (c & 1) * KV_SLOT : (c - 2) * KV_SLOT; }
#define PRIO_H 0
#define PSL 3
#define GDIST 12
#define JS 0
#define ROT 10
#define ROTF (ROT == 10 ? (g1 + 5 * (r & 1) + xcd) % 10 : ROT == 5 ? 2 * ((g1 + 2 * (r & 1) + xcd) % 5) : ROT == 3 ? 3 * ((g1 + (r & 1) + xcd) % 3) : ROT == 2 ? 5 * ((g1 + r + xcd) & 1) : 0)
#define FPOS 1
#define POSTP 14
constexpr unsigned SPIN_CAP = 20000, LDS_SPIN = 0x20000;

DI u32x4 ld_sc1(__amdgpu_buffer_rsrc_t rs, unsigned voff, unsigned soff) { return __builtin_amdgcn_raw_buffer_load_b128(rs, voff, soff, AUX_SC1); }
DI unsigned nosent(unsigned u) { return min(u, SENT - 1u); }
DI unsigned umax3(unsigned a, unsigned b, unsigned c) { return max(max(a, b), c); }
DI int tail_slot(int tokn) { return (tokn & 24) | ((tokn & 3) << 1) | ((tokn >> 2) & 1); }
DI unsigned kv_src(int n, int head) {
  const int pl = ((n - 80) * 745) >> 14, row = n - 80 - 22 * pl;
  return n < 80 ? head * 1280 + n * 16 : row < 20 ? 40960 + pl * 10240 + (20 * head + (row < 16 ? swap23(row) : row)) * 16 : 81920 + (row - 20) * 16;
}
DI unsigned kv_cell(int n, int half, int head) { const int rec = (n * 1561) >> 18; return (32 * half + min(rec, 3)) * KV_BLK + kv_src(n - 168 * rec, head); }
struct Setup { unsigned kvs[3], kvw, kA, kB, vA, qv0, qv1, jo0, jo4, jvo[2], jcs, jso, jcso; int mat; };
DI Setup make_setup(int lane, int w, int bid, int head) {
  asm volatile("" : "+v"(lane));
  asm volatile("" : "+s"(w));
  Setup s;
  const int hi = lane >> 5, r32 = lane & 31, lq = lane >> 4, lt = lane & 15;
#pragma unroll
  for (int i = 0; i < 3; ++i) { const int pc = min(w + 8 * i, 21), h2 = pc >= 11; s.kvs[i] = kv_cell((pc - 11 * h2) * 64 + lane, h2, head); }
  { const int i = min(w, 5), h2 = i >= 3; s.kvw = kv_cell((i - 3 * h2) * 64 + lane, h2, head); }
  const unsigned kvb = L_KV0 + (w >> 2) * KV_HALF;
  s.kA = kvb + hi * 512 + r32 * 16; s.kB = kvb + 1024 + tail_slot(swap23(r32)) * 8; s.vA = kvb + 1280 + hi * 352 + min(r32, 21) * 16;
  s.qv0 = hi * 512 + r32 * 16; s.qv1 = hi * QF_TILE + tail_slot(r32) * 8;
  const int job = w * 256 + bid;
  int mat, ks, cb;
  if (job < 300) { mat = job / 100; const int e = job - 100 * mat; ks = e / 5; cb = e - 5 * ks; }
  else if (job < 580) { mat = 3; const int e = job - 300; ks = e / 14; cb = e - 14 * ks; }
  else { mat = 4; const int e = job - 580; ks = e / 5; cb = e - 5 * ks; }
  int kpos = ks;
  if (mat == 4) { const int half = ks >= 27, k27 = ks - 27 * half, j4 = min(k27 / 7, 3), jj = 4 * half + j4, pi = k27 - 7 * j4; kpos = pi < 2 ? 2 * jj + pi : pi < 5 ? 14 + 3 * jj + pi : 35 + 2 * jj - (jj > 3) + pi; }
  s.mat = mat;
  s.jso = OFF_WF + ((kpos / 5) * 25 + (kpos % 5) * 5) * 1024;
  s.jcso = mat == 1 ? OFF_CS1 + (ks * 5 + cb) * 512 : OFF_CS3 + (ks * 14 + cb) * 512;
  const int jn = mat == 3 ? F_ : D_, n0 = 128 * cb + 8 * lt, kk = 4 * cb + (lt >> 2);
  int k0 = 32 * ks + 8 * lq, k4 = k0 + 4;
  if (mat == 0) { const int o = 4 * ks + lq, i = (o * 205) >> 10, oo = o - 5 * i; k0 = 40 * i + (oo < 3 ? 8 * oo : 8 * oo - 4); k4 = k0 + (oo == 2 ? 20 : 4); }
  s.jo0 = (k0 * jn + (n0 < jn ? n0 : 0)) * 2; s.jo4 = (k4 * jn + (n0 < jn ? n0 : 0)) * 2;
#pragma unroll
  for (int h = 0; h < 2; ++h) {
    int jj, t, pos, G;
    if (mat == 3) {
      const int half = kk >= 27, k27 = kk - 27 * half, j4 = min((k27 * 37) >> 8, 3);
      jj = 4 * half + j4; t = 2 * (k27 - 7 * j4) + h; pos = lt & 3;
      const int pass = (t * 13) >> 6; t -= 5 * pass; G = 12 + 4 * pass;
    } else {
      const int per5 = (kk * 13) >> 6, m5 = kk - 5 * per5;
      const bool single = m5 == 2;
      jj = 2 * per5 + (single ? ((lt >> 1) & 1) : (m5 > 2)); t = single ? 4 : 2 * (m5 > 2 ? m5 - 3 : m5) + h;
      pos = single ? 2 * (lt & 1) + h : lt & 3;
      G = mat == 4 ? 24 : 4 * mat;
    }
    s.jvo[h] = (mat != 3 || kk < 54) ? (unsigned)((jj * NGRP + G) * 25 + t) * 1024 + (lq * 16 + pos) * 16 : 0x80000000u;
  }
  s.jcs = lt < 2 ? lt * 64 + lq * 16 : 0x80000000u;
  asm volatile("" : "+v"(s.kvs[0]), "+v"(s.kvs[1]), "+v"(s.kvs[2]), "+v"(s.kvw), "+v"(s.kA), "+v"(s.kB), "+v"(s.vA), "+v"(s.qv0), "+v"(s.qv1), "+v"(s.jo0), "+v"(s.jo4),
               "+v"(s.jvo[0]), "+v"(s.jvo[1]), "+v"(s.jcs));
  asm volatile("" : "+s"(s.jso), "+s"(s.jcso), "+s"(s.mat));
  return s;
}
DI f32x16 attn_safe(__amdgpu_buffer_rsrc_t rs, unsigned qp, int head, int lane) {
  const int hi = lane >> 5, r32 = lane & 31;
  const bf16x8 q1 = __builtin_bit_cast(bf16x8, ld_sc1(rs, hi * 512 + r32 * 16, qp));
  const u32x2 q2l = __builtin_amdgcn_raw_buffer_load_b64(rs, 1024 + tail_slot(r32) * 8, qp, AUX_SC1);
  const bf16x8 q2 = __builtin_bit_cast(bf16x8, u32x4{hi ? 0u : q2l[0], hi ? 0u : q2l[1], 0u, 0u});
  f32x16 O = {};
  float m = -3.0e38f;
#pragma unroll 1
  for (int t = 0; t < 64; ++t) {
    const unsigned kb = OFF_KV + t * KV_BLK + head * 1280, vb = OFF_KV + ((t & 32) | ((t + 1) & 31)) * KV_BLK;
    const bf16x8 k1 = __builtin_bit_cast(bf16x8, ld_sc1(rs, hi * 512 + r32 * 16, kb));
    const u32x2 k2l = __builtin_amdgcn_raw_buffer_load_b64(rs, 1024 + tail_slot(swap23(r32)) * 8, kb, AUX_SC1);
    const bf16x8 v0 = __builtin_bit_cast(bf16x8, ld_sc1(rs, kv_src(80 + 22 * hi + min(r32, 21), head), vb)),
                 v1 = __builtin_bit_cast(bf16x8, ld_sc1(rs, kv_src(124 + 22 * hi + min(r32, 21), head), vb));
    f32x16 sc = MFMA32(k1, q1, f32x16{});
    sc = MFMA32(__builtin_bit_cast(bf16x8, u32x4{k2l[0], k2l[1], 0u, 0u}), q2, sc);
    float tm = sc[0];
#pragma unroll
    for (int i = 1; i < 16; ++i) tm = fmaxf(tm, sc[i]);
    { auto rr = __builtin_amdgcn_permlane32_swap(__float_as_uint(tm), __float_as_uint(tm), false, false); tm = fmaxf(__uint_as_float(rr[0]), __uint_as_float(rr[1])); }
    const float mn = fmaxf(m, tm), al = __builtin_amdgcn_exp2f(m - mn);
    m = mn;
    u32x4 p0, p1;
#pragma unroll
    for (int i = 0; i < 4; ++i) {
      p0[i] = pack2(__builtin_amdgcn_exp2f(sc[2 * i] - mn), __builtin_amdgcn_exp2f(sc[2 * i + 1] - mn));
      p1[i] = pack2(__builtin_amdgcn_exp2f(sc[8 + 2 * i] - mn), __builtin_amdgcn_exp2f(sc[9 + 2 * i] - mn));
    }
#pragma unroll
    for (int i = 0; i < 16; ++i) O[i] *= al;
    O = MFMA32(v0, __builtin_bit_cast(bf16x8, p0), O);
    O = MFMA32(v1, __builtin_bit_cast(bf16x8, p1), O);
  }
  return O;
}

constexpr int R_A = 128;
template <int A0, int N> DI void ld_frags(u32x4 srd, unsigned voff, unsigned soff) {
  unsigned t;
  asm volatile("s_add_u32 %0, %3, 4096\n\t.set i_, 0\n\t.rept %c5\n\t"
               ".if i_ < 4\n\tbuffer_load_dwordx4 v[%c4+4*i_:%c4+4*i_+3], %1, %2, %3 offen offset:1024*i_ sc1\n\t"
               ".else\n\tbuffer_load_dwordx4 v[%c4+4*i_:%c4+4*i_+3], %1, %2, %0 offen sc1\n\t.endif\n\t.set i_, i_+1\n\t.endr"
               : "=&s"(t) : "v"(voff), "s"(srd), "s"(soff), "n"(R_A + 4 * A0), "n"(N) : "memory", "scc");
}
DI void ld_unit(u32x4 srd, const unsigned (&vo)[4], unsigned soff) {
  ld_frags<0, 5>(srd, vo[0], soff); ld_frags<5, 5>(srd, vo[1], soff + 5120); ld_frags<10, 5>(srd, vo[2], soff + 10240); ld_frags<15, 5>(srd, vo[3], soff + 15360);
}
DI void ld_lead(u32x4 srd, const unsigned (&vo)[4], unsigned soff) { ld_frags<0, 5>(srd, vo[0], soff); ld_frags<5, 3>(srd, vo[1], soff + 5120); }
template <int G> DI void w_prime(const unsigned (&wb)[6]) {
  unsigned u;
  asm volatile("s_mov_b32 %0, 0\n\t1:\n\tds_read_b32 v254, %2\n\ts_add_u32 %0, %0, 1\n\ts_cmp_gt_u32 %0, 0x20000\n\ts_waitcnt lgkmcnt(0)\n\tv_cmp_gt_u32 vcc, %c3+1, v254\n\t"
               "s_cbranch_scc1 2f\n\ts_cbranch_vccnz 1b\n\t2:\n\t"
               ".set i_, 0\n\t.rept 5\n\tds_read_b128 v[208+4*i_:211+4*i_], %1 offset:%c4+1024*i_\n\t.set i_, i_+1\n\t.endr\n\ts_waitcnt lgkmcnt(0)"
               : "=&s"(u) : "v"(wb[(G % 6) >> 1]), "v"(wb[3]), "n"(G), "n"(((G % 6) & 1) * GRP_B) : "memory", "scc", "vcc");
}
#define GST_A
#define GST_B
#define GST_CL
constexpr unsigned long long vmw(int a, int b = 63, int c = 63, int d = 63, int e = 63, int f = 63) {
  return (unsigned long long)a | (unsigned long long)b << 6 | (unsigned long long)c << 12 | (unsigned long long)d << 18 | (unsigned long long)e << 24 | (unsigned long long)f << 30;
}
template <int G0, int NG, int KS0, int KSE, unsigned long long VMW, int LEAD, int NRL, int RS, int RB, int XG, int XOP, int AVM = -1, bool TAIL = true>
DI unsigned long long run_unit(const unsigned (&wb)[6], u32x4 srd, unsigned voff, unsigned soff, unsigned xoff, unsigned aoff,
                               u32x4 asrd, const unsigned (&vq)[4], unsigned aso STAMP_PAR) {
  unsigned long long m; unsigned u, so;
#define DBG_INC
#define DBG_OP
  asm volatile(
      "s_mov_b32 %[so], %[soff]\n\t"
      ".set gi_, 0\n\t.rept %c[ng]\n\t"
      ".set g_, %c[g0]+gi_\n\t.set gn_, g_+1\n\t.if gn_ > 34\n\t.set gn_, 34\n\t.endif\n\t"
      ".set sc_, g_-6*(g_/6)\n\t.set sn_, gn_-6*(gn_/6)\n\t.set nk_, %c[kse]-%c[ks0]-5*gi_\n\t.if nk_ > 5\n\t.set nk_, 5\n\t.endif\n\t"
      "ds_read_b32 v254, %[fl]\n\t" GST_A
      ".set vw_, (%c[vmw]>>(6*gi_))&63\n\t.if vw_ < 63\n\ts_waitcnt vmcnt(vw_)\n\t.endif\n\t"
      GST_B ".set k_, 0\n\t.rept nk_\n\t"
      ".set kr_, 5*gi_+k_\n\t.set ks_, %c[ks0]+kr_\n\t.set a_, 128+4*(ks_-20*(ks_/20))\n\t"
      ".if %c[lead]\n\t.set vw_, 19-ks_\n\t.if vw_ > %c[lead]-1\n\t.set vw_, %c[lead]-1\n\t.endif\n\t.if %c[xop] == 1 && gi_ > %c[xg]\n\t.set vw_, vw_+1\n\t.endif\n\ts_waitcnt vmcnt(vw_)\n\t.endif\n\t"
      ".set last_, %c[lead] && ks_ == 19 && g_ < 12\n\t"
      ".if k_ == nk_-1 && !last_\n\t"
      "s_cbranch_vccnz 3f\n\t2:\n\t.subsection 1\n\t3:\n\ts_mov_b32 %[u], 0\n\t"
      "1:\n\t" DBG_INC "s_sleep 1\n\tds_read_b32 v254, %[fl]\n\ts_add_u32 %[u], %[u], 1\n\ts_cmp_gt_u32 %[u], 0x20000\n\ts_waitcnt lgkmcnt(0)\n\tv_cmp_gt_u32 vcc, gn_+1, v254\n\t"
      "s_cbranch_scc1 2b\n\ts_cbranch_vccnz 1b\n\ts_branch 2b\n\t.subsection 0\n\t"
      ".endif\n\t"
      ".set t_, 0\n\t.rept 5\n\t"
      ".set f_, 5*k_+t_\n\t"
      ".if last_ && t_ >= 2\n\t.if (t_ & 1) == 0\n\ts_waitcnt lgkmcnt((4-t_)/2)\n\t.endif\n\t"
      ".elseif f_ == 0 || f_ == 2\n\ts_waitcnt lgkmcnt(5)\n\t.elseif (f_ & 1) == 0\n\ts_waitcnt lgkmcnt(3)\n\t.endif\n\t"
      ".if ks_ == 0\n\tv_mfma_f32_16x16x32_bf16 v[228+4*t_:231+4*t_], v[208+4*t_:211+4*t_], v[a_:a_+3], 0\n\t"
      ".else\n\tv_mfma_f32_16x16x32_bf16 v[228+4*t_:231+4*t_], v[208+4*t_:211+4*t_], v[a_:a_+3], v[228+4*t_:231+4*t_]\n\t.endif\n\t"
      ".set s_, sc_\n\t.set o_, 5120*(k_+1)+1024*t_\n\t.if k_ == nk_-1\n\t.set s_, sn_\n\t.set o_, 1024*t_\n\t.endif\n\t"
      ".if last_\n\t.elseif s_ < 2\n\tds_read_b128 v[208+4*t_:211+4*t_], %[w0] offset:25600*s_+o_\n\t"
      ".elseif s_ < 4\n\tds_read_b128 v[208+4*t_:211+4*t_], %[w1] offset:25600*(s_-2)+o_\n\t"
      ".else\n\tds_read_b128 v[208+4*t_:211+4*t_], %[w2] offset:25600*(s_-4)+o_\n\t.endif\n\t"
      ".if k_ == nk_-2 && t_ == 3\n\tv_cmp_gt_u32 vcc, gn_+1, v254\n\t.endif\n\t"
      ".set t_, t_+1\n\t.endr\n\t"
      ".set fn_, ks_+%c[lead]\n\t.if %c[lead] && fn_ < 20\n\t"
      ".if fn_ < 10\n\tbuffer_load_dwordx4 v[128+4*fn_:131+4*fn_], %[q1], %[asrd], %[aso] offen offset:1024*(fn_&3) sc1\n\t"
      ".elseif fn_ < 15\n\tbuffer_load_dwordx4 v[128+4*fn_:131+4*fn_], %[q2], %[asrd], %[aso] offen offset:1024*(fn_&3) sc1\n\t"
      ".else\n\tbuffer_load_dwordx4 v[128+4*fn_:131+4*fn_], %[q3], %[asrd], %[aso] offen offset:1024*(fn_&3) sc1\n\t.endif\n\t"
      ".if (fn_&3) == 3\n\ts_addk_i32 %[aso], 4096\n\t.endif\n\t.endif\n\t"
      ".set rn_, kr_-%c[rs]\n\t.if rn_ >= 0 && rn_ < %c[nrl]\n\t.set rr_, %c[rb]+rn_\n\t.set rr_, 128+4*(rr_-20*(rr_/20))\n\t"
      "buffer_load_dwordx4 v[rr_:rr_+3], %[voff], %[srd], %[so] offen offset:1024*(rn_&3) sc1\n\t"
      ".if (rn_&3) == 3\n\ts_addk_i32 %[so], 4096\n\t.endif\n\t"
      ".endif\n\t"
      ".set k_, k_+1\n\t.endr\n\t"
      "ds_add_u32 %[cs], %[one]\n\t"
      ".if gi_ == 0 && %c[avm] >= 0\n\ts_waitcnt vmcnt(%c[avm])\n\ts_mov_b64 %[m], exec\n\ts_mov_b64 exec, 0xff\n\tv_mov_b32 v125, 1\n\tbuffer_atomic_add v125, %[ao], %[srd], 0 offen sc1\n\ts_mov_b64 exec, %[m]\n\t.endif\n\t"
      ".if gi_ == %c[xg] && %c[xop] == 1\n\tbuffer_load_dwordx4 v[250:253], %[voff], %[srd], %[so] offen sc1\n\t.endif\n\t"
      ".if gi_ == %c[xg] && %c[xop] >= 2\n\ts_mov_b64 %[m], exec\n\ts_mov_b64 exec, 1\n\tv_mov_b32 v124, 0\n\tbuffer_atomic_add v124, %[xo], %[srd], 0 offen sc0 sc1\n\ts_mov_b64 exec, %[m]\n\t.endif\n\t"
      ".set gi_, gi_+1\n\t.endr\n\t"
      ".if %c[xop] == 1 || %c[xop] == 2\n\ts_waitcnt vmcnt(0)\n\t.endif\n\t"
      ".if %c[tail]\n\ts_nop 4\n\tv_cmp_u_f32 %[m], v228, v228\n\t.endif"
      : [m] "=&s"(m), [u] "=&s"(u), [so] "=&s"(so), [aso] "+s"(aso) DBG_OP
      : [w0] "v"(wb[0]), [w1] "v"(wb[1]), [w2] "v"(wb[2]), [fl] "v"(wb[3]), [cs] "v"(wb[4]), [one] "v"(wb[5]), [voff] "v"(voff), [xo] "v"(xoff), [ao] "v"(aoff), [avm] "n"(AVM), [tail] "n"(TAIL ? 1 : 0), [srd] "s"(srd), [soff] "s"(soff),
        [g0] "n"(G0), [ng] "n"(NG), [ks0] "n"(KS0), [kse] "n"(KSE), [vmw] "n"(VMW), [lead] "n"(LEAD), [q1] "v"(vq[1]), [q2] "v"(vq[2]), [q3] "v"(vq[3]), [asrd] "s"(asrd), [nrl] "n"(NRL), [rs] "n"(RS), [rb] "n"(RB), [xg] "n"(XG), [xop] "n"(XOP)
      : "memory", "scc", "vcc" GST_CL);
  return m;
}
template <bool RELOAD> DI void redo_unit(__amdgpu_buffer_rsrc_t ra, const unsigned (&vo)[4], unsigned soff, int nks, __amdgpu_buffer_rsrc_t rw, unsigned woff, u32x4 srd_a) {
  f32x4 acc[5];
#pragma unroll
  for (int t = 0; t < 5; ++t) acc[t] = f32x4{0.f, 0.f, 0.f, 0.f};
  asm volatile("s_waitcnt vmcnt(0)" ::: "memory");
#pragma unroll 1
  for (int ks = 0; ks < nks; ++ks) {
    const unsigned va = ks < 5 ? vo[0] : ks < 10 ? vo[1] : ks < 15 ? vo[2] : vo[3];
    u32x4 av;
#pragma unroll 1
    for (unsigned sp = 0; sp < SPIN_CAP; ++sp) {
      av = ld_sc1(ra, va, soff + ks * 1024);
      if (!__any(umax3(max(av[0], av[1]), av[2], av[3]) == SENT)) break;
      asm volatile("s_sleep 8" ::: "memory");
    }
#pragma unroll
    for (int t = 0; t < 5; ++t) acc[t] = MFMA16(__builtin_bit_cast(bf16x8, ld_sc1(rw, woff, (ks * 5 + t) * 1024)), __builtin_bit_cast(bf16x8, av), acc[t]);
  }
  static_for<20>([&](auto I_) AI { vset<228 + I_.value>(__float_as_uint(acc[I_.value >> 2][I_.value & 3])); });
  if (RELOAD) { ld_unit(srd_a, vo, soff); asm volatile("s_waitcnt vmcnt(0)" ::: "memory"); }
}

#define EP_PACK(d, r) "v_cvt_pk_bf16_f32 v[" #d "], v[" #r "], v[" #r "+1]\n\tv_cvt_pk_bf16_f32 v[" #d "+1], v[" #r "+2], v[" #r "+3]\n\t"
#define EP_GUARD(d) "v_min_u32 v[" #d "], -2, v[" #d "]\n\tv_min_u32 v[" #d "+1], -2, v[" #d "+1]\n\t"
#define EP_GELU(R, NP)                                                                                                                       \
  ".set i_, 0\n\t.rept " #NP "\n\tv_pk_mul_f32 v[116+2*i_:117+2*i_], v[" #R "+2*i_:" #R "+1+2*i_], v[" #R "+2*i_:" #R "+1+2*i_]\n\t.set i_, i_+1\n\t.endr\n\t" \
  ".set i_, 0\n\t.rept " #NP "\n\tv_pk_fma_f32 v[116+2*i_:117+2*i_], v[116+2*i_:117+2*i_], %[B], %[A]\n\t.set i_, i_+1\n\t.endr\n\t"          \
  ".set i_, 0\n\t.rept " #NP "\n\tv_pk_mul_f32 v[116+2*i_:117+2*i_], v[116+2*i_:117+2*i_], v[" #R "+2*i_:" #R "+1+2*i_]\n\t.set i_, i_+1\n\t.endr\n\t" \
  ".set i_, 0\n\t.rept 2*" #NP "\n\tv_exp_f32 v[116+i_], v[116+i_]\n\t.set i_, i_+1\n\t.endr\n\t"                                           \
  ".set i_, 0\n\t.rept " #NP "\n\tv_pk_fma_f32 v[116+2*i_:117+2*i_], v[116+2*i_:117+2*i_], %[I], %[I]\n\t.set i_, i_+1\n\t.endr\n\t"          \
  ".set i_, 0\n\t.rept 2*" #NP "\n\tv_rcp_f32 v[116+i_], v[116+i_]\n\t.set i_, i_+1\n\t.endr\n\t"                                           \
  ".set i_, 0\n\t.rept " #NP "\n\tv_pk_mul_f32 v[" #R "+2*i_:" #R "+1+2*i_], v[" #R "+2*i_:" #R "+1+2*i_], v[116+2*i_:117+2*i_]\n\t.set i_, i_+1\n\t.endr\n\t"
struct GeluK { f32x2 nm, A, B, I; };
DI GeluK gelu_consts(float mean, float r, float ir) {
  const float c0 = -2.f * 1.4426950408889634f * 0.7978845608028654f, c1 = c0 * 0.044715f, A = c0 * r, B = c1 * r * r * r;
  return GeluK{f32x2{-mean, -mean}, f32x2{A, A}, f32x2{B, B}, f32x2{ir, ir}};
}
template <int T, int NT, int D> DI void ep_gelu(const GeluK& k, f32x4 c0, f32x4 c1) {
  asm volatile(
      "v_pk_fma_f32 v[%c[r]:%c[r]+1], %[nm], %[ca], v[%c[r]:%c[r]+1]\n\tv_pk_fma_f32 v[%c[r]+2:%c[r]+3], %[nm], %[cb], v[%c[r]+2:%c[r]+3]\n\t"
      ".if %c[nt] == 2\n\tv_pk_fma_f32 v[%c[r]+4:%c[r]+5], %[nm], %[cc], v[%c[r]+4:%c[r]+5]\n\tv_pk_fma_f32 v[%c[r]+6:%c[r]+7], %[nm], %[cd], v[%c[r]+6:%c[r]+7]\n\t.endif\n\t"
      EP_GELU(%c[r], 2*%c[nt])
      EP_PACK(%c[d], %c[r]) ".if %c[nt] == 2\n\t" EP_PACK(%c[d]+2, %c[r]+4) ".endif\n\t"
      EP_GUARD(%c[d]) ".if %c[nt] == 2\n\t" EP_GUARD(%c[d]+2) ".endif"
      ::[nm] "v"(k.nm), [A] "v"(k.A), [B] "v"(k.B), [I] "v"(k.I), [ca] "v"(f32x2{c0[0], c0[1]}), [cb] "v"(f32x2{c0[2], c0[3]}), [cc] "v"(f32x2{c1[0], c1[1]}),
        [cd] "v"(f32x2{c1[2], c1[3]}), [r] "n"(228 + 4 * T), [nt] "n"(NT), [d] "n"(D));
}
template <int R> DI void st_frag(u32x4 srd, unsigned voff, unsigned soff) { asm volatile("buffer_store_dwordx4 v[%c3:%c3+3], %0, %1, %2 offen sc1\n\ts_nop 1" ::"v"(voff), "s"(srd), "s"(soff), "n"(R) : "memory"); }
template <int R> DI void st_single(u32x4 srd, unsigned vsg, unsigned soff) {
  asm volatile("v_mov_b32 v[%c3+2], v[%c3]\n\tv_mov_b32 v[%c3+3], v[%c3+1]\n\tv_mov_b32 v248, v[%c3]\n\tv_mov_b32 v249, v[%c3+1]\n\ts_nop 1\n\t"
               "v_permlane16_swap_b32 v248, v[%c3+2]\n\tv_permlane16_swap_b32 v249, v[%c3+3]\n\t"
               "buffer_store_dwordx4 v[%c3:%c3+3], %0, %1, %2 offen sc1\n\ts_nop 1" ::"v"(vsg), "s"(srd), "s"(soff), "n"(R) : "memory");
}
template <int R> DI void st_tile(u32x4 srd, unsigned lane16, unsigned vsg, unsigned sp, unsigned ssg) {
  asm volatile(EP_PACK(116, %c0) EP_PACK(118, %c0+4) EP_GUARD(116) EP_GUARD(118) EP_PACK(120, %c0+8) EP_PACK(122, %c0+12) ::"n"(R));
  st_frag<116>(srd, lane16, sp);
  asm volatile(EP_GUARD(120) EP_GUARD(122) EP_PACK(250, %c0+16) ::"n"(R));
  st_frag<120>(srd, lane16, sp + 1024);
  asm volatile(EP_GUARD(250));
  st_single<250>(srd, vsg, ssg);
}
template <int R> DI void st_stats(__amdgpu_buffer_rsrc_t rs, unsigned vst, unsigned off) {
  float s, ss;
  asm volatile("v_pk_add_f32 v[116:117], v[%c2:%c2+1], v[%c2+2:%c2+3]\n\tv_pk_mul_f32 v[118:119], v[%c2:%c2+1], v[%c2:%c2+1]\n\tv_pk_mul_f32 v[120:121], v[%c2+2:%c2+3], v[%c2+2:%c2+3]\n\t"
               ".set i_, 2\n\t.rept 8\n\tv_pk_add_f32 v[116:117], v[116:117], v[%c2+2*i_:%c2+2*i_+1]\n\t"
               "v_pk_fma_f32 v[118+2*(i_&1):119+2*(i_&1)], v[%c2+2*i_:%c2+2*i_+1], v[%c2+2*i_:%c2+2*i_+1], v[118+2*(i_&1):119+2*(i_&1)]\n\t.set i_, i_+1\n\t.endr\n\t"
               "v_pk_add_f32 v[118:119], v[118:119], v[120:121]\n\tv_add_f32 %0, v116, v117\n\tv_add_f32 %1, v118, v119"
               : "=v"(s), "=v"(ss) : "n"(R));
  s = sum_q(s); ss = sum_q(ss);
  st_sc1(u32x2{nosent(__float_as_uint(s)), nosent(__float_as_uint(ss))}, rs, vst, off);
}
DI void read_stats(__amdgpu_buffer_rsrc_t rs, unsigned voff, unsigned soff, float& mean, float& var) {
  u32x4 st = __builtin_bit_cast(u32x4, vget4<250>());
  if (__builtin_expect(__any(umax3(max(st[0], st[1]), st[2], st[3]) == SENT), 0)) {
#pragma unroll 1
    for (unsigned sp = 0; sp < SPIN_CAP; ++sp) {
      asm volatile("s_sleep 8" ::: "memory");
      st = ld_sc1(rs, voff, soff);
      if (!__any(umax3(max(st[0], st[1]), st[2], st[3]) == SENT)) break;
    }
  }
  const float s = sum_q(__uint_as_float(st[0]) + __uint_as_float(st[2])), ss = sum_q(__uint_as_float(st[1]) + __uint_as_float(st[3]));
  mean = s * (1.f / D_);
  var = fmaxf(ss * (1.f / D_) - mean * mean, 0.f);
}
template <unsigned long long MASK> DI void arrive(u32x4 srd, unsigned voff) {
  unsigned long long sv;
  asm volatile("s_mov_b64 %0, exec\n\ts_mov_b64 exec, %3\n\tv_mov_b32 v249, 1\n\tbuffer_atomic_add v249, %1, %2, 0 offen sc1\n\ts_mov_b64 exec, %0"
               : "=&s"(sv) : "v"(voff), "s"(srd), "n"(MASK) : "memory");
}
template <unsigned long long MASK> DI void await(u32x4 srd, unsigned voff, unsigned expect) {
  unsigned long long sv; unsigned n;
#define POLL(R) "s_waitcnt vmcnt(2)\n\tv_cmp_ne_u32 vcc, %4, " #R "\n\ts_cbranch_vccz 2f\n\tv_mov_b32 " #R ", 0\n\tbuffer_atomic_add " #R ", %2, %3, 0 offen sc0 sc1\n\t"
  asm volatile("s_mov_b64 %0, exec\n\ts_mov_b64 exec, %5\n\ts_mov_b32 %1, 0\n\tv_mov_b32 v126, 0\n\tv_mov_b32 v127, 0\n\tv_mov_b32 v248, 0\n\t"
               "buffer_atomic_add v126, %2, %3, 0 offen sc0 sc1\n\ts_sleep %c6\n\tbuffer_atomic_add v127, %2, %3, 0 offen sc0 sc1\n\ts_sleep %c6\n\tbuffer_atomic_add v248, %2, %3, 0 offen sc0 sc1\n\t"
               "1:\n\t" POLL(v126) POLL(v127) POLL(v248)
               "s_add_u32 %1, %1, 1\n\ts_cmp_lt_u32 %1, 0x8000\n\ts_cbranch_scc1 1b\n\t"
               "2:\n\ts_mov_b64 exec, %0"
               : "=&s"(sv), "=&s"(n) : "v"(voff), "s"(srd), "v"(expect), "n"(MASK), "n"(PSL) : "memory", "scc", "vcc");
#undef POLL
}

template <int R> DI void warm_code(int part, int lane) {
  unsigned long long pc; unsigned len;
  asm volatile("s_getpc_b64 %0\n\t1:\n\ts_mov_b32 %1, .Lk_code_end-1b" : "=s"(pc), "=s"(len));
  asm volatile("global_load_dword v[%c2], %0, %1" ::"v"(min((unsigned)(part * 64 + lane) * 128u, len - 4u)), "s"(pc), "n"(R) : "memory");
}
template <int R> DI void ld_cs(u32x4 srd, unsigned voff, unsigned step) {
  asm volatile(".set i_, 0\n\t.rept 20\n\tbuffer_load_dwordx4 v[%c3+4*i_:%c3+4*i_+3], %0, %1, 0 offen sc1\n\tv_add_u32 %0, %0, %2\n\t.set i_, i_+1\n\t.endr" : "+v"(voff) : "s"(srd), "v"(step), "n"(R) : "memory");
}
template <int R> DI void all_flags(__amdgpu_buffer_rsrc_t rs, unsigned off, unsigned epoch, unsigned lane16) {
  u32x4 f = __builtin_bit_cast(u32x4, vget4<R>());
#pragma unroll 1
  for (unsigned sp = 0; sp < SPIN_CAP && !__all(f[0] == epoch && f[1] == epoch && f[2] == epoch && f[3] == epoch); ++sp) {
    asm volatile("s_sleep 8" ::: "memory");
    f = ld_sc1(rs, lane16, off);
  }
}
DI void peek(u32x4 srd, unsigned voff) {
  unsigned long long sv;
  asm volatile("s_mov_b64 %0, exec\n\ts_mov_b64 exec, 1\n\tv_mov_b32 v124, 0\n\tbuffer_atomic_add v124, %1, %2, 0 offen sc0 sc1\n\ts_mov_b64 exec, %0" : "=&s"(sv) : "v"(voff), "s"(srd) : "memory");
}
template <int VM> DI void peek_await(u32x4 srd, unsigned voff, unsigned expect) {
  unsigned c;
  asm volatile("s_waitcnt vmcnt(%c1)\n\tv_readfirstlane_b32 %0, v124" : "=s"(c) : "n"(VM) : "memory");
  if (__builtin_expect(c != expect, 0)) await<1ull>(srd, voff, expect);
}
template <int R> DI u32x4 tr_col(unsigned sel) {
  unsigned o0, o1, o2, o3;
  asm volatile("v_perm_b32 %0, v[%c5+4], v[%c5], %4\n\tv_perm_b32 %1, v[%c5+12], v[%c5+8], %4\n\tv_perm_b32 %2, v[%c5+20], v[%c5+16], %4\n\tv_perm_b32 %3, v[%c5+28], v[%c5+24], %4\n\ts_nop 1"
               : "=v"(o0), "=v"(o1), "=v"(o2), "=v"(o3) : "s"(sel), "n"(R));
  return u32x4{o0, o1, o2, o3};
}

#define AE(i) "v_exp_f32 v[%c[sc]+" #i "], v[%c[sc]+" #i "]\n\t"
#define AC(d, i) "v_cvt_pk_bf16_f32 v" #d ", v[%c[sc]+" #i "], v[%c[sc]+" #i "+1]\n\t"
template <int SC, int SN, int OV> DI void att_a(unsigned vA) {
  asm volatile(
      "s_waitcnt lgkmcnt(1)\n\t"
      "v_mfma_f32_32x32x16_bf16 v[144:159], v[242:245], v[228:231], v[144:159]\n\t"
      AE(0) AE(1) "ds_read_b128 v[242:245], %[va] offset:%c[ov]+704\n\t" AE(2) AE(3) AC(224, 0)
      "v_mfma_f32_32x32x4_2b_bf16 v[%c[sn]:%c[sn]+31], v[236:237], v[254:255], 0\n\t"
      AC(225, 2) AE(4) AE(5) AE(6) AE(7) AC(226, 4) AC(227, 6) AE(8) AE(9) AE(10) AE(11) AC(228, 8)
      "s_waitcnt lgkmcnt(1)\n\t"
      "v_mfma_f32_32x32x16_bf16 v[128:143], v[238:241], v[224:227], v[128:143]\n\t"
      AC(229, 10) AE(12) AE(13) AE(14) AE(15) AC(230, 12) AC(231, 14)
      ::[va] "v"(vA), [sc] "n"(SC), [sn] "n"(SN), [ov] "n"(OV) : "memory");
}
template <int SC, int SN, int OK> DI void att_b(unsigned kA, unsigned kB, unsigned vA) {
  asm volatile(
      AE(16) "s_waitcnt lgkmcnt(0)\n\t"
      "v_mfma_f32_32x32x16_bf16 v[128:143], v[242:245], v[228:231], v[128:143]\n\t"
      AE(17) AE(18) AE(19) AC(224, 16)
      "v_mfma_f32_32x32x16_bf16 v[%c[sn]:%c[sn]+15], v[232:235], v[246:249], v[%c[sn]:%c[sn]+15]\n\t"
      AC(225, 18) AE(20) AE(21) AE(22) AE(23) AC(226, 20) AC(227, 22)
      "v_mfma_f32_32x32x16_bf16 v[%c[sn]+16:%c[sn]+31], v[232:235], v[250:253], v[%c[sn]+16:%c[sn]+31]\n\t"
      AE(24) AE(25) "ds_read_b128 v[232:235], %[ka] offset:%c[ok]\n\tds_read_b64 v[236:237], %[kb] offset:%c[ok]\n\t" AE(26) AE(27) AC(228, 24)
      "v_mfma_f32_32x32x16_bf16 v[144:159], v[238:241], v[224:227], v[144:159]\n\t"
      AC(229, 26) AE(28) AE(29) AE(30) AE(31) AC(230, 28) AC(231, 30)
      "ds_read_b128 v[238:241], %[va] offset:%c[ok]"
      ::[ka] "v"(kA), [kb] "v"(kB), [va] "v"(vA), [sc] "n"(SC), [sn] "n"(SN), [ok] "n"(OK) : "memory");
}
#undef AE
#undef AC
template <int SEND, int KEEP> DI unsigned long long att_merge(unsigned mine, unsigned theirs) {
  unsigned long long m;
  asm volatile("ds_write_b128 %1, v[%c3:%c3+3]\n\tds_write_b128 %1, v[%c3+4:%c3+7] offset:1024\n\tds_write_b128 %1, v[%c3+8:%c3+11] offset:2048\n\t"
               "s_waitcnt vmcnt(0) lgkmcnt(0)\n\ts_barrier\n\t"
               "ds_read_b128 v[168:171], %2 offset:2048\n\tds_read_b128 v[160:163], %2\n\tds_read_b128 v[164:167], %2 offset:1024\n\ts_waitcnt lgkmcnt(2)\n\t"
               "v_pk_add_f32 v[168:169], v[168:169], v[%c4+8:%c4+9]\n\tv_pk_add_f32 v[170:171], v[170:171], v[%c4+10:%c4+11]\n\t"
               "v_mov_b32 v172, v168\n\tv_mov_b32 v173, v168\n\ts_waitcnt lgkmcnt(0)\n\t"
               ".set i_, 0\n\t.rept 4\n\tv_pk_add_f32 v[160+2*i_:161+2*i_], v[160+2*i_:161+2*i_], v[%c4+2*i_:%c4+2*i_+1]\n\t.set i_, i_+1\n\t.endr\n\t"
               "v_permlane32_swap_b32 v172, v173\n\t"
               "v_pk_mul_f32 v[174:175], v[168:169], 0\n\tv_pk_mul_f32 v[178:179], v[170:171], 0\n\tv_rcp_f32 v176, v173\n\t"
               "v_pk_fma_f32 v[174:175], v[160:161], 0, v[174:175]\n\tv_pk_fma_f32 v[178:179], v[162:163], 0, v[178:179]\n\t"
               "v_pk_fma_f32 v[174:175], v[164:165], 0, v[174:175]\n\tv_pk_fma_f32 v[178:179], v[166:167], 0, v[178:179]\n\t"
               "v_pk_add_f32 v[174:175], v[174:175], v[178:179]\n\tv_add_f32 v173, v173, v174\n\tv_add_f32 v173, v173, v175\n\t"
               "v_cmp_nle_f32 vcc, 0x21800000, v173\n\ts_mov_b64 %0, vcc\n\tv_cmp_nge_f32 vcc, 0x71800000, v173\n\ts_or_b64 %0, %0, vcc"
               : "=&s"(m) : "v"(mine), "v"(theirs), "n"(SEND), "n"(KEEP) : "memory", "vcc", "scc");
  return m;
}

__global__ void __launch_bounds__(512, 2) __attribute__((amdgpu_num_vgpr(48))) k_main(const char* __restrict__ x, const char* __restrict__ wqkv, const char* __restrict__ wo, const char* __restrict__ w1, const char* __restrict__ w2,
    const char* __restrict__ w3, const char* __restrict__ w4, char* __restrict__ ws, char* __restrict__ out) {
  extern __shared__ __attribute__((aligned(16))) char smem[];
  lds_ptr lds = (lds_ptr)smem;
  OWN_ALL_VGPRS();
  asm volatile(".subsection 2\n\t.rept 96\n\ts_nop 0\n\t.endr\n\t.Lk_code_end:\n\t.subsection 0");
  STAMP_DECL; STAMP(0); SPAN_BEGIN;
  const unsigned ldsb = (unsigned)(size_t)lds;
  const int tid = threadIdx.x, lane = tid & 63, w = __builtin_amdgcn_readfirstlane(tid >> 6), q = lane >> 4, tok = lane & 15;
  const int bid = blockIdx.x, xcd = bid & 7, r = bid >> 3;
  const int j = r & 7, head = 8 * (xcd & 3) + j, qb = 4 * (xcd >> 2) + (r >> 3), rb = 4 * qb + (xcd & 3);
  const __amdgpu_buffer_rsrc_t rsw = mk_rsrc(ws, WS_END);
  const unsigned wfs = OFF_WF + j * NGRP * GRP_B;
  const unsigned lane16 = lane * 16;

  const int rb1 = 2 * xcd + (r & 1), g1 = ((r >> 1) + 15) & 15;
  P1Load L1;
  const bool loader = w >= 4 && g1 < 15;
  if (__builtin_expect(loader, 1)) L1 = p1_start(x, wqkv, rb1, g1, ROTF, w - 4, lane);
  const u32x4 srd = mk_srd(ws, WS_END);
  unsigned epoch = 0;
  Setup su;
  auto fills = [&](int n, int first) AI {
    const u32x4 sent = {SENT, SENT, SENT, SENT};
#pragma unroll 1
    for (int i = 0; i < n; ++i) st_sc1(sent, rsw, lane16, first + i < 40 ? OFF_O + (bid * 40 + first + i) * 1024 : OFF_ST1 + bid * 1024);
  };
  if (loader) {
    asm volatile("s_waitcnt vmcnt(16)\n\ts_barrier" ::: "memory");
    p1_issue(L1, 3);
#pragma unroll 1
    for (int pp = 0; pp < 6; ++pp) {
      asm volatile("s_waitcnt vmcnt(16)\n\ts_barrier" ::: "memory");
      p1_issue(L1, pp + 4);
    }
    asm volatile("s_waitcnt vmcnt(16)\n\ts_barrier" ::: "memory");
    if (w == 4) {
      epoch = *(volatile __attribute__((address_space(3))) unsigned*)(lds + L_EPO);
      peek(srd, OFF_AXF + rb1 * 128);
      asm volatile("s_waitcnt vmcnt(9)\n\ts_barrier\n\ts_waitcnt vmcnt(0)" ::: "memory");
      const bool late = __builtin_amdgcn_readfirstlane(__float_as_uint(vget<124>())) != __builtin_amdgcn_readfirstlane(epoch);
      if (late) { asm volatile("s_barrier\n\ts_barrier" ::: "memory"); await<1ull>(srd, OFF_AXF + rb1 * 128, epoch); }
      const u32x4 xs = ld_sc1(rsw, lane16, OFF_XST + rb1 * 1024);
      if (!late) asm volatile("s_barrier\n\ts_barrier" ::: "memory");
      *(__attribute__((address_space(3))) u32x4*)(lds + 16384 + lane16) = xs;
      asm volatile("s_waitcnt lgkmcnt(0)\n\ts_barrier" ::: "memory");
    } else {
      if (FPOS == 0) fills(14, min(14 * (w - 5), 27));
      asm volatile("s_waitcnt vmcnt(%c0)\n\ts_barrier" ::"n"(FPOS == 0 ? 22 : 8) : "memory");
      if (FPOS == 1) fills(14, min(14 * (w - 5), 27));
      asm volatile("s_waitcnt vmcnt(%c0)\n\ts_barrier" ::"n"(FPOS <= 1 ? 14 : 0) : "memory");
      if (FPOS == 2) fills(14, min(14 * (w - 5), 27));
      asm volatile("s_barrier\n\ts_barrier" ::: "memory");
      if (FPOS == 3) fills(14, min(14 * (w - 5), 27));
    }
    su = make_setup(lane, w, bid, head);
  } else {
    if (w == 0) {
      epoch = __builtin_amdgcn_raw_buffer_load_b32(rsw, 0, OFF_EPO, AUX_SC1);
      st_sc1(u32x4{0u, 0u, 0u, 0u}, rsw, (lane >> 4) * 2048 + (lane & 15) * 16, OFF_CNT + (32 * rb + j) * 256);
      st_sc1(u32x4{0u, 0u, 0u, 0u}, rsw, lane < 16 ? lane16 : 0x80000000u, OFF_QC + (head * 8 + qb) * 256);
    }
    if (w == 1) warm_code<255>(r & 7, lane);
    auto ready = [&]() AI {
      epoch = __builtin_amdgcn_readfirstlane(epoch) + 1u;
      __builtin_amdgcn_raw_buffer_store_b32(epoch, rsw, lane == 0 ? bid * 4 : 0x80000000u, OFF_RDY, AUX_SC1);
      *(__attribute__((address_space(3))) unsigned*)(lds + L_EPO) = epoch;
    };
    if (g1 == 15) {
      p1_stats(x, rsw, rb1, w, lane, [&]() AI { if (w == 0) ready(); });
      if (w == 0) __builtin_amdgcn_raw_buffer_store_b32(epoch, rsw, lane == 0 ? 0 : 0x80000000u, OFF_AXF + rb1 * 128, AUX_SC1);
      if (w > 4) fills(14, min(14 * (w - 5), 27));
      su = make_setup(lane, w, bid, head);
    } else {
      su = make_setup(lane, w, bid, head);
    }
    auto started = [&]() AI { if (w == 0) { asm volatile("s_waitcnt vmcnt(0)" ::: "memory"); ready(); } };
    STAMP(47);
    if (tid < 80 && tid != 3) *(__attribute__((address_space(3))) unsigned*)(lds + L_FILL + tid * 4) = tid == 0 ? 4u : (tid >> 2) != 1 ? 0u : tid < 6 ? tid : (unsigned)NGRP;
    if (g1 < 10) p1_compute<true>(srd, rsw, lds, rb1, g1, w, lane, started STAMP_ARG);
    else if (g1 < 15) p1_compute<false>(srd, rsw, lds, rb1, g1, w, lane, started STAMP_ARG);
    else if (w == 0) {
      asm volatile("s_sleep 64" ::: "memory");
      asm volatile("buffer_load_dwordx4 v[250:253], %0, %1, %2 offen sc1" ::"v"(lane16), "s"(srd), "s"(OFF_RDY) : "memory");
    }
  }
  STAMP(43);
  const int hi = lane >> 5, r32 = lane & 31, wq = w & 3, wk = w >> 2;
  const bool has_job = w * 256 + bid < 850;
  unsigned kA = su.kA, kB = su.kB, vA = su.vA;
  const char* src = su.mat == 0 ? wo : su.mat == 1 ? w1 : su.mat == 2 ? w2 : su.mat == 3 ? w3 : w4;
  auto job_rows = [&]() AI {
    const int n2 = su.mat == 3 ? 2 * F_ : 2 * D_;
    gload4<96, 0>(src, su.jo0); gload4<100, 0>(src + n2, su.jo0); gload4<104, 0>(src + 2 * n2, su.jo0); gload4<108, 0>(src + 3 * n2, su.jo0);
    gload4<112, 0>(src, su.jo4); gload4<116, 0>(src + n2, su.jo4); gload4<120, 0>(src + 2 * n2, su.jo4); gload4<124, 0>(src + 3 * n2, su.jo4);
  };
  auto job_cols = [&](auto S_) AI {
    constexpr int sl = S_.value;
    const u32x4 o0 = tr_col<96 + sl>(0x05040100u), o1 = tr_col<96 + sl>(0x07060302u);
    st_sc1(o0, rsw, su.jvo[sl >> 1] + (sl & 1) * 128, su.jso);
    st_sc1(o1, rsw, su.jvo[sl >> 1] + (sl & 1) * 128 + 64, su.jso);
    if (su.mat == 1 || su.mat == 3) {
      const bf16x8 ones = {0x3f80, 0x3f80, 0x3f80, 0x3f80, 0x3f80, 0x3f80, 0x3f80, 0x3f80};
      const f32x4 z = {0.f, 0.f, 0.f, 0.f};
      const f32x4 c0 = MFMA16(__builtin_bit_cast(bf16x8, o0), ones, z), c1 = MFMA16(__builtin_bit_cast(bf16x8, o1), ones, z);
      st_sc1(__builtin_bit_cast(u32x4, (lane & 1) ? c1 : c0), rsw, su.jcs, su.jcso + sl * 128);
    }
  };
  {
    asm volatile("s_waitcnt vmcnt(0) lgkmcnt(0)\n\ts_barrier" ::: "memory");
    STAMP(44);
    asm volatile(".set i_, 0\n\t.rept 32\n\tv_mov_b32 v[128+i_], 0\n\t.set i_, i_+1\n\t.endr\n\t"
                 ".set i_, 0\n\t.rept 4\n\tv_mov_b32 v[228+i_], 0\n\tv_mov_b32 v[242+i_], 0\n\t.set i_, i_+1\n\t.endr");
    if (w == 0) {
      all_flags<250>(rsw, OFF_RDY, epoch, lane16);
      const int gt = g1 < 5 ? g1 : g1 < 10 ? g1 - 5 : g1 - 10, hh = (128 * gt) / 20 + (lane >> 3);
      if (g1 < 15 && hh <= (128 * gt + 127) / 20 && (g1 >= 5 || (lane & 7) == (rb1 >> 1)))
        __hip_atomic_fetch_add((unsigned*)(ws + OFF_QC + (hh * 8 + (lane & 7)) * 256 + (rb1 & 3) * 64), 1u, __ATOMIC_RELAXED, __HIP_MEMORY_SCOPE_AGENT);
      if (bid == 0) __builtin_amdgcn_raw_buffer_store_b32(epoch, rsw, lane == 0 ? 0 : 0x80000000u, OFF_EPO, AUX_SC1);
      const int span = 1 + ((20 * head + 19) >> 7) - ((20 * head) >> 7);
      await<0xfull>(srd, OFF_QC + (head * 8 + qb) * 256 + lane * 64, (8 + ((lane >> 1) == (qb & 1))) * span);
    }
    asm volatile("s_barrier" ::: "memory");
    STAMP(45);
  }

  {
    asm volatile("buffer_load_dwordx4 v[246:249], %0, %2, %3 offen sc1\n\tbuffer_load_dwordx4 v[250:253], %0, %2, %3 offen offset:1280 sc1\n\t"
                 "buffer_load_dwordx2 v[254:255], %1, %2, %3 offen offset:1024 sc1" ::"v"(su.qv0), "v"(su.qv1), "s"(srd), "s"(OFF_QF + (head * 64 + 8 * qb + 2 * wq) * QF_TILE) : "memory");
  }
  auto kv_piece = [&](int c, int i) AI { bdma_sc1(srd, su.kvs[i], OFF_KV + c * (4 * KV_BLK), min(w + 8 * i, 21) * 1024 + kv_at(c)); };
  kv_piece(0, 0); kv_piece(0, 1); kv_piece(0, 2);
  kv_piece(1, 0); kv_piece(1, 1); kv_piece(1, 2);
  asm volatile(
      "s_waitcnt vmcnt(3) lgkmcnt(0)\n\ts_barrier\n\t"
      "ds_read_b64 v[236:237], %1\n\tds_read_b128 v[232:235], %0\n\ts_waitcnt lgkmcnt(1)\n\t"
      "v_mfma_f32_32x32x4_2b_bf16 v[160:191], v[236:237], v[254:255], 0\n\t"
      "s_nop 15\n\ts_nop 1\n\ts_waitcnt lgkmcnt(0)\n\t"
      "v_mfma_f32_32x32x16_bf16 v[160:175], v[232:235], v[246:249], v[160:175]\n\t"
      "v_mfma_f32_32x32x16_bf16 v[176:191], v[232:235], v[250:253], v[176:191]\n\t"
      "ds_read_b128 v[232:235], %0 offset:%c3\n\tds_read_b64 v[236:237], %1 offset:%c3\n\tds_read_b128 v[238:241], %2 offset:%c3\n\t"
      "s_nop 7"
      ::"v"(kA), "v"(kB), "v"(vA), "n"(KV_REC) : "memory");
  STAMP(2);
  const unsigned rt = 4 * rb + (w & 3);
  const unsigned kp = 5 * (j >> 1) + 3 * (j & 1), ksg = 5 * (j >> 1) + 2;
  const unsigned rowoff = ((16 * rt + tok) * D_) * 2;
  const unsigned colp = (32 * kp + 8 * q) * 2, cols = (32 * ksg + 16 * (j & 1) + 4 * q) * 2;
  auto side = [&](auto C_, auto S_) AI {
    constexpr int c = C_.value, sl = S_.value, t = 7 * c + sl;
    if constexpr (c == 0) {
      if constexpr (sl < 6) { kv_piece(2 + 2 * sl / 3, 2 * sl % 3); kv_piece(2 + (2 * sl + 1) / 3, (2 * sl + 1) % 3); }
      else { const int i = min(w, 5); bdma_sc1(srd, su.kvw, OFF_KV, ldsb + L_WRAP + i * 1024); }
    } else if constexpr (c == 1) {
      if constexpr (sl >= 4) { kv_piece(6, sl - 4); kv_piece(7, sl - 4); }
      if constexpr (sl == 6) {
        if (has_job) job_rows();
        if (w == 7) warm_code<126>(r & 7, lane);
      }
    } else if constexpr (c == 2) {
    } else if constexpr (c == 3) {
      if constexpr (sl >= JS && sl < JS + 4) if (has_job) {
        if (sl == JS) asm volatile("s_waitcnt vmcnt(0)" ::: "memory");
        job_cols(IC<sl - JS>{});
      }
    } else if constexpr (c == 4) {
      if constexpr (sl == 4) if (tid == 0) __hip_atomic_store((unsigned*)(ws + OFF_FLAG) + bid, epoch, __ATOMIC_RELAXED, __HIP_MEMORY_SCOPE_AGENT);
      if constexpr (sl == 5) if (w < 4) {
        gload4<116, 0>(x, rowoff + colp); gload4<120, 64>(x, rowoff + colp);
        asm volatile("global_load_dwordx2 v[124:125], %0, %1" ::"v"(rowoff + cols), "s"(x) : "memory");
      }
    } else if constexpr (t < 39) {
      if constexpr (sl == 1) if (w == 0) asm volatile("buffer_load_dwordx4 v[96:99], %0, %1, %2 offen sc1" ::"v"(lane16), "s"(srd), "s"(OFF_FLAG) : "memory");
    } else if constexpr (t < 52) {
      const unsigned pc = (w + 8 * (t - 39)) * 1024;
      if (t < 51 || w < 4) bdma_sc1(srd, lane16, wfs + pc, ldsb + pc);
    }
  };
  const int qt = 8 * qb + 2 * wq + wk;
  const unsigned mine = ldsb + (w < 6 ? L_KV0 + w * 3072 : L_SCR2 + (w - 6) * 3072) + lane16;
  const unsigned theirs = ldsb + ((w ^ 4) < 6 ? L_KV0 + (w ^ 4) * 3072 : L_SCR2 + ((w ^ 4) - 6) * 3072) + lane16;
  const unsigned rto = OFF_O + (2 * qt + (r32 >> 4)) * FM_RT + (r32 & 15) * 16;
  const unsigned ov0 = rto + (5 * (head >> 1) + 3 * (head & 1) + hi) * 256, ov1 = hi ? 0x80000000u : rto + (5 * (head >> 1) + 2) * 256 + (head & 1) * 8;
  const unsigned ov2 = OFF_CNT + (16 * qt + lane) * 256 + (head >> 3) * 64;
  static_for<8>([&](auto C_) AI {
    constexpr int c = C_.value;
    if constexpr (c == 7) asm volatile("" ::"v"(mine), "v"(theirs), "v"(ov0), "v"(ov1), "v"(ov2));
    __builtin_amdgcn_s_setprio(2);
    att_a<160, 192, 1 * KV_REC>(vA); side(C_, IC<0>{}); att_b<160, 192, 2 * KV_REC>(kA, kB, vA); side(C_, IC<1>{});
    __builtin_amdgcn_s_setprio(1);
    att_a<192, 160, 2 * KV_REC>(vA); side(C_, IC<2>{}); att_b<192, 160, 3 * KV_REC>(kA, kB, vA); side(C_, IC<3>{});
    __builtin_amdgcn_s_setprio(0);
    att_a<160, 192, 3 * KV_REC>(vA);
    if constexpr (c == 5) if (w == 0) {
      asm volatile("s_waitcnt vmcnt(0)" ::: "memory");
      all_flags<96>(rsw, OFF_FLAG, epoch, lane16);
    }
    if constexpr (c == 0) asm volatile("s_waitcnt vmcnt(8) lgkmcnt(0)\n\ts_barrier" ::: "memory");
    else if constexpr (c == 1 || c == 4) asm volatile("s_waitcnt vmcnt(0) lgkmcnt(0)\n\ts_barrier" ::: "memory");
    else if constexpr (c == 5 || c == 6) asm volatile("s_waitcnt lgkmcnt(0)\n\ts_barrier" ::: "memory");
    __builtin_amdgcn_s_setprio(3);
    { const int d = c < 7 ? kv_at(c + 1) - kv_at(c) : L_WRAP - kv_at(7) + wk * (3072 - KV_HALF); kA += d; kB += d; vA += d; }
    att_b<160, 192, 0>(kA, kB, vA); side(C_, IC<4>{});
    att_a<192, 160, 0>(vA); side(C_, IC<5>{});
    att_b<192, 160, KV_REC>(kA, kB, vA); side(C_, IC<6>{});
  });
  {
    asm volatile("s_waitcnt lgkmcnt(0)\n\tv_mfma_f32_32x32x16_bf16 v[144:159], v[242:245], v[228:231], v[144:159]\n\ts_nop 11" ::: "memory");
    STAMP(3);
    if (__builtin_expect((wk == 0 ? att_merge<144, 128>(mine, theirs) : att_merge<128, 144>(mine, theirs)) != 0, 0)) {
      const f32x16 Os = attn_safe(rsw, OFF_QF + (head * 64 + qt) * QF_TILE, head, lane);
      static_for<12>([&](auto I_) AI { vset<160 + I_.value>(__float_as_uint(Os[I_.value])); });
      auto r2 = __builtin_amdgcn_permlane32_swap(__float_as_uint(Os[8]), __float_as_uint(Os[8]), false, false);
      vset<176>(__float_as_uint(__builtin_amdgcn_rcpf(__uint_as_float(r2[1]))));
    }
    asm volatile(".set i_, 0\n\t.rept 6\n\tv_pk_mul_f32 v[160+2*i_:161+2*i_], v[160+2*i_:161+2*i_], v[176:177] op_sel_hi:[1,0]\n\t.set i_, i_+1\n\t.endr\n\t"
                 EP_PACK(180, 160) EP_PACK(182, 164) EP_PACK(184, 168) EP_GUARD(180) EP_GUARD(182) EP_GUARD(184)
                 "buffer_store_dwordx4 v[180:183], %0, %2, 0 offen sc1\n\tbuffer_store_dwordx2 v[184:185], %1, %2, 0 offen sc1" ::"v"(ov0), "v"(ov1), "s"(srd) : "memory");
    arrive<0xffffull>(srd, ov2);
    asm volatile("s_barrier" ::: "memory");
  }
  if (w >= 4) {
    typedef volatile __attribute__((address_space(3))) unsigned* lword;
    __builtin_amdgcn_s_setprio(PRIO_H);
    auto min_cons = [&]() AI {
      const unsigned c = *(lword)(lds + L_CONS + (lane & 3) * 64);
      return (int)(min(min(__builtin_amdgcn_readlane(c, 0), __builtin_amdgcn_readlane(c, 1)), min(__builtin_amdgcn_readlane(c, 2), __builtin_amdgcn_readlane(c, 3))) >> 2);
    };
    auto wait_cons = [&](int need) AI {
      int have;
#pragma unroll 1
      for (unsigned sp = 0; (have = min_cons()) < need && sp < LDS_SPIN; ++sp) asm volatile("s_sleep 2" ::: "memory");
      return have;
    };
    auto publish = [&](unsigned g) AI {
      unsigned long long sv;
      asm volatile("s_mov_b64 %0, exec\n\ts_mov_b64 exec, 1\n\tds_write_b32 %1, %3\n\tds_read_b128 v[252:255], %2 offset:16\n\ts_waitcnt lgkmcnt(0)\n\t"
                   "v_min3_u32 v252, v252, v253, v254\n\tv_min_u32 v252, v252, v255\n\tds_max_u32 %2, v252\n\ts_mov_b64 exec, %0"
                   : "=&s"(sv) : "v"(ldsb + L_OWN + (w - 4) * 4), "v"(ldsb + L_FILL), "v"(g) : "memory");
    };
    if (w >= 6) {
      if (w == 7) wait_cons(1);
      {
        const bool is1 = w == 6;
        const int c0 = is1 ? 10 * j : 4 * (27 * (j >> 2) + 7 * (j & 3)), nc = is1 ? 10 : 28;
        const int i = lane & 7, cq = (c0 & ~3) + 4 * (lane >> 3);
        ld_cs<128>(srd, (is1 ? OFF_CS1 : OFF_CS3) + ((cq >> 4) * 128 + i * 16 + (cq & 15)) * 4, (is1 ? 640 : 1792) * 4);
        asm volatile("s_waitcnt vmcnt(0)\n\t.set i_, 1\n\t.rept 19\n\tv_pk_add_f32 v[128:129], v[128:129], v[128+4*i_:129+4*i_]\n\t"
                     "v_pk_add_f32 v[130:131], v[130:131], v[130+4*i_:131+4*i_]\n\t.set i_, i_+1\n\t.endr" ::: "memory");
        static_for<4>([&](auto E_) AI {
          const float sacc = vget<128 + E_.value>();
          const int c = cq + E_.value - c0;
          int slot;
          if (is1) {
            const int cp = (j & 1) ? c - 2 : c;
            slot = (cp >= 0 && cp < 8) ? (2 * (cp >> 2) + (i >> 2)) * 16 + 4 * (cp & 3) + (i & 3) : 64 + 8 * (c & 1) + i;
          } else slot = 80 + (2 * (c >> 2) + (i >> 2)) * 16 + 4 * (c & 3) + (i & 3);
          if (c >= 0 && c < nc) *(__attribute__((address_space(3))) float*)(lds + L_CS1 + slot * 4) = sacc;
        });
      }
      *(lword)(lds + L_CSF + (w - 6) * 4) = 1u;
      return;
    }
    auto issue = [&](int G) AI {
      const unsigned so = __builtin_amdgcn_readfirstlane(wfs + G * GRP_B), d = __builtin_amdgcn_readfirstlane(ldsb + (G % 6) * GRP_B);
      if (G >= 20 && (G < 24 || G == 34)) {
        const unsigned st = G == 34 ? 4096 : 5120;
#pragma unroll
        for (int i = 0; i < 5; ++i) { bdma4_sc1(srd, lane16, so + i * st, d + i * st); }
        asm volatile("s_waitcnt vmcnt(20)" ::: "memory");
      } else {
#pragma unroll
        for (int i = 0; i < 6; ++i) { bdma4_sc1(srd, lane16, so + i * 4096, d + i * 4096); }
        bdma_sc1(srd, lane16, so + 24576, d + 24576);
        asm volatile("s_waitcnt vmcnt(25)" ::: "memory");
      }
    };
    int have = 0;
#pragma unroll 1
    for (int G = w; G < NGRP; G += 2) {
      const int need = (G >= 8 && G < POSTP && (G & 2) == 0) ? (G & ~3) - 3 : G - 5;
      if (have < need && (have = min_cons()) < need) {
        asm volatile("s_waitcnt vmcnt(0)" ::: "memory");
        publish(G);
        have = wait_cons(need);
      }
      issue(G);
      publish(G);
    }
    asm volatile("s_waitcnt vmcnt(0)" ::: "memory");
    publish(NGRP);
    return;
  }

  __builtin_amdgcn_s_setprio(3);
  const unsigned wl = ldsb + (q * 16 + 4 * (tok & 3) + (tok >> 2)) * 16;
  const unsigned wb[6] = {wl, wl + 2 * GRP_B, wl + 4 * GRP_B, ldsb + L_FILL, ldsb + L_CONS + w * 64 + tok * 4, 1u};
  const unsigned vo0[4] = {lane16, lane16, lane16, lane16};
  w_prime<0>(wb);
  const unsigned cown = OFF_CNT + (rt * 8 + j) * 256, cgrp = OFF_CNT + (rt * 8 + lane) * 256;
  const unsigned vsg = (q & 1) ? 0x80000000u : (2 * (j & 1) + (q >> 1)) * 256 + tok * 16;
  const unsigned vst = lane < 16 ? (j >> 1) * 256 + lane16 + (j & 1) * 8 : 0x80000000u;
  const unsigned wfl = wfs + (wl - ldsb);
  typedef const __attribute__((address_space(3))) f32x4* lcs;
  asm volatile(".set i_, 0\n\t.rept 10\n\tv_lshlrev_b32 v[96+2*i_], 16, v[116+i_]\n\tv_and_b32 v[97+2*i_], 0xffff0000, v[116+i_]\n\t.set i_, i_+1\n\t.endr");
  STAMP(4);
  await<0xfull>(srd, cown + lane * 64, 8);
  STAMP(5);
  ld_lead(srd, vo0, OFF_O + rt * FM_RT);
  if (__builtin_expect(run_unit<0, 4, 0, 20, vmw(63), 8, 0, 0, 0, -1, 0>(wb, srd, 0, 0, 0, 0, srd, vo0, OFF_O + rt * FM_RT + 8192 STAMP_ARG) != 0, 0)) redo_unit<false>(rsw, vo0, OFF_O + rt * FM_RT, 20, rsw, wfl, srd);
  STAMP(6);
  asm volatile(".set i_, 0\n\t.rept 10\n\tv_pk_add_f32 v[96+2*i_:97+2*i_], v[96+2*i_:97+2*i_], v[228+2*i_:229+2*i_]\n\t.set i_, i_+1\n\t.endr");
  st_tile<96>(srd, lane16, vsg, OFF_H + rt * FM_RT + kp * 1024, OFF_H + rt * FM_RT + ksg * 1024);
  arrive<0xffull>(srd, cgrp + 4);
  st_stats<96>(rsw, vst, OFF_ST1 + rt * 1024);
  STAMP(7);
  w_prime<4>(wb);
  await<1ull>(srd, cown + 4, 8);
  STAMP(8);
  ld_lead(srd, vo0, OFF_H + rt * FM_RT);
#pragma unroll 1
  for (unsigned sp = 0; sp < LDS_SPIN && !__builtin_amdgcn_readfirstlane(*(volatile __attribute__((address_space(3))) unsigned*)(lds + L_CSF)); ++sp) asm volatile("s_sleep 1" ::: "memory");
  f32x4 cs[5];
#pragma unroll
  for (int t = 0; t < 5; ++t) cs[t] = *(lcs)(lds + L_CS1 + t * 64 + q * 16);
  if (__builtin_expect(run_unit<4, 4, 0, 20, vmw(63), 8, 0, 0, 0, 2, 1>(wb, srd, lane16, OFF_ST1 + rt * 1024, 0, 0, srd, vo0, OFF_H + rt * FM_RT + 8192 STAMP_ARG) != 0, 0)) redo_unit<false>(rsw, vo0, OFF_H + rt * FM_RT, 20, rsw, wfl + 4 * GRP_B, srd);
  STAMP(9);
  float mean1, r1;
  {
    float var;
    read_stats(rsw, lane16, OFF_ST1 + rt * 1024, mean1, var);
    r1 = __builtin_amdgcn_rsqf(var + LN_EPS);
    const float v2 = var * r1 * r1 + LN_EPS, r2 = r1 * __builtin_amdgcn_rsqf(v2);
    const GeluK gk = gelu_consts(mean1, r2, __builtin_amdgcn_sqrtf((var + LN_EPS) * v2));
    const unsigned o = OFF_H1 + rt * FM_RT;
    ep_gelu<0, 2, 228>(gk, cs[0], cs[1]); st_frag<228>(srd, lane16, o + kp * 1024);
    ep_gelu<2, 2, 236>(gk, cs[2], cs[3]); st_frag<236>(srd, lane16, o + kp * 1024 + 1024);
    ep_gelu<4, 1, 244>(gk, cs[4], cs[4]); st_single<244>(srd, vsg, o + ksg * 1024);
    const int rn = (j & 2) ? (int)rt - 1 : (int)rt + 1;
    if (rn >= 0 && rn < 128) arrive<0xffffull>(srd, (lane < 8 ? cgrp : OFF_CNT + (rn * 8 + lane - 8) * 256) + 68);
    else arrive<0xffull>(srd, cgrp + 68);
  }
  {
    const u32x4 srd1 = mk_srd(ws + OFF_H1, SZ_FM);
    unsigned vo[4];
#pragma unroll
    for (int g = 0; g < 4; ++g) {
      const int T = 16 * (int)rt + tok + (g == 0 ? -1 : g == 1 ? 1 : g == 2 ? -2 : 2);
      vo[g] = (T >= 0 && T < S_) ? (unsigned)((T >> 4) * FM_RT + q * 256 + (T & 15) * 16) : 0x80000000u;
    }
    const f32x2 rr = {r1, r1}, nmr = {-mean1 * r1, -mean1 * r1};
    STAMP(10);
    w_prime<8>(wb);
    await<1ull>(srd, cown + 68, (rt == 0 || rt == 127) ? 12 : 16);
    STAMP(1);
    ld_lead(srd1, vo, 0);
    if (__builtin_expect(run_unit<8, 4, 0, 20, vmw(63), 8, 0, 0, 0, -1, 0>(wb, srd, 0, 0, 0, 0, srd1, vo, 8192 STAMP_ARG) != 0, 0)) redo_unit<false>(mk_rsrc(ws + OFF_H1, SZ_FM), vo, 0, 20, rsw, wfl + 8 * GRP_B, srd1);
    STAMP(11);
    asm volatile(".set i_, 0\n\t.rept 10\n\tv_pk_fma_f32 v[228+2*i_:229+2*i_], v[96+2*i_:97+2*i_], %0, v[228+2*i_:229+2*i_]\n\t.set i_, i_+1\n\t.endr\n\t"
                 ".set i_, 0\n\t.rept 10\n\tv_pk_add_f32 v[228+2*i_:229+2*i_], v[228+2*i_:229+2*i_], %1\n\t.set i_, i_+1\n\t.endr" ::"v"(rr), "v"(nmr));
    st_tile<228>(srd, lane16, vsg, OFF_X1 + rt * FM_RT + kp * 1024, OFF_X1 + rt * FM_RT + ksg * 1024);
    arrive<0xffull>(srd, cgrp + 132);
    st_stats<228>(rsw, vst, OFF_ST2 + rt * 1024);
    asm volatile(".set i_, 0\n\t.rept 10\n\tv_pk_add_f32 v[96+2*i_:97+2*i_], v[96+2*i_:97+2*i_], v[228+2*i_:229+2*i_]\n\t.set i_, i_+1\n\t.endr");
  }
  STAMP(12);
#pragma unroll 1
  for (unsigned sp = 0; sp < LDS_SPIN && !__builtin_amdgcn_readfirstlane(*(volatile __attribute__((address_space(3))) unsigned*)(lds + L_CSF + 4)); ++sp) asm volatile("s_sleep 1" ::: "memory");
#pragma unroll
  for (int t = 0; t < 5; ++t) cs[t] = *(lcs)(lds + L_CS3 + t * 64 + q * 16);
  w_prime<12>(wb);
  await<1ull>(srd, cown + 132, 8);
  ld_lead(srd, vo0, OFF_X1 + rt * FM_RT);
  {
    constexpr int GD = GDIST;
    const unsigned gs = OFF_G + rt * G_RT;
    const unsigned gl = gs;
    if (__builtin_expect(run_unit<12, 4, 0, 20, vmw(63), 8, 0, 0, 0, 2, 1>(wb, srd, lane16, OFF_ST2 + rt * 1024, 0, 0, srd, vo0, OFF_X1 + rt * FM_RT + 8192 STAMP_ARG) != 0, 0)) redo_unit<true>(rsw, vo0, OFF_X1 + rt * FM_RT, 20, rsw, wfl + 12 * GRP_B, srd);
    STAMP(13);
    float mean, var;
    read_stats(rsw, lane16, OFF_ST2 + rt * 1024, mean, var);
    const GeluK gk = gelu_consts(mean, __builtin_amdgcn_rsqf(var + LN_EPS), __builtin_amdgcn_sqrtf(var + LN_EPS));
    ep_gelu<0, 2, 228>(gk, cs[0], cs[1]); st_frag<228>(srd, lane16, gs + 2 * j * 1024);
    ep_gelu<2, 2, 236>(gk, cs[2], cs[3]); st_frag<236>(srd, lane16, gs + (2 * j + 1) * 1024);
    ep_gelu<4, 1, 250>(gk, cs[4], cs[4]);
#pragma unroll
    for (int t = 0; t < 5; ++t) cs[t] = *(lcs)(lds + L_CS3 + (5 + t) * 64 + q * 16);
    STAMP(14);
    run_unit<16, 4, 0, 20, vmw(63), 0, 0, 0, 0, -1, 0, 0>(wb, srd, 0, 0, 0, cgrp + 196, srd, vo0, 0 STAMP_ARG);
    peek(srd, cown + 196);
    ep_gelu<0, 1, 252>(gk, cs[0], cs[0]); st_frag<250>(srd, lane16, gs + (16 + 3 * j) * 1024);
    ep_gelu<1, 2, 232>(gk, cs[1], cs[2]); st_frag<232>(srd, lane16, gs + (17 + 3 * j) * 1024);
    ep_gelu<3, 2, 240>(gk, cs[3], cs[4]); st_frag<240>(srd, lane16, gs + (18 + 3 * j) * 1024);
#pragma unroll
    for (int t = 0; t < 4; ++t) cs[t] = *(lcs)(lds + L_CS3 + (10 + t) * 64 + q * 16);
    peek_await<3>(srd, cown + 196, 8);
    run_unit<20, 4, 0, 20, vmw(63), 0, GD, 20 - GD, 0, -1, 0, 0>(wb, srd, lane16, gl, 0, cgrp + 8, srd, vo0, 0 STAMP_ARG);
    peek(srd, cown + 8);
    const unsigned g3 = gs + (40 + 2 * j - (j > 3)) * 1024;
    ep_gelu<0, 2, 228>(gk, cs[0], cs[1]); st_frag<228>(srd, lane16, g3);
    if ((j & 3) != 3) { ep_gelu<2, 2, 236>(gk, cs[2], cs[3]); st_frag<236>(srd, lane16, g3 + 1024); }
    STAMP(15);
    if ((j & 3) != 3) peek_await<2>(srd, cown + 8, 8); else peek_await<1>(srd, cown + 8, 8);
    STAMP(16);
    run_unit<24, 5, 0, 25, vmw(63, 63, GD - 4, GD - 5, GD - 4), 0, 25, 0, GD, 2, 3, 5, false>(wb, srd, lane16, gl + GD * 1024, cown + 72, cgrp + 72, srd, vo0, 0 STAMP_ARG);
    peek_await<10>(srd, cown + 72, 8);
    run_unit<29, 6, 25, 54, vmw(GD - 5, GD - 5, GD - 5, GD - 5, 4, 0), 0, 29 - GD, 0, (25 + GD) % 20, -1, 0>(wb, srd, lane16, gl + (25 + GD) * 1024, 0, 0, srd, vo0, 0 STAMP_ARG);
    STAMP(17);
    asm volatile(".set i_, 0\n\t.rept 10\n\tv_pk_add_f32 v[228+2*i_:229+2*i_], v[96+2*i_:97+2*i_], v[228+2*i_:229+2*i_]\n\t.set i_, i_+1\n\t.endr\n\t"
                 EP_PACK(116, 228) EP_PACK(118, 232) EP_PACK(120, 236) EP_PACK(122, 240) EP_PACK(124, 244)
                 "global_store_dwordx4 %0, v[116:119], %2 sc1\n\tglobal_store_dwordx4 %0, v[120:123], %2 offset:64 sc1\n\tglobal_store_dwordx2 %1, v[124:125], %2 sc1\n\ts_waitcnt lgkmcnt(0)"
                 ::"v"(rowoff + colp), "v"(rowoff + cols), "s"(out) : "memory");
  }
  STAMP(18); SPAN_END;
  STAMP_OUT(1);
}

extern "C" void kernel_launch(void* const* d_in, const int* in_sizes, int n_in, void* d_out, int out_size, void* d_ws, size_t ws_size, hipStream_t stream) {
  (void)in_sizes; (void)n_in; (void)out_size; (void)ws_size;
  (void)hipFuncSetAttribute((const void*)k_main, hipFuncAttributeMaxDynamicSharedMemorySize, K2_LDS);
  k_main<<<256, 512, K2_LDS, stream>>>((const char*)d_in[0], (const char*)d_in[1], (const char*)d_in[2], (const char*)d_in[4], (const char*)d_in[5], (const char*)d_in[6],
                                              (const char*)d_in[7], (char*)d_ws, (char*)d_out);
}
```

```cpp
#include <hip/hip_runtime.h>
#include <hip/hip_bf16.h>

typedef __attribute__((ext_vector_type(8))) short bf16x8;
typedef __attribute__((ext_vector_type(4))) short s16x4;
typedef __attribute__((ext_vector_type(2))) __bf16 bf16x2;
typedef __attribute__((ext_vector_type(2))) float f32x2;
typedef __attribute__((ext_vector_type(4))) float f32x4;
typedef __attribute__((ext_vector_type(16))) float f32x16;
typedef __attribute__((ext_vector_type(4))) unsigned u32x4;
typedef __attribute__((ext_vector_type(2))) unsigned u32x2;
typedef __attribute__((address_space(3))) const char* lds_cptr;
typedef __attribute__((address_space(3))) char* lds_ptr;
#define DI __device__ __forceinline__
#define AI __attribute__((always_inline))
#define SBAR() __builtin_amdgcn_sched_barrier(0)
#define PIN(x) asm volatile("" : "+v"(x))
#define MFMA16(a, b, c) __builtin_amdgcn_mfma_f32_16x16x32_bf16((a), (b), (c), 0, 0, 0)
#define MFMA32(a, b, c) __builtin_amdgcn_mfma_f32_32x32x16_bf16((a), (b), (c), 0, 0, 0)

constexpr int S_ = 2048, D_ = 640, F_ = 1728;
constexpr float LN_EPS = 1e-5f;
constexpr float QSCALE = 0.22360679774997896f * 1.4426950408889634f;
constexpr unsigned SENT = 0xFFFFFFFFu;

constexpr unsigned QF_TILE = 1280, KV_REC = 2688, FM_RT = 20 * 1024, G_RT = 54 * 1024;
constexpr unsigned SZ_FM = 128 * FM_RT;
constexpr unsigned OFF_QF = 0;
constexpr unsigned OFF_KV = OFF_QF + 32 * 64 * QF_TILE;
constexpr unsigned KV_BLK = 40960 + 40960 + 128;
constexpr unsigned OFF_O = OFF_KV + 64 * KV_BLK;
constexpr unsigned OFF_H = OFF_O + SZ_FM;
constexpr unsigned OFF_H1 = OFF_H + SZ_FM;
constexpr unsigned OFF_X1 = OFF_H1 + SZ_FM;
constexpr unsigned OFF_G = OFF_X1 + SZ_FM;
constexpr unsigned OFF_ST1 = OFF_G + 128 * G_RT;
constexpr unsigned OFF_ST2 = OFF_ST1 + 128 * 1024;
constexpr int NGRP = 35;
constexpr unsigned GRP_B = 25 * 1024;
constexpr unsigned OFF_WF = OFF_ST2 + 128 * 1024;
constexpr unsigned OFF_CS1 = OFF_WF + 8 * NGRP * GRP_B;
constexpr unsigned OFF_CS3 = OFF_CS1 + 20 * 640 * 4;
constexpr unsigned OFF_FLAG = OFF_CS3 + 20 * 1792 * 4;
constexpr unsigned OFF_CNT = OFF_FLAG + 1024;
constexpr unsigned OFF_QC = OFF_CNT + 1024 * 256;
constexpr unsigned OFF_RDY = OFF_QC + 256 * 256;
constexpr unsigned OFF_AXF = OFF_RDY + 1024;
constexpr unsigned OFF_XST = OFF_AXF + 16 * 128;
constexpr unsigned OFF_EPO = OFF_XST + 16 * 1024;
constexpr unsigned WS_END = OFF_EPO + 256;
static_assert(WS_END > (28u << 20) && WS_END <= (32u << 20), "page walk below");

#define SPAN_BEGIN
#define SPAN_END
#define STAMP_DECL
#define STAMP_ARG
#define STAMP_PAR
#define STAMP(i)
#define STAMPF(i)
#define STAMP_OUT(k)
DI unsigned pack2(float lo, float hi) { f32x2 v = {lo, hi}; return __builtin_bit_cast(unsigned, __builtin_convertvector(v, bf16x2)); }
DI float bflo(unsigned u) { return __uint_as_float(u << 16); }
DI float bfhi(unsigned u) { return __uint_as_float(u & 0xffff0000u); }
DI float dot2(unsigned a, unsigned b, float c) { return __builtin_amdgcn_fdot2_f32_bf16(__builtin_bit_cast(bf16x2, a), __builtin_bit_cast(bf16x2, b), c, false); }
DI float sum_q(float x) {
  auto s = __builtin_amdgcn_permlane16_swap(__float_as_uint(x), __float_as_uint(x), false, false);
  x = __uint_as_float(s[0]) + __uint_as_float(s[1]);
  auto t = __builtin_amdgcn_permlane32_swap(__float_as_uint(x), __float_as_uint(x), false, false);
  return __uint_as_float(t[0]) + __uint_as_float(t[1]);
}
DI float sum8(float x) {
  x += __builtin_bit_cast(float, __builtin_amdgcn_update_dpp(0, __builtin_bit_cast(int, x), 0xB1, 0xf, 0xf, true));
  x += __builtin_bit_cast(float, __builtin_amdgcn_update_dpp(0, __builtin_bit_cast(int, x), 0x4E, 0xf, 0xf, true));
  x += __builtin_bit_cast(float, __builtin_amdgcn_update_dpp(0, __builtin_bit_cast(int, x), 0x141, 0xf, 0xf, true));
  return x;
}
template <int N> struct IC { static constexpr int value = N; };
template <int N, int I = 0, class Fn> DI void static_for(Fn&& f) { if constexpr (I < N) { f(IC<I>{}); static_for<N, I + 1>(f); } }
DI int swap23(int r) { return (r & ~12) | ((r & 4) << 1) | ((r & 8) >> 1); }
DI bf16x8 lds16(lds_cptr p) { return *(const __attribute__((address_space(3))) bf16x8*)p; }
typedef short v4i16_t __attribute__((ext_vector_type(4)));
DI s16x4 ldstr(lds_cptr p) { return __builtin_bit_cast(s16x4, __builtin_amdgcn_ds_read_tr16_b64_v4i16((__attribute__((address_space(3))) v4i16_t*)p)); }

#define OWN_ALL_VGPRS() asm volatile("" ::: "v255")
template <int R> DI float vget() { float r; asm volatile("v_mov_b32 %0, v[%c1]" : "=v"(r) : "n"(R)); return r; }
template <int R0, int R1> DI float vadd() { float r; asm volatile("v_add_f32 %0, v[%c1], v[%c2]" : "=v"(r) : "n"(R0), "n"(R1)); return r; }
template <int R> DI f32x4 vget4() { return f32x4{vget<R>(), vget<R + 1>(), vget<R + 2>(), vget<R + 3>()}; }
template <int R> DI void vset(unsigned v) { asm volatile("v_mov_b32 v[%c1], %0" ::"v"(v), "n"(R)); }
template <int R, int OFF> DI void gload4(const char* sbase, unsigned voff) {
  asm volatile("global_load_dwordx4 v[%c2:%c2+3], %0, %1 offset:%c3" ::"v"(voff), "s"(sbase), "n"(R), "n"(OFF) : "memory");
}
constexpr int AUX_SC1 = 16;
DI __amdgpu_buffer_rsrc_t mk_rsrc(const char* p, unsigned bytes) { return __builtin_amdgcn_make_buffer_rsrc((void*)p, 0, bytes, 0x00020000); }
DI void st_sc1(u32x4 v, __amdgpu_buffer_rsrc_t rs, unsigned voff, unsigned soff) { __builtin_amdgcn_raw_buffer_store_b128(v, rs, voff, soff, AUX_SC1); }
DI void st_sc1(u32x2 v, __amdgpu_buffer_rsrc_t rs, unsigned voff, unsigned soff) { __builtin_amdgcn_raw_buffer_store_b64(v, rs, voff, soff, AUX_SC1); }

DI u32x4 mk_srd(const char* p, unsigned bytes) { const unsigned long long v = (unsigned long long)p; return u32x4{(unsigned)v, (unsigned)(v >> 32) & 0xffffu, bytes, 0x00020000u}; }
DI void bdma_sc1(u32x4 srd, unsigned voff, unsigned soff, unsigned ldsdst) {
  asm volatile("s_mov_b32 m0, %3\n\ts_nop 0\n\tbuffer_load_dwordx4 %0, %1, %2 offen sc1 lds" ::"v"(voff), "s"(srd), "s"(soff), "s"(ldsdst) : "memory");
}
DI void bdma4_sc1(u32x4 srd, unsigned voff, unsigned soff, unsigned ldsdst) {
  asm volatile("s_mov_b32 m0, %3\n\ts_nop 0\n\tbuffer_load_dwordx4 %0, %1, %2 offen sc1 lds\n\tbuffer_load_dwordx4 %0, %1, %2 offen offset:1024 sc1 lds\n\t"
               "buffer_load_dwordx4 %0, %1, %2 offen offset:2048 sc1 lds\n\tbuffer_load_dwordx4 %0, %1, %2 offen offset:3072 sc1 lds" ::"v"(voff), "s"(srd), "s"(soff), "s"(ldsdst) : "memory");
}
DI void bdma(u32x4 srd, unsigned voff, unsigned soff, unsigned ldsdst) {
  asm volatile("s_mov_b32 m0, %3\n\ts_nop 0\n\tbuffer_load_dwordx4 %0, %1, %2 offen lds" ::"v"(voff), "s"(srd), "s"(soff), "s"(ldsdst) : "memory");
}

#define P1_RD(n, NXT, S2N, OFF)                                                                                                    \
  ".if " #n " < 4\n\tds_read_b64_tr_b16 v[" #NXT "+16+4*" #n ":" #NXT "+17+4*" #n "], v[246+" #n "] offset:" #OFF "+8192*" #S2N "\n\t"    \
  "ds_read_b64_tr_b16 v[" #NXT "+18+4*" #n ":" #NXT "+19+4*" #n "], v[246+" #n "] offset:" #OFF "+8192*" #S2N "+1024\n\t"                \
  ".elseif " #n " < 8\n\tds_read_b128 v[" #NXT "+4*(" #n "-4):" #NXT "+4*(" #n "-4)+3], v[244+" #S2N "] offset:" #OFF "+4096*((" #n "-4)/2)+512*(" #n "&1)\n\t.endif\n\t"
#define P1_STEP(SET, NXT, S2N, OFF, S2)                                                                                            \
  ".set n_, 0\n\t.rept 16\n\t.set m_, n_/4\n\t.set e_, n_&3\n\t"                                                                    \
  ".if %c[tf]\n\tv_mfma_f32_16x16x32_bf16 v[96+4*n_:99+4*n_], v[" #SET "+16+4*e_:" #SET "+19+4*e_], v[" #SET "+4*m_:" #SET "+3+4*m_], v[96+4*n_:99+4*n_]\n\t" \
  ".else\n\tv_mfma_f32_16x16x32_bf16 v[96+4*n_:99+4*n_], v[" #SET "+4*m_:" #SET "+3+4*m_], v[" #SET "+16+4*e_:" #SET "+19+4*e_], v[96+4*n_:99+4*n_]\n\t.endif\n\t" \
  P1_RD(n_, NXT, S2N, OFF)                                                                                                         \
  ".set n_, n_+1\n\t.endr\n\t"                                                                                                     \
  "s_cmp_lg_u32 %[wm], " #S2 "\n\ts_cbranch_scc1 1f\n\t.set e_, 0\n\t.rept 4\n\t"                                                   \
  ".if %c[tf]\n\tv_mfma_f32_16x16x32_bf16 v[160+4*e_:163+4*e_], v[" #SET "+16+4*e_:" #SET "+19+4*e_], v[240:243], v[160+4*e_:163+4*e_]\n\t" \
  ".else\n\tv_mfma_f32_16x16x32_bf16 v[160+4*e_:163+4*e_], v[240:243], v[" #SET "+16+4*e_:" #SET "+19+4*e_], v[160+4*e_:163+4*e_]\n\t.endif\n\t" \
  ".set e_, e_+1\n\t.endr\n\t1:\n\t"
template <bool TF> DI void p1_periods(int wm, unsigned n2) {
  asm volatile(
      "2:\n\t"
      P1_STEP(176, 208, 1, 0, 0) "s_waitcnt lgkmcnt(0)\n\ts_barrier\n\t"
      P1_STEP(208, 176, 0, 32768, 1) "s_waitcnt lgkmcnt(0)\n\t"
      P1_STEP(176, 208, 1, 32768, 0) "s_waitcnt lgkmcnt(0)\n\ts_barrier\n\t"
      ".set i_, 244\n\t.rept 6\n\tv_xor_b32 v[i_], 0x10000, v[i_]\n\t.set i_, i_+1\n\t.endr\n\t"
      P1_STEP(208, 176, 0, 0, 1)
      "s_sub_u32 %[cnt], %[cnt], 1\n\ts_cmp_lg_u32 %[cnt], 0\n\ts_waitcnt lgkmcnt(0)\n\ts_cbranch_scc1 2b\n\t"
      "s_nop 7"
      : [cnt] "+s"(n2) : [wm] "s"(wm), [tf] "n"(TF ? 1 : 0) : "memory", "scc");
}
typedef __attribute__((address_space(3))) f32x4 lf32x4;
typedef __attribute__((address_space(3))) f32x2 lf32x2;

template <int P> DI void p1_store_t(u32x4 srd, unsigned a0, unsigned a1, unsigned rec, unsigned long long tails) {
  unsigned long long sv;
  asm volatile(
      "s_mov_b64 %0, exec\n\t.set m_, 0\n\t.rept 4\n\t.set r_, 96+16*m_+8*%c6\n\t.set i_, 0\n\t.rept 4\n\t"
      "v_pk_fma_f32 v[r_+2*i_:r_+2*i_+1], v[192+2*m_:193+2*m_], v[160+8*%c6+2*i_:161+8*%c6+2*i_], v[r_+2*i_:r_+2*i_+1] op_sel_hi:[0,1,1] neg_lo:[1,0,0] neg_hi:[1,0,0]\n\t.set i_, i_+1\n\t.endr\n\t"
      ".set i_, 0\n\t.rept 4\n\tv_pk_mul_f32 v[r_+2*i_:r_+2*i_+1], v[r_+2*i_:r_+2*i_+1], v[192+2*m_:193+2*m_] op_sel:[0,1]\n\t.set i_, i_+1\n\t.endr\n\t"
      ".set i_, 0\n\t.rept 4\n\tv_cvt_pk_bf16_f32 v[r_+i_], v[r_+2*i_], v[r_+2*i_+1]\n\t.set i_, i_+1\n\t.endr\n\t"
      ".if m_ & 1\n\t"
      "s_mov_b32 exec_lo, 0\n\tv_swap_b32 v[r_-16], v[r_-14]\n\tv_swap_b32 v[r_-15], v[r_-13]\n\tv_swap_b32 v[r_], v[r_+2]\n\tv_swap_b32 v[r_+1], v[r_+3]\n\t"
      "s_mov_b64 exec, %5\n\tv_swap_b32 v[r_-14], v[r_]\n\tv_swap_b32 v[r_-13], v[r_+1]\n\ts_mov_b64 exec, %0\n\t"
      ".if m_ == 1\n\tbuffer_store_dwordx4 v[r_-16:r_-13], %1, %3, 0 offen sc1\n\tbuffer_store_dwordx4 v[r_:r_+3], %2, %3, 0 offen sc1\n\t"
      ".else\n\tbuffer_store_dwordx4 v[r_-16:r_-13], %1, %3, %4 offen sc1\n\tbuffer_store_dwordx4 v[r_:r_+3], %2, %3, %4 offen sc1\n\t.endif\n\t"
      ".endif\n\t.set m_, m_+1\n\t.endr"
      : "=&s"(sv) : "v"(a0), "v"(a1), "s"(srd), "s"(rec), "s"(tails), "n"(P) : "memory");
}
template <int A> DI void p1_store_n(u32x4 srd, unsigned a0, unsigned a1, unsigned so) {
  asm volatile(
      ".set e_, 0\n\t.rept 4\n\t.set b_, 0\n\t.rept 2\n\t.set r_, 96+32*%c4+16*b_+4*e_\n\t.set s_, 192+16*%c4+8*b_\n\t"
      "v_pk_fma_f32 v[r_:r_+1], v[160+4*e_:161+4*e_], v[s_:s_+1], v[r_:r_+1] neg_lo:[1,0,0] neg_hi:[1,0,0]\n\t"
      "v_pk_fma_f32 v[r_+2:r_+3], v[160+4*e_:161+4*e_], v[s_+2:s_+3], v[r_+2:r_+3] neg_lo:[1,0,0] neg_hi:[1,0,0]\n\t.set b_, b_+1\n\t.endr\n\t"
      ".set b_, 0\n\t.rept 2\n\t.set r_, 96+32*%c4+16*b_+4*e_\n\t.set s_, 192+16*%c4+8*b_\n\t"
      "v_pk_mul_f32 v[r_:r_+1], v[r_:r_+1], v[s_+4:s_+5]\n\tv_pk_mul_f32 v[r_+2:r_+3], v[r_+2:r_+3], v[s_+6:s_+7]\n\t.set b_, b_+1\n\t.endr\n\t"
      ".set r_, 96+32*%c4+4*e_\n\t"
      "v_cvt_pk_bf16_f32 v[r_], v[r_], v[r_+1]\n\tv_cvt_pk_bf16_f32 v[r_+1], v[r_+2], v[r_+3]\n\tv_cvt_pk_bf16_f32 v[r_+2], v[r_+16], v[r_+17]\n\tv_cvt_pk_bf16_f32 v[r_+3], v[r_+18], v[r_+19]\n\t"
      ".if e_ & 1\n\tbuffer_store_dwordx4 v[r_:r_+3], %1, %2, %3 offen offset:512*(e_/2) sc1\n\t.else\n\tbuffer_store_dwordx4 v[r_:r_+3], %0, %2, %3 offen offset:512*(e_/2) sc1\n\t.endif\n\t"
      ".set e_, e_+1\n\t.endr"
      ::"v"(a0), "v"(a1), "s"(srd), "s"(so), "n"(A) : "memory");
}
template <bool TF, class Fn>
DI void p1_compute(u32x4 srd, __amdgpu_buffer_rsrc_t rsw, lds_ptr lds, int rb, int g, int w, int lane, Fn started STAMP_PAR) {
  const int q = lane >> 4, i16 = lane & 15, wm = w >> 1, wn = w & 1;
  {
    const unsigned xa = 16384 + (64 * wm + 8 * (i16 >> 2) + (i16 & 3)) * 128 + ((q ^ (i16 >> 1)) * 16);
    const int p4 = lane & 3, qq = (lane >> 2) & 3;
    const unsigned wa = (8 * q + qq) * 256 + (((8 * wn + p4) ^ (2 * (qq + 4 * (q & 1)))) * 16) + 8 * (p4 >> 1);
    vset<244>(xa); vset<245>(xa ^ 64); vset<246>(wa); vset<247>(wa ^ 8); vset<248>(wa ^ 64); vset<249>(wa ^ 72);
  }
  const int kt0 = 4 * rb + 2 * wm;
  const unsigned rec = g < 5 ? QF_TILE : KV_BLK;
  unsigned ad[4], sta = 16384 + wm * 512 + (TF ? i16 * 4 : q * 16);
  unsigned long long tl[2];
  if (TF) {
    const bool isq = g < 5;
    const unsigned hst = isq ? 64 * QF_TILE : 1280;
    const int r0 = 8 * (i16 >> 2) + (i16 & 3);
#pragma unroll
    for (int P = 0; P < 2; ++P) {
      const int cq = 16 * (isq ? g : g - 5) + 8 * wn + 4 * P + q, i5 = (cq * 205) >> 10, m5 = cq - 5 * i5;
      const unsigned base = (isq ? OFF_QF : OFF_KV) + (2 * i5 + (m5 > 2)) * hst + kt0 * rec;
      const unsigned oct = base + (m5 > 2 ? m5 - 3 : m5) * 512, tail = base + 1024 + (8 * (i16 >> 2) + 2 * (i16 & 3)) * 8;
      ad[2 * P] = m5 == 2 ? tail : oct + (isq ? r0 : swap23(r0)) * 16;
      ad[2 * P + 1] = m5 == 2 ? tail + hst : oct + (isq ? r0 + 4 : swap23(r0 + 4)) * 16;
      tl[P] = __ballot(m5 == 2);
    }
  } else {
    const int p4 = i16 >> 2, cl = 64 * wn + 8 * p4 + (i16 & 3);
    ad[0] = q * 10240 + (cl + 4 * (p4 >> 1)) * 16; ad[1] = q * 10240 + (cl + 4 - 4 * (p4 >> 1)) * 16; ad[2] = ad[3] = 0; tl[0] = tl[1] = 0;
  }
  asm volatile("" : "+v"(ad[0]), "+v"(ad[1]), "+v"(ad[2]), "+v"(ad[3]), "+v"(sta));
  asm volatile(".set i_, 96\n\t.rept 80\n\tv_mov_b32 v[i_], 0\n\t.set i_, i_+1\n\t.endr\n\t"
               "v_mov_b32 v240, 0x3f803f80\n\tv_mov_b32 v241, v240\n\tv_mov_b32 v242, v240\n\tv_mov_b32 v243, v240\n\t"
               "s_waitcnt lgkmcnt(0)\n\ts_barrier\n\t.set n_, 0\n\t.rept 8\n\t" P1_RD(n_, 176, 0, 0) ".set n_, n_+1\n\t.endr\n\ts_waitcnt lgkmcnt(0)" ::: "memory");
  STAMP(41);
  started();
  p1_periods<TF>(wm, 3);
  if (w == 0) asm volatile("buffer_load_dwordx4 v[250:253], %0, %1, %2 offen sc1" ::"v"(lane * 16), "s"(srd), "s"(OFF_RDY) : "memory");
  p1_periods<TF>(wm, 2);
  STAMP(42);
  asm volatile(".set i_, 0\n\t.rept 4\n\tds_write_b128 %0, v[160+4*i_:163+4*i_] offset:1024*i_\n\t.set i_, i_+1\n\t.endr\n\ts_waitcnt lgkmcnt(0)\n\ts_barrier\n\t"
               ".set i_, 0\n\t.rept 4\n\tds_read_b128 v[176+4*i_:179+4*i_], %1 offset:1024*i_\n\t.set i_, i_+1\n\t.endr\n\t"
               ".set i_, 0\n\t.rept 4\n\t.if %c3\n\tds_read2_b32 v[192+2*i_:193+2*i_], %2 offset0:32*i_ offset1:32*i_+16\n\t"
               ".else\n\tds_read_b128 v[192+8*i_:195+8*i_], %2 offset:128*i_\n\tds_read_b128 v[196+8*i_:199+8*i_], %2 offset:128*i_+64\n\t.endif\n\t.set i_, i_+1\n\t.endr\n\t"
               "s_waitcnt lgkmcnt(0)\n\t.set i_, 0\n\t.rept 8\n\tv_pk_add_f32 v[160+2*i_:161+2*i_], v[160+2*i_:161+2*i_], v[176+2*i_:177+2*i_]\n\t.set i_, i_+1\n\t.endr"
               ::"v"(w * 4096 + lane * 16), "v"((w ^ 2) * 4096 + lane * 16), "v"(sta), "n"(TF ? 1 : 0) : "memory");
  STAMP(46);
  if (TF) {
    if (g < 5) asm volatile(".set i_, 0\n\t.rept 4\n\tv_mul_f32 v[193+2*i_], %0, v[193+2*i_]\n\t.set i_, i_+1\n\t.endr" ::"s"(QSCALE));
    static_for<2>([&](auto P_) AI { p1_store_t<P_.value>(srd, ad[2 * P_.value], ad[2 * P_.value + 1], rec, tl[P_.value]); });
  } else {
    static_for<2>([&](auto A_) AI { p1_store_n<A_.value>(srd, ad[0], ad[1], OFF_KV + ((kt0 & 32) | ((kt0 + A_.value + 1) & 31)) * KV_BLK + 40960 + (g - 10) * 2048); });
  }
}

struct P1Load { u32x4 srd; unsigned vo[4], pstep, hstep, dst; int rot; };
DI void p1_issue(const P1Load& L, int p) {
  int pp = p + L.rot; pp -= pp >= 10 ? 10 : 0;
  const unsigned so = pp * L.pstep, d = (p & 3) * 32768 + L.dst;
#pragma unroll
  for (int i = 0; i < 8; ++i) bdma(L.srd, L.vo[i & 3], so + (i >> 2) * L.hstep, d + i * 1024);
}
DI P1Load p1_start(const char* x, const char* wqkv, int rb, int g, int rot, int lw, int lane) {
  const bool isx = lw >= 2;
  const int hf = lw & 1, sh = isx ? 3 : 4;
  P1Load L;
  const unsigned stride = isx ? 1280 : 3840, r0 = isx ? 128 * rb + 64 * hf : 32 * hf, col = isx ? 0 : 256 * g;
  const unsigned base = (r0 + (lane >> sh)) * stride + col, ch = (lane & ((1 << sh) - 1)) ^ (((lane >> 4) & (isx ? 1 : 3)) << (isx ? 0 : 1));
#pragma unroll
  for (int i = 0; i < 4; ++i) L.vo[i] = base + i * (isx ? 10240 : 15360) + 16 * (ch ^ (isx ? 2 * i : 8 * (i >> 1)));
  asm volatile("" : "+v"(L.vo[0]), "+v"(L.vo[1]), "+v"(L.vo[2]), "+v"(L.vo[3]));
  asm volatile("" : "+s"(x), "+s"(wqkv));
  L.srd = mk_srd(isx ? x : wqkv, isx ? S_ * D_ * 2 : D_ * 3 * D_ * 2);
  L.pstep = isx ? 128 : 64 * 3840; L.hstep = isx ? 32 * 1280 : 16 * 3840; L.dst = lw * 8192; L.rot = rot;
  p1_issue(L, 0); p1_issue(L, 1); p1_issue(L, 2);
  return L;
}
template <class Fn> DI void p1_stats(const char* x, __amdgpu_buffer_rsrc_t rsw, int rb, int w, int lane, Fn landed) {
  const char* p = x + (size_t)(128 * rb + 16 * w + (lane >> 3)) * 1280 + (lane & 7) * 16;
  u32x4 v[2][10];
#pragma unroll
  for (int h = 0; h < 2; ++h)
#pragma unroll
    for (int k = 0; k < 10; ++k) v[h][k] = *(const u32x4*)(p + h * 8 * 1280 + k * 128);
  asm volatile("s_waitcnt vmcnt(20)" ::: "memory");
  landed();
#pragma unroll
  for (int h = 0; h < 2; ++h) {
    f32x4 sm4 = {0.f, 0.f, 0.f, 0.f}, sq4 = sm4;
#pragma unroll
    for (int k = 0; k < 10; ++k)
#pragma unroll
      for (int d = 0; d < 4; ++d) { sm4[d] = dot2(v[h][k][d], 0x3f803f80u, sm4[d]); sq4[d] = dot2(v[h][k][d], v[h][k][d], sq4[d]); }
    float sm = (sm4[0] + sm4[1]) + (sm4[2] + sm4[3]), sq = (sq4[0] + sq4[1]) + (sq4[2] + sq4[3]);
    sm = sum8(sm); sq = sum8(sq);
    const float mean = sm * (1.f / D_);
    const float rstd = __builtin_amdgcn_rsqf(fmaxf(sq * (1.f / D_) - mean * mean, 0.f) + LN_EPS);
    const int t = 16 * w + 8 * h + (lane >> 3);
    __builtin_amdgcn_raw_buffer_store_b32(__float_as_uint((lane & 1) ? rstd : mean), rsw,
        (lane & 6) == 0 ? (2 * (t >> 5) + ((t >> 2) & 1)) * 128 + (lane & 1) * 64 + (4 * ((t >> 3) & 3) + (t & 3)) * 4 : 0x80000000u, OFF_XST + rb * 1024, AUX_SC1);
  }
  { const unsigned c = lane ? 0u : 0x3f803f80u; st_sc1(u32x4{c, c, c, c}, rsw, (lane < 2 && w < 4) ? 81920 + lane * 16 : 0x80000000u, OFF_KV + (4 * rb + (w & 3)) * KV_BLK); }
  asm volatile("s_waitcnt vmcnt(0) lgkmcnt(0)\n\ts_barrier" ::: "memory");
}

constexpr int KV_HALF = 11 * 1024, KV_SLOT = 2 * KV_HALF;
constexpr int L_KV0 = 4 * GRP_B, L_WRAP = 144 * 1024, L_CS1 = 6 * GRP_B, L_CS3 = L_CS1 + 320, L_FILL = L_CS3 + 960, L_CSF = L_FILL + 4, L_EPO = L_FILL + 12,
              L_OWN = L_FILL + 16, L_CONS = L_FILL + 64, L_SCR2 = L_CONS + 256, K2_LDS = L_SCR2 + 6144 + 512;
constexpr int kv_at(int c) { return (c < 2 || c > 5) ? L_KV0 + (c & 1) * KV_SLOT : (c - 2) * KV_SLOT; }
#define PRIO_H 0
#define PSL 3
#define GDIST 12
#define JS 0
#define ROT 10
#define ROTF (ROT == 10 ? (g1 + 5 * (r & 1) + xcd) % 10 : ROT == 5 ? 2 * ((g1 + 2 * (r & 1) + xcd) % 5) : ROT == 3 ? 3 * ((g1 + (r & 1) + xcd) % 3) : ROT == 2 ? 5 * ((g1 + r + xcd) & 1) : 0)
#define FPOS 1
#define POSTP 14
constexpr unsigned SPIN_CAP = 20000, LDS_SPIN = 0x20000;

DI u32x4 ld_sc1(__amdgpu_buffer_rsrc_t rs, unsigned voff, unsigned soff) { return __builtin_amdgcn_raw_buffer_load_b128(rs, voff, soff, AUX_SC1); }
DI unsigned nosent(unsigned u) { return min(u, SENT - 1u); }
DI unsigned umax3(unsigned a, unsigned b, unsigned c) { return max(max(a, b), c); }
DI int tail_slot(int tokn) { return (tokn & 24) | ((tokn & 3) << 1) | ((tokn >> 2) & 1); }
DI unsigned kv_src(int n, int head) {
  const int pl = ((n - 80) * 745) >> 14, row = n - 80 - 22 * pl;
  return n < 80 ? head * 1280 + n * 16 : row < 20 ? 40960 + pl * 10240 + (20 * head + (row < 16 ? swap23(row) : row)) * 16 : 81920 + (row - 20) * 16;
}
DI unsigned kv_cell(int n, int half, int head) { const int rec = (n * 1561) >> 18; return (32 * half + min(rec, 3)) * KV_BLK + kv_src(n - 168 * rec, head); }
struct Setup { unsigned kvs[3], kvw, kA, kB, vA, qv0, qv1, jo0, jo4, jvo[2], jcs, jso, jcso; int mat; };
DI Setup make_setup(int lane, int w, int bid, int head) {
  asm volatile("" : "+v"(lane));
  asm volatile("" : "+s"(w));
  Setup s;
  const int hi = lane >> 5, r32 = lane & 31, lq = lane >> 4, lt = lane & 15;
#pragma unroll
  for (int i = 0; i < 3; ++i) { const int pc = min(w + 8 * i, 21), h2 = pc >= 11; s.kvs[i] = kv_cell((pc - 11 * h2) * 64 + lane, h2, head); }
  { const int i = min(w, 5), h2 = i >= 3; s.kvw = kv_cell((i - 3 * h2) * 64 + lane, h2, head); }
  const unsigned kvb = L_KV0 + (w >> 2) * KV_HALF;
  s.kA = kvb + hi * 512 + r32 * 16; s.kB = kvb + 1024 + tail_slot(swap23(r32)) * 8; s.vA = kvb + 1280 + hi * 352 + min(r32, 21) * 16;
  s.qv0 = hi * 512 + r32 * 16; s.qv1 = hi * QF_TILE + tail_slot(r32) * 8;
  const int job = w * 256 + bid;
  int mat, ks, cb;
  if (job < 300) { mat = job / 100; const int e = job - 100 * mat; ks = e / 5; cb = e - 5 * ks; }
  else if (job < 580) { mat = 3; const int e = job - 300; ks = e / 14; cb = e - 14 * ks; }
  else { mat = 4; const int e = job - 580; ks = e / 5; cb = e - 5 * ks; }
  int kpos = ks;
  if (mat == 4) { const int half = ks >= 27, k27 = ks - 27 * half, j4 = min(k27 / 7, 3), jj = 4 * half + j4, pi = k27 - 7 * j4; kpos = pi < 2 ? 2 * jj + pi : pi < 5 ? 14 + 3 * jj + pi : 35 + 2 * jj - (jj > 3) + pi; }
  s.mat = mat;
  s.jso = OFF_WF + ((kpos / 5) * 25 + (kpos % 5) * 5) * 1024;
  s.jcso = mat == 1 ? OFF_CS1 + (ks * 5 + cb) * 512 : OFF_CS3 + (ks * 14 + cb) * 512;
  const int jn = mat == 3 ? F_ : D_, n0 = 128 * cb + 8 * lt, kk = 4 * cb + (lt >> 2);
  int k0 = 32 * ks + 8 * lq, k4 = k0 + 4;
  if (mat == 0) { const int o = 4 * ks + lq, i = (o * 205) >> 10, oo = o - 5 * i; k0 = 40 * i + (oo < 3 ? 8 * oo : 8 * oo - 4); k4 = k0 + (oo == 2 ? 20 : 4); }
  s.jo0 = (k0 * jn + (n0 < jn ? n0 : 0)) * 2; s.jo4 = (k4 * jn + (n0 < jn ? n0 : 0)) * 2;
#pragma unroll
  for (int h = 0; h < 2; ++h) {
    int jj, t, pos, G;
    if (mat == 3) {
      const int half = kk >= 27, k27 = kk - 27 * half, j4 = min((k27 * 37) >> 8, 3);
      jj = 4 * half + j4; t = 2 * (k27 - 7 * j4) + h; pos = lt & 3;
      const int pass = (t * 13) >> 6; t -= 5 * pass; G = 12 + 4 * pass;
    } else {
      const int per5 = (kk * 13) >> 6, m5 = kk - 5 * per5;
      const bool single = m5 == 2;
      jj = 2 * per5 + (single ? ((lt >> 1) & 1) : (m5 > 2)); t = single ? 4 : 2 * (m5 > 2 ? m5 - 3 : m5) + h;
      pos = single ? 2 * (lt & 1) + h : lt & 3;
      G = mat == 4 ? 24 : 4 * mat;
    }
    s.jvo[h] = (mat != 3 || kk < 54) ? (unsigned)((jj * NGRP + G) * 25 + t) * 1024 + (lq * 16 + pos) * 16 : 0x80000000u;
  }
  s.jcs = lt < 2 ? lt * 64 + lq * 16 : 0x80000000u;
  asm volatile("" : "+v"(s.kvs[0]), "+v"(s.kvs[1]), "+v"(s.kvs[2]), "+v"(s.kvw), "+v"(s.kA), "+v"(s.kB), "+v"(s.vA), "+v"(s.qv0), "+v"(s.qv1), "+v"(s.jo0), "+v"(s.jo4),
               "+v"(s.jvo[0]), "+v"(s.jvo[1]), "+v"(s.jcs));
  asm volatile("" : "+s"(s.jso), "+s"(s.jcso), "+s"(s.mat));
  return s;
}
DI f32x16 attn_safe(__amdgpu_buffer_rsrc_t rs, unsigned qp, int head, int lane) {
  const int hi = lane >> 5, r32 = lane & 31;
  const bf16x8 q1 = __builtin_bit_cast(bf16x8, ld_sc1(rs, hi * 512 + r32 * 16, qp));
  const u32x2 q2l = __builtin_amdgcn_raw_buffer_load_b64(rs, 1024 + tail_slot(r32) * 8, qp, AUX_SC1);
  const bf16x8 q2 = __builtin_bit_cast(bf16x8, u32x4{hi ? 0u : q2l[0], hi ? 0u : q2l[1], 0u, 0u});
  f32x16 O = {};
  float m = -3.0e38f;
#pragma unroll 1
  for (int t = 0; t < 64; ++t) {
    const unsigned kb = OFF_KV + t * KV_BLK + head * 1280, vb = OFF_KV + ((t & 32) | ((t + 1) & 31)) * KV_BLK;
    const bf16x8 k1 = __builtin_bit_cast(bf16x8, ld_sc1(rs, hi * 512 + r32 * 16, kb));
    const u32x2 k2l = __builtin_amdgcn_raw_buffer_load_b64(rs, 1024 + tail_slot(swap23(r32)) * 8, kb, AUX_SC1);
    const bf16x8 v0 = __builtin_bit_cast(bf16x8, ld_sc1(rs, kv_src(80 + 22 * hi + min(r32, 21), head), vb)),
                 v1 = __builtin_bit_cast(bf16x8, ld_sc1(rs, kv_src(124 + 22 * hi + min(r32, 21), head), vb));
    f32x16 sc = MFMA32(k1, q1, f32x16{});
    sc = MFMA32(__builtin_bit_cast(bf16x8, u32x4{k2l[0], k2l[1], 0u, 0u}), q2, sc);
    float tm = sc[0];
#pragma unroll
    for (int i = 1; i < 16; ++i) tm = fmaxf(tm, sc[i]);
    { auto rr = __builtin_amdgcn_permlane32_swap(__float_as_uint(tm), __float_as_uint(tm), false, false); tm = fmaxf(__uint_as_float(rr[0]), __uint_as_float(rr[1])); }
    const float mn = fmaxf(m, tm), al = __builtin_amdgcn_exp2f(m - mn);
    m = mn;
    u32x4 p0, p1;
#pragma unroll
    for (int i = 0; i < 4; ++i) {
      p0[i] = pack2(__builtin_amdgcn_exp2f(sc[2 * i] - mn), __builtin_amdgcn_exp2f(sc[2 * i + 1] - mn));
      p1[i] = pack2(__builtin_amdgcn_exp2f(sc[8 + 2 * i] - mn), __builtin_amdgcn_exp2f(sc[9 + 2 * i] - mn));
    }
#pragma unroll
    for (int i = 0; i < 16; ++i) O[i] *= al;
    O = MFMA32(v0, __builtin_bit_cast(bf16x8, p0), O);
    O = MFMA32(v1, __builtin_bit_cast(bf16x8, p1), O);
  }
  return O;
}

constexpr int R_A = 128;
template <int A0, int N> DI void ld_frags(u32x4 srd, unsigned voff, unsigned soff) {
  unsigned t;
  asm volatile("s_add_u32 %0, %3, 4096\n\t.set i_, 0\n\t.rept %c5\n\t"
               ".if i_ < 4\n\tbuffer_load_dwordx4 v[%c4+4*i_:%c4+4*i_+3], %1, %2, %3 offen offset:1024*i_ sc1\n\t"
               ".else\n\tbuffer_load_dwordx4 v[%c4+4*i_:%c4+4*i_+3], %1, %2, %0 offen sc1\n\t.endif\n\t.set i_, i_+1\n\t.endr"
               : "=&s"(t) : "v"(voff), "s"(srd), "s"(soff), "n"(R_A + 4 * A0), "n"(N) : "memory", "scc");
}
DI void ld_unit(u32x4 srd, const unsigned (&vo)[4], unsigned soff) {
  ld_frags<0, 5>(srd, vo[0], soff); ld_frags<5, 5>(srd, vo[1], soff + 5120); ld_frags<10, 5>(srd, vo[2], soff + 10240); ld_frags<15, 5>(srd, vo[3], soff + 15360);
}
DI void ld_lead(u32x4 srd, const unsigned (&vo)[4], unsigned soff) { ld_frags<0, 5>(srd, vo[0], soff); ld_frags<5, 3>(srd, vo[1], soff + 5120); }
template <int G> DI void w_prime(const unsigned (&wb)[6]) {
  unsigned u;
  asm volatile("s_mov_b32 %0, 0\n\t1:\n\tds_read_b32 v254, %2\n\ts_add_u32 %0, %0, 1\n\ts_cmp_gt_u32 %0, 0x20000\n\ts_waitcnt lgkmcnt(0)\n\tv_cmp_gt_u32 vcc, %c3+1, v254\n\t"
               "s_cbranch_scc1 2f\n\ts_cbranch_vccnz 1b\n\t2:\n\t"
               ".set i_, 0\n\t.rept 5\n\tds_read_b128 v[208+4*i_:211+4*i_], %1 offset:%c4+1024*i_\n\t.set i_, i_+1\n\t.endr\n\ts_waitcnt lgkmcnt(0)"
               : "=&s"(u) : "v"(wb[(G % 6) >> 1]), "v"(wb[3]), "n"(G), "n"(((G % 6) & 1) * GRP_B) : "memory", "scc", "vcc");
}
#define GST_A
#define GST_B
#define GST_CL
constexpr unsigned long long vmw(int a, int b = 63, int c = 63, int d = 63, int e = 63, int f = 63) {
  return (unsigned long long)a | (unsigned long long)b << 6 | (unsigned long long)c << 12 | (unsigned long long)d << 18 | (unsigned long long)e << 24 | (unsigned long long)f << 30;
}
template <int G0, int NG, int KS0, int KSE, unsigned long long VMW, int LEAD, int NRL, int RS, int RB, int XG, int XOP, int AVM = -1, bool TAIL = true>
DI unsigned long long run_unit(const unsigned (&wb)[6], u32x4 srd, unsigned voff, unsigned soff, unsigned xoff, unsigned aoff,
                               u32x4 asrd, const unsigned (&vq)[4], unsigned aso STAMP_PAR) {
  unsigned long long m; unsigned u, so;
#define DBG_INC
#define DBG_OP
  asm volatile(
      "s_mov_b32 %[so], %[soff]\n\t"
      ".set gi_, 0\n\t.rept %c[ng]\n\t"
      ".set g_, %c[g0]+gi_\n\t.set gn_, g_+1\n\t.if gn_ > 34\n\t.set gn_, 34\n\t.endif\n\t"
      ".set sc_, g_-6*(g_/6)\n\t.set sn_, gn_-6*(gn_/6)\n\t.set nk_, %c[kse]-%c[ks0]-5*gi_\n\t.if nk_ > 5\n\t.set nk_, 5\n\t.endif\n\t"
      "ds_read_b32 v254, %[fl]\n\t" GST_A
      ".set vw_, (%c[vmw]>>(6*gi_))&63\n\t.if vw_ < 63\n\ts_waitcnt vmcnt(vw_)\n\t.endif\n\t"
      GST_B ".set k_, 0\n\t.rept nk_\n\t"
      ".set kr_, 5*gi_+k_\n\t.set ks_, %c[ks0]+kr_\n\t.set a_, 128+4*(ks_-20*(ks_/20))\n\t"
      ".if %c[lead]\n\t.set vw_, 19-ks_\n\t.if vw_ > %c[lead]-1\n\t.set vw_, %c[lead]-1\n\t.endif\n\t.if %c[xop] == 1 && gi_ > %c[xg]\n\t.set vw_, vw_+1\n\t.endif\n\ts_waitcnt vmcnt(vw_)\n\t.endif\n\t"
      ".set last_, %c[lead] && ks_ == 19 && g_ < 12\n\t"
      ".if k_ == nk_-1 && !last_\n\t"
      "s_cbranch_vccnz 3f\n\t2:\n\t.subsection 1\n\t3:\n\ts_mov_b32 %[u], 0\n\t"
      "1:\n\t" DBG_INC "s_sleep 1\n\tds_read_b32 v254, %[fl]\n\ts_add_u32 %[u], %[u], 1\n\ts_cmp_gt_u32 %[u], 0x20000\n\ts_waitcnt lgkmcnt(0)\n\tv_cmp_gt_u32 vcc, gn_+1, v254\n\t"
      "s_cbranch_scc1 2b\n\ts_cbranch_vccnz 1b\n\ts_branch 2b\n\t.subsection 0\n\t"
      ".endif\n\t"
      ".set t_, 0\n\t.rept 5\n\t"
      ".set f_, 5*k_+t_\n\t"
      ".if last_ && t_ >= 2\n\t.if (t_ & 1) == 0\n\ts_waitcnt lgkmcnt((4-t_)/2)\n\t.endif\n\t"
      ".elseif f_ == 0 || f_ == 2\n\ts_waitcnt lgkmcnt(5)\n\t.elseif (f_ & 1) == 0\n\ts_waitcnt lgkmcnt(3)\n\t.endif\n\t"
      ".if ks_ == 0\n\tv_mfma_f32_16x16x32_bf16 v[228+4*t_:231+4*t_], v[208+4*t_:211+4*t_], v[a_:a_+3], 0\n\t"
      ".else\n\tv_mfma_f32_16x16x32_bf16 v[228+4*t_:231+4*t_], v[208+4*t_:211+4*t_], v[a_:a_+3], v[228+4*t_:231+4*t_]\n\t.endif\n\t"
      ".set s_, sc_\n\t.set o_, 5120*(k_+1)+1024*t_\n\t.if k_ == nk_-1\n\t.set s_, sn_\n\t.set o_, 1024*t_\n\t.endif\n\t"
      ".if last_\n\t.elseif s_ < 2\n\tds_read_b128 v[208+4*t_:211+4*t_], %[w0] offset:25600*s_+o_\n\t"
      ".elseif s_ < 4\n\tds_read_b128 v[208+4*t_:211+4*t_], %[w1] offset:25600*(s_-2)+o_\n\t"
      ".else\n\tds_read_b128 v[208+4*t_:211+4*t_], %[w2] offset:25600*(s_-4)+o_\n\t.endif\n\t"
      ".if k_ == nk_-2 && t_ == 3\n\tv_cmp_gt_u32 vcc, gn_+1, v254\n\t.endif\n\t"
      ".set t_, t_+1\n\t.endr\n\t"
      ".set fn_, ks_+%c[lead]\n\t.if %c[lead] && fn_ < 20\n\t"
      ".if fn_ < 10\n\tbuffer_load_dwordx4 v[128+4*fn_:131+4*fn_], %[q1], %[asrd], %[aso] offen offset:1024*(fn_&3) sc1\n\t"
      ".elseif fn_ < 15\n\tbuffer_load_dwordx4 v[128+4*fn_:131+4*fn_], %[q2], %[asrd], %[aso] offen offset:1024*(fn_&3) sc1\n\t"
      ".else\n\tbuffer_load_dwordx4 v[128+4*fn_:131+4*fn_], %[q3], %[asrd], %[aso] offen offset:1024*(fn_&3) sc1\n\t.endif\n\t"
      ".if (fn_&3) == 3\n\ts_addk_i32 %[aso], 4096\n\t.endif\n\t.endif\n\t"
      ".set rn_, kr_-%c[rs]\n\t.if rn_ >= 0 && rn_ < %c[nrl]\n\t.set rr_, %c[rb]+rn_\n\t.set rr_, 128+4*(rr_-20*(rr_/20))\n\t"
      "buffer_load_dwordx4 v[rr_:rr_+3], %[voff], %[srd], %[so] offen offset:1024*(rn_&3) sc1\n\t"
      ".if (rn_&3) == 3\n\ts_addk_i32 %[so], 4096\n\t.endif\n\t"
      ".endif\n\t"
      ".set k_, k_+1\n\t.endr\n\t"
      "ds_add_u32 %[cs], %[one]\n\t"
      ".if gi_ == 0 && %c[avm] >= 0\n\ts_waitcnt vmcnt(%c[avm])\n\ts_mov_b64 %[m], exec\n\ts_mov_b64 exec, 0xff\n\tv_mov_b32 v125, 1\n\tbuffer_atomic_add v125, %[ao], %[srd], 0 offen sc1\n\ts_mov_b64 exec, %[m]\n\t.endif\n\t"
      ".if gi_ == %c[xg] && %c[xop] == 1\n\tbuffer_load_dwordx4 v[250:253], %[voff], %[srd], %[so] offen sc1\n\t.endif\n\t"
      ".if gi_ == %c[xg] && %c[xop] >= 2\n\ts_mov_b64 %[m], exec\n\ts_mov_b64 exec, 1\n\tv_mov_b32 v124, 0\n\tbuffer_atomic_add v124, %[xo], %[srd], 0 offen sc0 sc1\n\ts_mov_b64 exec, %[m]\n\t.endif\n\t"
      ".set gi_, gi_+1\n\t.endr\n\t"
      ".if %c[xop] == 1 || %c[xop] == 2\n\ts_waitcnt vmcnt(0)\n\t.endif\n\t"
      ".if %c[tail]\n\ts_nop 4\n\tv_cmp_u_f32 %[m], v228, v228\n\t.endif"
      : [m] "=&s"(m), [u] "=&s"(u), [so] "=&s"(so), [aso] "+s"(aso) DBG_OP
      : [w0] "v"(wb[0]), [w1] "v"(wb[1]), [w2] "v"(wb[2]), [fl] "v"(wb[3]), [cs] "v"(wb[4]), [one] "v"(wb[5]), [voff] "v"(voff), [xo] "v"(xoff), [ao] "v"(aoff), [avm] "n"(AVM), [tail] "n"(TAIL ? 1 : 0), [srd] "s"(srd), [soff] "s"(soff),
        [g0] "n"(G0), [ng] "n"(NG), [ks0] "n"(KS0), [kse] "n"(KSE), [vmw] "n"(VMW), [lead] "n"(LEAD), [q1] "v"(vq[1]), [q2] "v"(vq[2]), [q3] "v"(vq[3]), [asrd] "s"(asrd), [nrl] "n"(NRL), [rs] "n"(RS), [rb] "n"(RB), [xg] "n"(XG), [xop] "n"(XOP)
      : "memory", "scc", "vcc" GST_CL);
  return m;
}
template <bool RELOAD> DI void redo_unit(__amdgpu_buffer_rsrc_t ra, const unsigned (&vo)[4], unsigned soff, int nks, __amdgpu_buffer_rsrc_t rw, unsigned woff, u32x4 srd_a) {
  f32x4 acc[5];
#pragma unroll
  for (int t = 0; t < 5; ++t) acc[t] = f32x4{0.f, 0.f, 0.f, 0.f};
  asm volatile("s_waitcnt vmcnt(0)" ::: "memory");
#pragma unroll 1
  for (int ks = 0; ks < nks; ++ks) {
    const unsigned va = ks < 5 ? vo[0] : ks < 10 ? vo[1] : ks < 15 ? vo[2] : vo[3];
    u32x4 av;
#pragma unroll 1
    for (unsigned sp = 0; sp < SPIN_CAP; ++sp) {
      av = ld_sc1(ra, va, soff + ks * 1024);
      if (!__any(umax3(max(av[0], av[1]), av[2], av[3]) == SENT)) break;
      asm volatile("s_sleep 8" ::: "memory");
    }
#pragma unroll
    for (int t = 0; t < 5; ++t) acc[t] = MFMA16(__builtin_bit_cast(bf16x8, ld_sc1(rw, woff, (ks * 5 + t) * 1024)), __builtin_bit_cast(bf16x8, av), acc[t]);
  }
  static_for<20>([&](auto I_) AI { vset<228 + I_.value>(__float_as_uint(acc[I_.value >> 2][I_.value & 3])); });
  if (RELOAD) { ld_unit(srd_a, vo, soff); asm volatile("s_waitcnt vmcnt(0)" ::: "memory"); }
}

#define EP_PACK(d, r) "v_cvt_pk_bf16_f32 v[" #d "], v[" #r "], v[" #r "+1]\n\tv_cvt_pk_bf16_f32 v[" #d "+1], v[" #r "+2], v[" #r "+3]\n\t"
#define EP_GUARD(d) "v_min_u32 v[" #d "], -2, v[" #d "]\n\tv_min_u32 v[" #d "+1], -2, v[" #d "+1]\n\t"
#define EP_GELU(R, NP)                                                                                                                       \
  ".set i_, 0\n\t.rept " #NP "\n\tv_pk_mul_f32 v[116+2*i_:117+2*i_], v[" #R "+2*i_:" #R "+1+2*i_], v[" #R "+2*i_:" #R "+1+2*i_]\n\t.set i_, i_+1\n\t.endr\n\t" \
  ".set i_, 0\n\t.rept " #NP "\n\tv_pk_fma_f32 v[116+2*i_:117+2*i_], v[116+2*i_:117+2*i_], %[B], %[A]\n\t.set i_, i_+1\n\t.endr\n\t"          \
  ".set i_, 0\n\t.rept " #NP "\n\tv_pk_mul_f32 v[116+2*i_:117+2*i_], v[116+2*i_:117+2*i_], v[" #R "+2*i_:" #R "+1+2*i_]\n\t.set i_, i_+1\n\t.endr\n\t" \
  ".set i_, 0\n\t.rept 2*" #NP "\n\tv_exp_f32 v[116+i_], v[116+i_]\n\t.set i_, i_+1\n\t.endr\n\t"                                           \
  ".set i_, 0\n\t.rept " #NP "\n\tv_pk_fma_f32 v[116+2*i_:117+2*i_], v[116+2*i_:117+2*i_], %[I], %[I]\n\t.set i_, i_+1\n\t.endr\n\t"          \
  ".set i_, 0\n\t.rept 2*" #NP "\n\tv_rcp_f32 v[116+i_], v[116+i_]\n\t.set i_, i_+1\n\t.endr\n\t"                                           \
  ".set i_, 0\n\t.rept " #NP "\n\tv_pk_mul_f32 v[" #R "+2*i_:" #R "+1+2*i_], v[" #R "+2*i_:" #R "+1+2*i_], v[116+2*i_:117+2*i_]\n\t.set i_, i_+1\n\t.endr\n\t"
struct GeluK { f32x2 nm, A, B, I; };
DI GeluK gelu_consts(float mean, float r, float ir) {
  const float c0 = -2.f * 1.4426950408889634f * 0.7978845608028654f, c1 = c0 * 0.044715f, A = c0 * r, B = c1 * r * r * r;
  return GeluK{f32x2{-mean, -mean}, f32x2{A, A}, f32x2{B, B}, f32x2{ir, ir}};
}
template <int T, int NT, int D> DI void ep_gelu(const GeluK& k, f32x4 c0, f32x4 c1) {
  asm volatile(
      "v_pk_fma_f32 v[%c[r]:%c[r]+1], %[nm], %[ca], v[%c[r]:%c[r]+1]\n\tv_pk_fma_f32 v[%c[r]+2:%c[r]+3], %[nm], %[cb], v[%c[r]+2:%c[r]+3]\n\t"
      ".if %c[nt] == 2\n\tv_pk_fma_f32 v[%c[r]+4:%c[r]+5], %[nm], %[cc], v[%c[r]+4:%c[r]+5]\n\tv_pk_fma_f32 v[%c[r]+6:%c[r]+7], %[nm], %[cd], v[%c[r]+6:%c[r]+7]\n\t.endif\n\t"
      EP_GELU(%c[r], 2*%c[nt])
      EP_PACK(%c[d], %c[r]) ".if %c[nt] == 2\n\t" EP_PACK(%c[d]+2, %c[r]+4) ".endif\n\t"
      EP_GUARD(%c[d]) ".if %c[nt] == 2\n\t" EP_GUARD(%c[d]+2) ".endif"
      ::[nm] "v"(k.nm), [A] "v"(k.A), [B] "v"(k.B), [I] "v"(k.I), [ca] "v"(f32x2{c0[0], c0[1]}), [cb] "v"(f32x2{c0[2], c0[3]}), [cc] "v"(f32x2{c1[0], c1[1]}),
        [cd] "v"(f32x2{c1[2], c1[3]}), [r] "n"(228 + 4 * T), [nt] "n"(NT), [d] "n"(D));
}
template <int R> DI void st_frag(u32x4 srd, unsigned voff, unsigned soff) { asm volatile("buffer_store_dwordx4 v[%c3:%c3+3], %0, %1, %2 offen sc1\n\ts_nop 1" ::"v"(voff), "s"(srd), "s"(soff), "n"(R) : "memory"); }
template <int R> DI void st_single(u32x4 srd, unsigned vsg, unsigned soff) {
  asm volatile("v_mov_b32 v[%c3+2], v[%c3]\n\tv_mov_b32 v[%c3+3], v[%c3+1]\n\tv_mov_b32 v248, v[%c3]\n\tv_mov_b32 v249, v[%c3+1]\n\ts_nop 1\n\t"
               "v_permlane16_swap_b32 v248, v[%c3+2]\n\tv_permlane16_swap_b32 v249, v[%c3+3]\n\t"
               "buffer_store_dwordx4 v[%c3:%c3+3], %0, %1, %2 offen sc1\n\ts_nop 1" ::"v"(vsg), "s"(srd), "s"(soff), "n"(R) : "memory");
}
template <int R> DI void st_tile(u32x4 srd, unsigned lane16, unsigned vsg, unsigned sp, unsigned ssg) {
  asm volatile(EP_PACK(116, %c0) EP_PACK(118, %c0+4) EP_GUARD(116) EP_GUARD(118) EP_PACK(120, %c0+8) EP_PACK(122, %c0+12) ::"n"(R));
  st_frag<116>(srd, lane16, sp);
  asm volatile(EP_GUARD(120) EP_GUARD(122) EP_PACK(250, %c0+16) ::"n"(R));
  st_frag<120>(srd, lane16, sp + 1024);
  asm volatile(EP_GUARD(250));
  st_single<250>(srd, vsg, ssg);
}
template <int R> DI void st_stats(__amdgpu_buffer_rsrc_t rs, unsigned vst, unsigned off) {
  float s, ss;
  asm volatile("v_pk_add_f32 v[116:117], v[%c2:%c2+1], v[%c2+2:%c2+3]\n\tv_pk_mul_f32 v[118:119], v[%c2:%c2+1], v[%c2:%c2+1]\n\tv_pk_mul_f32 v[120:121], v[%c2+2:%c2+3], v[%c2+2:%c2+3]\n\t"
               ".set i_, 2\n\t.rept 8\n\tv_pk_add_f32 v[116:117], v[116:117], v[%c2+2*i_:%c2+2*i_+1]\n\t"
               "v_pk_fma_f32 v[118+2*(i_&1):119+2*(i_&1)], v[%c2+2*i_:%c2+2*i_+1], v[%c2+2*i_:%c2+2*i_+1], v[118+2*(i_&1):119+2*(i_&1)]\n\t.set i_, i_+1\n\t.endr\n\t"
               "v_pk_add_f32 v[118:119], v[118:119], v[120:121]\n\tv_add_f32 %0, v116, v117\n\tv_add_f32 %1, v118, v119"
               : "=v"(s), "=v"(ss) : "n"(R));
  s = sum_q(s); ss = sum_q(ss);
  st_sc1(u32x2{nosent(__float_as_uint(s)), nosent(__float_as_uint(ss))}, rs, vst, off);
}
DI void read_stats(__amdgpu_buffer_rsrc_t rs, unsigned voff, unsigned soff, float& mean, float& var) {
  u32x4 st = __builtin_bit_cast(u32x4, vget4<250>());
  if (__builtin_expect(__any(umax3(max(st[0], st[1]), st[2], st[3]) == SENT), 0)) {
#pragma unroll 1
    for (unsigned sp = 0; sp < SPIN_CAP; ++sp) {
      asm volatile("s_sleep 8" ::: "memory");
      st = ld_sc1(rs, voff, soff);
      if (!__any(umax3(max(st[0], st[1]), st[2], st[3]) == SENT)) break;
    }
  }
  const float s = sum_q(__uint_as_float(st[0]) + __uint_as_float(st[2])), ss = sum_q(__uint_as_float(st[1]) + __uint_as_float(st[3]));
  mean = s * (1.f / D_);
  var = fmaxf(ss * (1.f / D_) - mean * mean, 0.f);
}
template <unsigned long long MASK> DI void arrive(u32x4 srd, unsigned voff) {
  unsigned long long sv;
  asm volatile("s_mov_b64 %0, exec\n\ts_mov_b64 exec, %3\n\tv_mov_b32 v249, 1\n\tbuffer_atomic_add v249, %1, %2, 0 offen sc1\n\ts_mov_b64 exec, %0"
               : "=&s"(sv) : "v"(voff), "s"(srd), "n"(MASK) : "memory");
}
template <unsigned long long MASK> DI void await(u32x4 srd, unsigned voff, unsigned expect) {
  unsigned long long sv; unsigned n;
#define POLL(R) "s_waitcnt vmcnt(2)\n\tv_cmp_ne_u32 vcc, %4, " #R "\n\ts_cbranch_vccz 2f\n\tv_mov_b32 " #R ", 0\n\tbuffer_atomic_add " #R ", %2, %3, 0 offen sc0 sc1\n\t"
  asm volatile("s_mov_b64 %0, exec\n\ts_mov_b64 exec, %5\n\ts_mov_b32 %1, 0\n\tv_mov_b32 v126, 0\n\tv_mov_b32 v127, 0\n\tv_mov_b32 v248, 0\n\t"
               "buffer_atomic_add v126, %2, %3, 0 offen sc0 sc1\n\ts_sleep %c6\n\tbuffer_atomic_add v127, %2, %3, 0 offen sc0 sc1\n\ts_sleep %c6\n\tbuffer_atomic_add v248, %2, %3, 0 offen sc0 sc1\n\t"
               "1:\n\t" POLL(v126) POLL(v127) POLL(v248)
               "s_add_u32 %1, %1, 1\n\ts_cmp_lt_u32 %1, 0x8000\n\ts_cbranch_scc1 1b\n\t"
               "2:\n\ts_mov_b64 exec, %0"
               : "=&s"(sv), "=&s"(n) : "v"(voff), "s"(srd), "v"(expect), "n"(MASK), "n"(PSL) : "memory", "scc", "vcc");
#undef POLL
}

template <int R> DI void warm_code(int part, int lane) {
  unsigned long long pc; unsigned len;
  asm volatile("s_getpc_b64 %0\n\t1:\n\ts_mov_b32 %1, .Lk_code_end-1b" : "=s"(pc), "=s"(len));
  asm volatile("global_load_dword v[%c2], %0, %1" ::"v"(min((unsigned)(part * 64 + lane) * 128u, len - 4u)), "s"(pc), "n"(R) : "memory");
}
template <int R> DI void ld_cs(u32x4 srd, unsigned voff, unsigned step) {
  asm volatile(".set i_, 0\n\t.rept 20\n\tbuffer_load_dwordx4 v[%c3+4*i_:%c3+4*i_+3], %0, %1, 0 offen sc1\n\tv_add_u32 %0, %0, %2\n\t.set i_, i_+1\n\t.endr" : "+v"(voff) : "s"(srd), "v"(step), "n"(R) : "memory");
}
template <int R> DI void all_flags(__amdgpu_buffer_rsrc_t rs, unsigned off, unsigned epoch, unsigned lane16) {
  u32x4 f = __builtin_bit_cast(u32x4, vget4<R>());
#pragma unroll 1
  for (unsigned sp = 0; sp < SPIN_CAP && !__all(f[0] == epoch && f[1] == epoch && f[2] == epoch && f[3] == epoch); ++sp) {
    asm volatile("s_sleep 8" ::: "memory");
    f = ld_sc1(rs, lane16, off);
  }
}
DI void peek(u32x4 srd, unsigned voff) {
  unsigned long long sv;
  asm volatile("s_mov_b64 %0, exec\n\ts_mov_b64 exec, 1\n\tv_mov_b32 v124, 0\n\tbuffer_atomic_add v124, %1, %2, 0 offen sc0 sc1\n\ts_mov_b64 exec, %0" : "=&s"(sv) : "v"(voff), "s"(srd) : "memory");
}
template <int VM> DI void peek_await(u32x4 srd, unsigned voff, unsigned expect) {
  unsigned c;
  asm volatile("s_waitcnt vmcnt(%c1)\n\tv_readfirstlane_b32 %0, v124" : "=s"(c) : "n"(VM) : "memory");
  if (__builtin_expect(c != expect, 0)) await<1ull>(srd, voff, expect);
}
template <int R> DI u32x4 tr_col(unsigned sel) {
  unsigned o0, o1, o2, o3;
  asm volatile("v_perm_b32 %0, v[%c5+4], v[%c5], %4\n\tv_perm_b32 %1, v[%c5+12], v[%c5+8], %4\n\tv_perm_b32 %2, v[%c5+20], v[%c5+16], %4\n\tv_perm_b32 %3, v[%c5+28], v[%c5+24], %4\n\ts_nop 1"
               : "=v"(o0), "=v"(o1), "=v"(o2), "=v"(o3) : "s"(sel), "n"(R));
  return u32x4{o0, o1, o2, o3};
}

#define AE(i) "v_exp_f32 v[%c[sc]+" #i "], v[%c[sc]+" #i "]\n\t"
#define AC(d, i) "v_cvt_pk_bf16_f32 v" #d ", v[%c[sc]+" #i "], v[%c[sc]+" #i "+1]\n\t"
template <int SC, int SN, int OV> DI void att_a(unsigned vA) {
  asm volatile(
      "s_waitcnt lgkmcnt(1)\n\t"
      "v_mfma_f32_32x32x16_bf16 v[144:159], v[242:245], v[228:231], v[144:159]\n\t"
      AE(0) AE(1) "ds_read_b128 v[242:245], %[va] offset:%c[ov]+704\n\t" AE(2) AE(3) AC(224, 0)
      "v_mfma_f32_32x32x4_2b_bf16 v[%c[sn]:%c[sn]+31], v[236:237], v[254:255], 0\n\t"
      AC(225, 2) AE(4) AE(5) AE(6) AE(7) AC(226, 4) AC(227, 6) AE(8) AE(9) AE(10) AE(11) AC(228, 8)
      "s_waitcnt lgkmcnt(1)\n\t"
      "v_mfma_f32_32x32x16_bf16 v[128:143], v[238:241], v[224:227], v[128:143]\n\t"
      AC(229, 10) AE(12) AE(13) AE(14) AE(15) AC(230, 12) AC(231, 14)
      ::[va] "v"(vA), [sc] "n"(SC), [sn] "n"(SN), [ov] "n"(OV) : "memory");
}
template <int SC, int SN, int OK> DI void att_b(unsigned kA, unsigned kB, unsigned vA) {
  asm volatile(
      AE(16) "s_waitcnt lgkmcnt(0)\n\t"
      "v_mfma_f32_32x32x16_bf16 v[128:143], v[242:245], v[228:231], v[128:143]\n\t"
      AE(17) AE(18) AE(19) AC(224, 16)
      "v_mfma_f32_32x32x16_bf16 v[%c[sn]:%c[sn]+15], v[232:235], v[246:249], v[%c[sn]:%c[sn]+15]\n\t"
      AC(225, 18) AE(20) AE(21) AE(22) AE(23) AC(226, 20) AC(227, 22)
      "v_mfma_f32_32x32x16_bf16 v[%c[sn]+16:%c[sn]+31], v[232:235], v[250:253], v[%c[sn]+16:%c[sn]+31]\n\t"
      AE(24) AE(25) "ds_read_b128 v[232:235], %[ka] offset:%c[ok]\n\tds_read_b64 v[236:237], %[kb] offset:%c[ok]\n\t" AE(26) AE(27) AC(228, 24)
      "v_mfma_f32_32x32x16_bf16 v[144:159], v[238:241], v[224:227], v[144:159]\n\t"
      AC(229, 26) AE(28) AE(29) AE(30) AE(31) AC(230, 28) AC(231, 30)
      "ds_read_b128 v[238:241], %[va] offset:%c[ok]"
      ::[ka] "v"(kA), [kb] "v"(kB), [va] "v"(vA), [sc] "n"(SC), [sn] "n"(SN), [ok] "n"(OK) : "memory");
}
#undef AE
#undef AC
template <int SEND, int KEEP> DI unsigned long long att_merge(unsigned mine, unsigned theirs) {
  unsigned long long m;
  asm volatile("ds_write_b128 %1, v[%c3:%c3+3]\n\tds_write_b128 %1, v[%c3+4:%c3+7] offset:1024\n\tds_write_b128 %1, v[%c3+8:%c3+11] offset:2048\n\t"
               "s_waitcnt vmcnt(0) lgkmcnt(0)\n\ts_barrier\n\t"
               "ds_read_b128 v[168:171], %2 offset:2048\n\tds_read_b128 v[160:163], %2\n\tds_read_b128 v[164:167], %2 offset:1024\n\ts_waitcnt lgkmcnt(2)\n\t"
               "v_pk_add_f32 v[168:169], v[168:169], v[%c4+8:%c4+9]\n\tv_pk_add_f32 v[170:171], v[170:171], v[%c4+10:%c4+11]\n\t"
               "v_mov_b32 v172, v168\n\tv_mov_b32 v173, v168\n\ts_waitcnt lgkmcnt(0)\n\t"
               ".set i_, 0\n\t.rept 4\n\tv_pk_add_f32 v[160+2*i_:161+2*i_], v[160+2*i_:161+2*i_], v[%c4+2*i_:%c4+2*i_+1]\n\t.set i_, i_+1\n\t.endr\n\t"
               "v_permlane32_swap_b32 v172, v173\n\t"
               "v_pk_mul_f32 v[174:175], v[168:169], 0\n\tv_pk_mul_f32 v[178:179], v[170:171], 0\n\tv_rcp_f32 v176, v173\n\t"
               "v_pk_fma_f32 v[174:175], v[160:161], 0, v[174:175]\n\tv_pk_fma_f32 v[178:179], v[162:163], 0, v[178:179]\n\t"
               "v_pk_fma_f32 v[174:175], v[164:165], 0, v[174:175]\n\tv_pk_fma_f32 v[178:179], v[166:167], 0, v[178:179]\n\t"
               "v_pk_add_f32 v[174:175], v[174:175], v[178:179]\n\tv_add_f32 v173, v173, v174\n\tv_add_f32 v173, v173, v175\n\t"
               "v_cmp_nle_f32 vcc, 0x21800000, v173\n\ts_mov_b64 %0, vcc\n\tv_cmp_nge_f32 vcc, 0x71800000, v173\n\ts_or_b64 %0, %0, vcc"
               : "=&s"(m) : "v"(mine), "v"(theirs), "n"(SEND), "n"(KEEP) : "memory", "vcc", "scc");
  return m;
}

__global__ void __launch_bounds__(512, 2) __attribute__((amdgpu_num_vgpr(48))) k_main(const char* __restrict__ x, const char* __restrict__ wqkv, const char* __restrict__ wo, const char* __restrict__ w1, const char* __restrict__ w2,
    const char* __restrict__ w3, const char* __restrict__ w4, char* __restrict__ ws, char* __restrict__ out) {
  extern __shared__ __attribute__((aligned(16))) char smem[];
  lds_ptr lds = (lds_ptr)smem;
  OWN_ALL_VGPRS();
  asm volatile(".subsection 2\n\t.rept 96\n\ts_nop 0\n\t.endr\n\t.Lk_code_end:\n\t.subsection 0");
  STAMP_DECL; STAMP(0); SPAN_BEGIN;
  const unsigned ldsb = (unsigned)(size_t)lds;
  const int tid = threadIdx.x, lane = tid & 63, w = __builtin_amdgcn_readfirstlane(tid >> 6), q = lane >> 4, tok = lane & 15;
  const int bid = blockIdx.x, xcd = bid & 7, r = bid >> 3;
  const int j = r & 7, head = 8 * (xcd & 3) + j, qb = 4 * (xcd >> 2) + (r >> 3), rb = 4 * qb + (xcd & 3);
  const __amdgpu_buffer_rsrc_t rsw = mk_rsrc(ws, WS_END);
  const unsigned wfs = OFF_WF + j * NGRP * GRP_B;
  const unsigned lane16 = lane * 16;

  const int rb1 = 2 * xcd + (r & 1), g1 = ((r >> 1) + 15) & 15;
  P1Load L1;
  const bool loader = w >= 4 && g1 < 15;
  if (__builtin_expect(loader, 1)) L1 = p1_start(x, wqkv, rb1, g1, ROTF, w - 4, lane);
  const u32x4 srd = mk_srd(ws, WS_END);
  unsigned epoch = 0;
  Setup su;
  auto fills = [&](int n, int first) AI {
    const u32x4 sent = {SENT, SENT, SENT, SENT};
#pragma unroll 1
    for (int i = 0; i < n; ++i) st_sc1(sent, rsw, lane16, first + i < 40 ? OFF_O + (bid * 40 + first + i) * 1024 : OFF_ST1 + bid * 1024);
  };
  if (loader) {
    asm volatile("s_waitcnt vmcnt(16)\n\ts_barrier" ::: "memory");
    p1_issue(L1, 3);
#pragma unroll 1
    for (int pp = 0; pp < 6; ++pp) {
      asm volatile("s_waitcnt vmcnt(16)\n\ts_barrier" ::: "memory");
      p1_issue(L1, pp + 4);
    }
    asm volatile("s_waitcnt vmcnt(16)\n\ts_barrier" ::: "memory");
    if (w == 4) {
      epoch = *(volatile __attribute__((address_space(3))) unsigned*)(lds + L_EPO);
      peek(srd, OFF_AXF + rb1 * 128);
      asm volatile("s_waitcnt vmcnt(9)\n\ts_barrier\n\ts_waitcnt vmcnt(0)" ::: "memory");
      const bool late = __builtin_amdgcn_readfirstlane(__float_as_uint(vget<124>())) != __builtin_amdgcn_readfirstlane(epoch);
      if (late) { asm volatile("s_barrier\n\ts_barrier" ::: "memory"); await<1ull>(srd, OFF_AXF + rb1 * 128, epoch); }
      const u32x4 xs = ld_sc1(rsw, lane16, OFF_XST + rb1 * 1024);
      if (!late) asm volatile("s_barrier\n\ts_barrier" ::: "memory");
      *(__attribute__((address_space(3))) u32x4*)(lds + 16384 + lane16) = xs;
      asm volatile("s_waitcnt lgkmcnt(0)\n\ts_barrier" ::: "memory");
    } else {
      if (FPOS == 0) fills(14, min(14 * (w - 5), 27));
      asm volatile("s_waitcnt vmcnt(%c0)\n\ts_barrier" ::"n"(FPOS == 0 ? 22 : 8) : "memory");
      if (FPOS == 1) fills(14, min(14 * (w - 5), 27));
      asm volatile("s_waitcnt vmcnt(%c0)\n\ts_barrier" ::"n"(FPOS <= 1 ? 14 : 0) : "memory");
      if (FPOS == 2) fills(14, min(14 * (w - 5), 27));
      asm volatile("s_barrier\n\ts_barrier" ::: "memory");
      if (FPOS == 3) fills(14, min(14 * (w - 5), 27));
    }
    su = make_setup(lane, w, bid, head);
  } else {
    if (w == 0) {
      epoch = __builtin_amdgcn_raw_buffer_load_b32(rsw, 0, OFF_EPO, AUX_SC1);
      st_sc1(u32x4{0u, 0u, 0u, 0u}, rsw, (lane >> 4) * 2048 + (lane & 15) * 16, OFF_CNT + (32 * rb + j) * 256);
      st_sc1(u32x4{0u, 0u, 0u, 0u}, rsw, lane < 16 ? lane16 : 0x80000000u, OFF_QC + (head * 8 + qb) * 256);
    }
    if (w == 1) warm_code<255>(r & 7, lane);
    if (w == 2 && r < 15) {
      const char* pb = r < 8 ? ws + r * (64u << 16) : r < 10 ? out : r == 10 ? wo : r == 11 ? w1 : r == 12 ? w2 : r == 13 ? w3 : w4;
      const unsigned pn = r < 8 ? WS_END - r * (64u << 16) : r < 10 ? S_ * D_ * 2 : r < 13 ? D_ * D_ * 2 : D_ * F_ * 2;
      asm volatile("global_load_dword v255, %0, off" ::"v"(pb + min((unsigned)lane << 16, pn - 4u)) : "memory");
    }
    auto ready = [&]() AI {
      epoch = __builtin_amdgcn_readfirstlane(epoch) + 1u;
      __builtin_amdgcn_raw_buffer_store_b32(epoch, rsw, lane == 0 ? bid * 4 : 0x80000000u, OFF_RDY, AUX_SC1);
      *(__attribute__((address_space(3))) unsigned*)(lds + L_EPO) = epoch;
    };
    if (g1 == 15) {
      p1_stats(x, rsw, rb1, w, lane, [&]() AI { if (w == 0) ready(); });
      if (w == 0) __builtin_amdgcn_raw_buffer_store_b32(epoch, rsw, lane == 0 ? 0 : 0x80000000u, OFF_AXF + rb1 * 128, AUX_SC1);
      if (w > 4) fills(14, min(14 * (w - 5), 27));
      su = make_setup(lane, w, bid, head);
    } else {
      su = make_setup(lane, w, bid, head);
    }
    auto started = [&]() AI { if (w == 0) { asm volatile("s_waitcnt vmcnt(0)" ::: "memory"); ready(); } };
    STAMP(47);
    if (tid < 80 && tid != 3) *(__attribute__((address_space(3))) unsigned*)(lds + L_FILL + tid * 4) = tid == 0 ? 4u : (tid >> 2) != 1 ? 0u : tid < 6 ? tid : (unsigned)NGRP;
    if (g1 < 10) p1_compute<true>(srd, rsw, lds, rb1, g1, w, lane, started STAMP_ARG);
    else if (g1 < 15) p1_compute<false>(srd, rsw, lds, rb1, g1, w, lane, started STAMP_ARG);
    else if (w == 0) {
      asm volatile("s_sleep 64" ::: "memory");
      asm volatile("buffer_load_dwordx4 v[250:253], %0, %1, %2 offen sc1" ::"v"(lane16), "s"(srd), "s"(OFF_RDY) : "memory");
    }
  }
  STAMP(43);
  const int hi = lane >> 5, r32 = lane & 31, wq = w & 3, wk = w >> 2;
  const bool has_job = w * 256 + bid < 850;
  unsigned kA = su.kA, kB = su.kB, vA = su.vA;
  const char* src = su.mat == 0 ? wo : su.mat == 1 ? w1 : su.mat == 2 ? w2 : su.mat == 3 ? w3 : w4;
  auto job_rows = [&]() AI {
    const int n2 = su.mat == 3 ? 2 * F_ : 2 * D_;
    gload4<96, 0>(src, su.jo0); gload4<100, 0>(src + n2, su.jo0); gload4<104, 0>(src + 2 * n2, su.jo0); gload4<108, 0>(src + 3 * n2, su.jo0);
    gload4<112, 0>(src, su.jo4); gload4<116, 0>(src + n2, su.jo4); gload4<120, 0>(src + 2 * n2, su.jo4); gload4<124, 0>(src + 3 * n2, su.jo4);
  };
  auto job_cols = [&](auto S_) AI {
    constexpr int sl = S_.value;
    const u32x4 o0 = tr_col<96 + sl>(0x05040100u), o1 = tr_col<96 + sl>(0x07060302u);
    st_sc1(o0, rsw, su.jvo[sl >> 1] + (sl & 1) * 128, su.jso);
    st_sc1(o1, rsw, su.jvo[sl >> 1] + (sl & 1) * 128 + 64, su.jso);
    if (su.mat == 1 || su.mat == 3) {
      const bf16x8 ones = {0x3f80, 0x3f80, 0x3f80, 0x3f80, 0x3f80, 0x3f80, 0x3f80, 0x3f80};
      const f32x4 z = {0.f, 0.f, 0.f, 0.f};
      const f32x4 c0 = MFMA16(__builtin_bit_cast(bf16x8, o0), ones, z), c1 = MFMA16(__builtin_bit_cast(bf16x8, o1), ones, z);
      st_sc1(__builtin_bit_cast(u32x4, (lane & 1) ? c1 : c0), rsw, su.jcs, su.jcso + sl * 128);
    }
  };
  {
    asm volatile("s_waitcnt vmcnt(0) lgkmcnt(0)\n\ts_barrier" ::: "memory");
    STAMP(44);
    asm volatile(".set i_, 0\n\t.rept 32\n\tv_mov_b32 v[128+i_], 0\n\t.set i_, i_+1\n\t.endr\n\t"
                 ".set i_, 0\n\t.rept 4\n\tv_mov_b32 v[228+i_], 0\n\tv_mov_b32 v[242+i_], 0\n\t.set i_, i_+1\n\t.endr");
    if (w == 0) {
      all_flags<250>(rsw, OFF_RDY, epoch, lane16);
      const int gt = g1 < 5 ? g1 : g1 < 10 ? g1 - 5 : g1 - 10, hh = (128 * gt) / 20 + (lane >> 3);
      if (g1 < 15 && hh <= (128 * gt + 127) / 20 && (g1 >= 5 || (lane & 7) == (rb1 >> 1)))
        __hip_atomic_fetch_add((unsigned*)(ws + OFF_QC + (hh * 8 + (lane & 7)) * 256 + (rb1 & 3) * 64), 1u, __ATOMIC_RELAXED, __HIP_MEMORY_SCOPE_AGENT);
      if (bid == 0) __builtin_amdgcn_raw_buffer_store_b32(epoch, rsw, lane == 0 ? 0 : 0x80000000u, OFF_EPO, AUX_SC1);
      const int span = 1 + ((20 * head + 19) >> 7) - ((20 * head) >> 7);
      await<0xfull>(srd, OFF_QC + (head * 8 + qb) * 256 + lane * 64, (8 + ((lane >> 1) == (qb & 1))) * span);
    }
    asm volatile("s_barrier" ::: "memory");
    STAMP(45);
  }

  {
    asm volatile("buffer_load_dwordx4 v[246:249], %0, %2, %3 offen sc1\n\tbuffer_load_dwordx4 v[250:253], %0, %2, %3 offen offset:1280 sc1\n\t"
                 "buffer_load_dwordx2 v[254:255], %1, %2, %3 offen offset:1024 sc1" ::"v"(su.qv0), "v"(su.qv1), "s"(srd), "s"(OFF_QF + (head * 64 + 8 * qb + 2 * wq) * QF_TILE) : "memory");
  }
  auto kv_piece = [&](int c, int i) AI { bdma_sc1(srd, su.kvs[i], OFF_KV + c * (4 * KV_BLK), min(w + 8 * i, 21) * 1024 + kv_at(c)); };
  kv_piece(0, 0); kv_piece(0, 1); kv_piece(0, 2);
  kv_piece(1, 0); kv_piece(1, 1); kv_piece(1, 2);
  asm volatile(
      "s_waitcnt vmcnt(3) lgkmcnt(0)\n\ts_barrier\n\t"
      "ds_read_b64 v[236:237], %1\n\tds_read_b128 v[232:235], %0\n\ts_waitcnt lgkmcnt(1)\n\t"
      "v_mfma_f32_32x32x4_2b_bf16 v[160:191], v[236:237], v[254:255], 0\n\t"
      "s_nop 15\n\ts_nop 1\n\ts_waitcnt lgkmcnt(0)\n\t"
      "v_mfma_f32_32x32x16_bf16 v[160:175], v[232:235], v[246:249], v[160:175]\n\t"
      "v_mfma_f32_32x32x16_bf16 v[176:191], v[232:235], v[250:253], v[176:191]\n\t"
      "ds_read_b128 v[232:235], %0 offset:%c3\n\tds_read_b64 v[236:237], %1 offset:%c3\n\tds_read_b128 v[238:241], %2 offset:%c3\n\t"
      "s_nop 7"
      ::"v"(kA), "v"(kB), "v"(vA), "n"(KV_REC) : "memory");
  STAMP(2);
  const unsigned rt = 4 * rb + (w & 3);
  const unsigned kp = 5 * (j >> 1) + 3 * (j & 1), ksg = 5 * (j >> 1) + 2;
  const unsigned rowoff = ((16 * rt + tok) * D_) * 2;
  const unsigned colp = (32 * kp + 8 * q) * 2, cols = (32 * ksg + 16 * (j & 1) + 4 * q) * 2;
  auto side = [&](auto C_, auto S_) AI {
    constexpr int c = C_.value, sl = S_.value, t = 7 * c + sl;
    if constexpr (c == 0) {
      if constexpr (sl < 6) { kv_piece(2 + 2 * sl / 3, 2 * sl % 3); kv_piece(2 + (2 * sl + 1) / 3, (2 * sl + 1) % 3); }
      else { const int i = min(w, 5); bdma_sc1(srd, su.kvw, OFF_KV, ldsb + L_WRAP + i * 1024); }
    } else if constexpr (c == 1) {
      if constexpr (sl >= 4) { kv_piece(6, sl - 4); kv_piece(7, sl - 4); }
      if constexpr (sl == 6) if (has_job) job_rows();
    } else if constexpr (c == 2) {
    } else if constexpr (c == 3) {
      if constexpr (sl >= JS && sl < JS + 4) if (has_job) {
        if (sl == JS) asm volatile("s_waitcnt vmcnt(0)" ::: "memory");
        job_cols(IC<sl - JS>{});
      }
    } else if constexpr (c == 4) {
      if constexpr (sl == 4) if (tid == 0) __hip_atomic_store((unsigned*)(ws + OFF_FLAG) + bid, epoch, __ATOMIC_RELAXED, __HIP_MEMORY_SCOPE_AGENT);
      if constexpr (sl == 5) if (w < 4) {
        gload4<116, 0>(x, rowoff + colp); gload4<120, 64>(x, rowoff + colp);
        asm volatile("global_load_dwordx2 v[124:125], %0, %1" ::"v"(rowoff + cols), "s"(x) : "memory");
      }
    } else if constexpr (t < 39) {
      if constexpr (sl == 1) if (w == 0) asm volatile("buffer_load_dwordx4 v[96:99], %0, %1, %2 offen sc1" ::"v"(lane16), "s"(srd), "s"(OFF_FLAG) : "memory");
    } else if constexpr (t < 52) {
      const unsigned pc = (w + 8 * (t - 39)) * 1024;
      if (t < 51 || w < 4) bdma_sc1(srd, lane16, wfs + pc, ldsb + pc);
    }
  };
  const int qt = 8 * qb + 2 * wq + wk;
  const unsigned mine = ldsb + (w < 6 ? L_KV0 + w * 3072 : L_SCR2 + (w - 6) * 3072) + lane16;
  const unsigned theirs = ldsb + ((w ^ 4) < 6 ? L_KV0 + (w ^ 4) * 3072 : L_SCR2 + ((w ^ 4) - 6) * 3072) + lane16;
  const unsigned rto = OFF_O + (2 * qt + (r32 >> 4)) * FM_RT + (r32 & 15) * 16;
  const unsigned ov0 = rto + (5 * (head >> 1) + 3 * (head & 1) + hi) * 256, ov1 = hi ? 0x80000000u : rto + (5 * (head >> 1) + 2) * 256 + (head & 1) * 8;
  const unsigned ov2 = OFF_CNT + (16 * qt + lane) * 256 + (head >> 3) * 64;
  static_for<8>([&](auto C_) AI {
    constexpr int c = C_.value;
    if constexpr (c == 7) asm volatile("" ::"v"(mine), "v"(theirs), "v"(ov0), "v"(ov1), "v"(ov2));
    __builtin_amdgcn_s_setprio(2);
    att_a<160, 192, 1 * KV_REC>(vA); side(C_, IC<0>{}); att_b<160, 192, 2 * KV_REC>(kA, kB, vA); side(C_, IC<1>{});
    __builtin_amdgcn_s_setprio(1);
    att_a<192, 160, 2 * KV_REC>(vA); side(C_, IC<2>{}); att_b<192, 160, 3 * KV_REC>(kA, kB, vA); side(C_, IC<3>{});
    __builtin_amdgcn_s_setprio(0);
    att_a<160, 192, 3 * KV_REC>(vA);
    if constexpr (c == 5) if (w == 0) {
      asm volatile("s_waitcnt vmcnt(0)" ::: "memory");
      all_flags<96>(rsw, OFF_FLAG, epoch, lane16);
    }
    if constexpr (c == 0) asm volatile("s_waitcnt vmcnt(8) lgkmcnt(0)\n\ts_barrier" ::: "memory");
    else if constexpr (c == 1 || c == 4) asm volatile("s_waitcnt vmcnt(0) lgkmcnt(0)\n\ts_barrier" ::: "memory");
    else if constexpr (c == 5 || c == 6) asm volatile("s_waitcnt lgkmcnt(0)\n\ts_barrier" ::: "memory");
    __builtin_amdgcn_s_setprio(3);
    { const int d = c < 7 ? kv_at(c + 1) - kv_at(c) : L_WRAP - kv_at(7) + wk * (3072 - KV_HALF); kA += d; kB += d; vA += d; }
    att_b<160, 192, 0>(kA, kB, vA); side(C_, IC<4>{});
    att_a<192, 160, 0>(vA); side(C_, IC<5>{});
    att_b<192, 160, KV_REC>(kA, kB, vA); side(C_, IC<6>{});
  });
  {
    asm volatile("s_waitcnt lgkmcnt(0)\n\tv_mfma_f32_32x32x16_bf16 v[144:159], v[242:245], v[228:231], v[144:159]\n\ts_nop 11" ::: "memory");
    STAMP(3);
    if (__builtin_expect((wk == 0 ? att_merge<144, 128>(mine, theirs) : att_merge<128, 144>(mine, theirs)) != 0, 0)) {
      const f32x16 Os = attn_safe(rsw, OFF_QF + (head * 64 + qt) * QF_TILE, head, lane);
      static_for<12>([&](auto I_) AI { vset<160 + I_.value>(__float_as_uint(Os[I_.value])); });
      auto r2 = __builtin_amdgcn_permlane32_swap(__float_as_uint(Os[8]), __float_as_uint(Os[8]), false, false);
      vset<176>(__float_as_uint(__builtin_amdgcn_rcpf(__uint_as_float(r2[1]))));
    }
    asm volatile(".set i_, 0\n\t.rept 6\n\tv_pk_mul_f32 v[160+2*i_:161+2*i_], v[160+2*i_:161+2*i_], v[176:177] op_sel_hi:[1,0]\n\t.set i_, i_+1\n\t.endr\n\t"
                 EP_PACK(180, 160) EP_PACK(182, 164) EP_PACK(184, 168) EP_GUARD(180) EP_GUARD(182) EP_GUARD(184)
                 "buffer_store_dwordx4 v[180:183], %0, %2, 0 offen sc1\n\tbuffer_store_dwordx2 v[184:185], %1, %2, 0 offen sc1" ::"v"(ov0), "v"(ov1), "s"(srd) : "memory");
    arrive<0xffffull>(srd, ov2);
    asm volatile("s_barrier" ::: "memory");
  }
  if (w >= 4) {
    typedef volatile __attribute__((address_space(3))) unsigned* lword;
    __builtin_amdgcn_s_setprio(PRIO_H);
    auto min_cons = [&]() AI {
      const unsigned c = *(lword)(lds + L_CONS + (lane & 3) * 64);
      return (int)(min(min(__builtin_amdgcn_readlane(c, 0), __builtin_amdgcn_readlane(c, 1)), min(__builtin_amdgcn_readlane(c, 2), __builtin_amdgcn_readlane(c, 3))) >> 2);
    };
    auto wait_cons = [&](int need) AI {
      int have;
#pragma unroll 1
      for (unsigned sp = 0; (have = min_cons()) < need && sp < LDS_SPIN; ++sp) asm volatile("s_sleep 2" ::: "memory");
      return have;
    };
    auto publish = [&](unsigned g) AI {
      unsigned long long sv;
      asm volatile("s_mov_b64 %0, exec\n\ts_mov_b64 exec, 1\n\tds_write_b32 %1, %3\n\tds_read_b128 v[252:255], %2 offset:16\n\ts_waitcnt lgkmcnt(0)\n\t"
                   "v_min3_u32 v252, v252, v253, v254\n\tv_min_u32 v252, v252, v255\n\tds_max_u32 %2, v252\n\ts_mov_b64 exec, %0"
                   : "=&s"(sv) : "v"(ldsb + L_OWN + (w - 4) * 4), "v"(ldsb + L_FILL), "v"(g) : "memory");
    };
    if (w >= 6) {
      if (w == 7) wait_cons(1);
      {
        const bool is1 = w == 6;
        const int c0 = is1 ? 10 * j : 4 * (27 * (j >> 2) + 7 * (j & 3)), nc = is1 ? 10 : 28;
        const int i = lane & 7, cq = (c0 & ~3) + 4 * (lane >> 3);
        ld_cs<128>(srd, (is1 ? OFF_CS1 : OFF_CS3) + ((cq >> 4) * 128 + i * 16 + (cq & 15)) * 4, (is1 ? 640 : 1792) * 4);
        asm volatile("s_waitcnt vmcnt(0)\n\t.set i_, 1\n\t.rept 19\n\tv_pk_add_f32 v[128:129], v[128:129], v[128+4*i_:129+4*i_]\n\t"
                     "v_pk_add_f32 v[130:131], v[130:131], v[130+4*i_:131+4*i_]\n\t.set i_, i_+1\n\t.endr" ::: "memory");
        static_for<4>([&](auto E_) AI {
          const float sacc = vget<128 + E_.value>();
          const int c = cq + E_.value - c0;
          int slot;
          if (is1) {
            const int cp = (j & 1) ? c - 2 : c;
            slot = (cp >= 0 && cp < 8) ? (2 * (cp >> 2) + (i >> 2)) * 16 + 4 * (cp & 3) + (i & 3) : 64 + 8 * (c & 1) + i;
          } else slot = 80 + (2 * (c >> 2) + (i >> 2)) * 16 + 4 * (c & 3) + (i & 3);
          if (c >= 0 && c < nc) *(__attribute__((address_space(3))) float*)(lds + L_CS1 + slot * 4) = sacc;
        });
      }
      *(lword)(lds + L_CSF + (w - 6) * 4) = 1u;
      return;
    }
    auto issue = [&](int G) AI {
      const unsigned so = __builtin_amdgcn_readfirstlane(wfs + G * GRP_B), d = __builtin_amdgcn_readfirstlane(ldsb + (G % 6) * GRP_B);
      if (G >= 20 && (G < 24 || G == 34)) {
        const unsigned st = G == 34 ? 4096 : 5120;
#pragma unroll
        for (int i = 0; i < 5; ++i) { bdma4_sc1(srd, lane16, so + i * st, d + i * st); }
        asm volatile("s_waitcnt vmcnt(20)" ::: "memory");
      } else {
#pragma unroll
        for (int i = 0; i < 6; ++i) { bdma4_sc1(srd, lane16, so + i * 4096, d + i * 4096); }
        bdma_sc1(srd, lane16, so + 24576, d + 24576);
        asm volatile("s_waitcnt vmcnt(25)" ::: "memory");
      }
    };
    int have = 0;
#pragma unroll 1
    for (int G = w; G < NGRP; G += 2) {
      const int need = (G >= 8 && G < POSTP && (G & 2) == 0) ? (G & ~3) - 3 : G - 5;
      if (have < need && (have = min_cons()) < need) {
        asm volatile("s_waitcnt vmcnt(0)" ::: "memory");
        publish(G);
        have = wait_cons(need);
      }
      issue(G);
      publish(G);
    }
    asm volatile("s_waitcnt vmcnt(0)" ::: "memory");
    publish(NGRP);
    return;
  }

  __builtin_amdgcn_s_setprio(3);
  const unsigned wl = ldsb + (q * 16 + 4 * (tok & 3) + (tok >> 2)) * 16;
  const unsigned wb[6] = {wl, wl + 2 * GRP_B, wl + 4 * GRP_B, ldsb + L_FILL, ldsb + L_CONS + w * 64 + tok * 4, 1u};
  const unsigned vo0[4] = {lane16, lane16, lane16, lane16};
  w_prime<0>(wb);
  const unsigned cown = OFF_CNT + (rt * 8 + j) * 256, cgrp = OFF_CNT + (rt * 8 + lane) * 256;
  const unsigned vsg = (q & 1) ? 0x80000000u : (2 * (j & 1) + (q >> 1)) * 256 + tok * 16;
  const unsigned vst = lane < 16 ? (j >> 1) * 256 + lane16 + (j & 1) * 8 : 0x80000000u;
  const unsigned wfl = wfs + (wl - ldsb);
  typedef const __attribute__((address_space(3))) f32x4* lcs;
  asm volatile(".set i_, 0\n\t.rept 10\n\tv_lshlrev_b32 v[96+2*i_], 16, v[116+i_]\n\tv_and_b32 v[97+2*i_], 0xffff0000, v[116+i_]\n\t.set i_, i_+1\n\t.endr");
  STAMP(4);
  await<0xfull>(srd, cown + lane * 64, 8);
  STAMP(5);
  ld_lead(srd, vo0, OFF_O + rt * FM_RT);
  if (__builtin_expect(run_unit<0, 4, 0, 20, vmw(63), 8, 0, 0, 0, -1, 0>(wb, srd, 0, 0, 0, 0, srd, vo0, OFF_O + rt * FM_RT + 8192 STAMP_ARG) != 0, 0)) redo_unit<false>(rsw, vo0, OFF_O + rt * FM_RT, 20, rsw, wfl, srd);
  STAMP(6);
  asm volatile(".set i_, 0\n\t.rept 10\n\tv_pk_add_f32 v[96+2*i_:97+2*i_], v[96+2*i_:97+2*i_], v[228+2*i_:229+2*i_]\n\t.set i_, i_+1\n\t.endr");
  st_tile<96>(srd, lane16, vsg, OFF_H + rt * FM_RT + kp * 1024, OFF_H + rt * FM_RT + ksg * 1024);
  arrive<0xffull>(srd, cgrp + 4);
  st_stats<96>(rsw, vst, OFF_ST1 + rt * 1024);
  STAMP(7);
  w_prime<4>(wb);
  await<1ull>(srd, cown + 4, 8);
  STAMP(8);
  ld_lead(srd, vo0, OFF_H + rt * FM_RT);
#pragma unroll 1
  for (unsigned sp = 0; sp < LDS_SPIN && !__builtin_amdgcn_readfirstlane(*(volatile __attribute__((address_space(3))) unsigned*)(lds + L_CSF)); ++sp) asm volatile("s_sleep 1" ::: "memory");
  f32x4 cs[5];
#pragma unroll
  for (int t = 0; t < 5; ++t) cs[t] = *(lcs)(lds + L_CS1 + t * 64 + q * 16);
  if (__builtin_expect(run_unit<4, 4, 0, 20, vmw(63), 8, 0, 0, 0, 2, 1>(wb, srd, lane16, OFF_ST1 + rt * 1024, 0, 0, srd, vo0, OFF_H + rt * FM_RT + 8192 STAMP_ARG) != 0, 0)) redo_unit<false>(rsw, vo0, OFF_H + rt * FM_RT, 20, rsw, wfl + 4 * GRP_B, srd);
  STAMP(9);
  float mean1, r1;
  {
    float var;
    read_stats(rsw, lane16, OFF_ST1 + rt * 1024, mean1, var);
    r1 = __builtin_amdgcn_rsqf(var + LN_EPS);
    const float v2 = var * r1 * r1 + LN_EPS, r2 = r1 * __builtin_amdgcn_rsqf(v2);
    const GeluK gk = gelu_consts(mean1, r2, __builtin_amdgcn_sqrtf((var + LN_EPS) * v2));
    const unsigned o = OFF_H1 + rt * FM_RT;
    ep_gelu<0, 2, 228>(gk, cs[0], cs[1]); st_frag<228>(srd, lane16, o + kp * 1024);
    ep_gelu<2, 2, 236>(gk, cs[2], cs[3]); st_frag<236>(srd, lane16, o + kp * 1024 + 1024);
    ep_gelu<4, 1, 244>(gk, cs[4], cs[4]); st_single<244>(srd, vsg, o + ksg * 1024);
    const int rn = (j & 2) ? (int)rt - 1 : (int)rt + 1;
    if (rn >= 0 && rn < 128) arrive<0xffffull>(srd, (lane < 8 ? cgrp : OFF_CNT + (rn * 8 + lane - 8) * 256) + 68);
    else arrive<0xffull>(srd, cgrp + 68);
  }
  {
    const u32x4 srd1 = mk_srd(ws + OFF_H1, SZ_FM);
    unsigned vo[4];
#pragma unroll
    for (int g = 0; g < 4; ++g) {
      const int T = 16 * (int)rt + tok + (g == 0 ? -1 : g == 1 ? 1 : g == 2 ? -2 : 2);
      vo[g] = (T >= 0 && T < S_) ? (unsigned)((T >> 4) * FM_RT + q * 256 + (T & 15) * 16) : 0x80000000u;
    }
    const f32x2 rr = {r1, r1}, nmr = {-mean1 * r1, -mean1 * r1};
    STAMP(10);
    w_prime<8>(wb);
    await<1ull>(srd, cown + 68, (rt == 0 || rt == 127) ? 12 : 16);
    STAMP(1);
    ld_lead(srd1, vo, 0);
    if (__builtin_expect(run_unit<8, 4, 0, 20, vmw(63), 8, 0, 0, 0, -1, 0>(wb, srd, 0, 0, 0, 0, srd1, vo, 8192 STAMP_ARG) != 0, 0)) redo_unit<false>(mk_rsrc(ws + OFF_H1, SZ_FM), vo, 0, 20, rsw, wfl + 8 * GRP_B, srd1);
    STAMP(11);
    asm volatile(".set i_, 0\n\t.rept 10\n\tv_pk_fma_f32 v[228+2*i_:229+2*i_], v[96+2*i_:97+2*i_], %0, v[228+2*i_:229+2*i_]\n\t.set i_, i_+1\n\t.endr\n\t"
                 ".set i_, 0\n\t.rept 10\n\tv_pk_add_f32 v[228+2*i_:229+2*i_], v[228+2*i_:229+2*i_], %1\n\t.set i_, i_+1\n\t.endr" ::"v"(rr), "v"(nmr));
    st_tile<228>(srd, lane16, vsg, OFF_X1 + rt * FM_RT + kp * 1024, OFF_X1 + rt * FM_RT + ksg * 1024);
    arrive<0xffull>(srd, cgrp + 132);
    st_stats<228>(rsw, vst, OFF_ST2 + rt * 1024);
    asm volatile(".set i_, 0\n\t.rept 10\n\tv_pk_add_f32 v[96+2*i_:97+2*i_], v[96+2*i_:97+2*i_], v[228+2*i_:229+2*i_]\n\t.set i_, i_+1\n\t.endr");
  }
  STAMP(12);
#pragma unroll 1
  for (unsigned sp = 0; sp < LDS_SPIN && !__builtin_amdgcn_readfirstlane(*(volatile __attribute__((address_space(3))) unsigned*)(lds + L_CSF + 4)); ++sp) asm volatile("s_sleep 1" ::: "memory");
#pragma unroll
  for (int t = 0; t < 5; ++t) cs[t] = *(lcs)(lds + L_CS3 + t * 64 + q * 16);
  w_prime<12>(wb);
  await<1ull>(srd, cown + 132, 8);
  ld_lead(srd, vo0, OFF_X1 + rt * FM_RT);
  {
    constexpr int GD = GDIST;
    const unsigned gs = OFF_G + rt * G_RT;
    const unsigned gl = gs;
    if (__builtin_expect(run_unit<12, 4, 0, 20, vmw(63), 8, 0, 0, 0, 2, 1>(wb, srd, lane16, OFF_ST2 + rt * 1024, 0, 0, srd, vo0, OFF_X1 + rt * FM_RT + 8192 STAMP_ARG) != 0, 0)) redo_unit<true>(rsw, vo0, OFF_X1 + rt * FM_RT, 20, rsw, wfl + 12 * GRP_B, srd);
    STAMP(13);
    float mean, var;
    read_stats(rsw, lane16, OFF_ST2 + rt * 1024, mean, var);
    const GeluK gk = gelu_consts(mean, __builtin_amdgcn_rsqf(var + LN_EPS), __builtin_amdgcn_sqrtf(var + LN_EPS));
    ep_gelu<0, 2, 228>(gk, cs[0], cs[1]); st_frag<228>(srd, lane16, gs + 2 * j * 1024);
    ep_gelu<2, 2, 236>(gk, cs[2], cs[3]); st_frag<236>(srd, lane16, gs + (2 * j + 1) * 1024);
    ep_gelu<4, 1, 250>(gk, cs[4], cs[4]);
#pragma unroll
    for (int t = 0; t < 5; ++t) cs[t] = *(lcs)(lds + L_CS3 + (5 + t) * 64 + q * 16);
    STAMP(14);
    run_unit<16, 4, 0, 20, vmw(63), 0, 0, 0, 0, -1, 0, 0>(wb, srd, 0, 0, 0, cgrp + 196, srd, vo0, 0 STAMP_ARG);
    peek(srd, cown + 196);
    ep_gelu<0, 1, 252>(gk, cs[0], cs[0]); st_frag<250>(srd, lane16, gs + (16 + 3 * j) * 1024);
    ep_gelu<1, 2, 232>(gk, cs[1], cs[2]); st_frag<232>(srd, lane16, gs + (17 + 3 * j) * 1024);
    ep_gelu<3, 2, 240>(gk, cs[3], cs[4]); st_frag<240>(srd, lane16, gs + (18 + 3 * j) * 1024);
#pragma unroll
    for (int t = 0; t < 4; ++t) cs[t] = *(lcs)(lds + L_CS3 + (10 + t) * 64 + q * 16);
    peek_await<3>(srd, cown + 196, 8);
    run_unit<20, 4, 0, 20, vmw(63), 0, GD, 20 - GD, 0, -1, 0, 0>(wb, srd, lane16, gl, 0, cgrp + 8, srd, vo0, 0 STAMP_ARG);
    peek(srd, cown + 8);
    const unsigned g3 = gs + (40 + 2 * j - (j > 3)) * 1024;
    ep_gelu<0, 2, 228>(gk, cs[0], cs[1]); st_frag<228>(srd, lane16, g3);
    if ((j & 3) != 3) { ep_gelu<2, 2, 236>(gk, cs[2], cs[3]); st_frag<236>(srd, lane16, g3 + 1024); }
    STAMP(15);
    if ((j & 3) != 3) peek_await<2>(srd, cown + 8, 8); else peek_await<1>(srd, cown + 8, 8);
    STAMP(16);
    run_unit<24, 5, 0, 25, vmw(63, 63, GD - 4, GD - 5, GD - 4), 0, 25, 0, GD, 2, 3, 5, false>(wb, srd, lane16, gl + GD * 1024, cown + 72, cgrp + 72, srd, vo0, 0 STAMP_ARG);
    peek_await<10>(srd, cown + 72, 8);
    run_unit<29, 6, 25, 54, vmw(GD - 5, GD - 5, GD - 5, GD - 5, 4, 0), 0, 29 - GD, 0, (25 + GD) % 20, -1, 0>(wb, srd, lane16, gl + (25 + GD) * 1024, 0, 0, srd, vo0, 0 STAMP_ARG);
    STAMP(17);
    asm volatile(".set i_, 0\n\t.rept 10\n\tv_pk_add_f32 v[228+2*i_:229+2*i_], v[96+2*i_:97+2*i_], v[228+2*i_:229+2*i_]\n\t.set i_, i_+1\n\t.endr\n\t"
                 EP_PACK(116, 228) EP_PACK(118, 232) EP_PACK(120, 236) EP_PACK(122, 240) EP_PACK(124, 244)
                 "global_store_dwordx4 %0, v[116:119], %2\n\tglobal_store_dwordx4 %0, v[120:123], %2 offset:64\n\tglobal_store_dwordx2 %1, v[124:125], %2\n\ts_waitcnt lgkmcnt(0)"
                 ::"v"(rowoff + colp), "v"(rowoff + cols), "s"(out) : "memory");
  }
  STAMP(18); SPAN_END;
  STAMP_OUT(1);
}

extern "C" void kernel_launch(void* const* d_in, const int* in_sizes, int n_in, void* d_out, int out_size, void* d_ws, size_t ws_size, hipStream_t stream) {
  (void)in_sizes; (void)n_in; (void)out_size; (void)ws_size;
  (void)hipFuncSetAttribute((const void*)k_main, hipFuncAttributeMaxDynamicSharedMemorySize, K2_LDS);
  k_main<<<256, 512, K2_LDS, stream>>>((const char*)d_in[0], (const char*)d_in[1], (const char*)d_in[2], (const char*)d_in[4], (const char*)d_in[5], (const char*)d_in[6],
                                              (const char*)d_in[7], (char*)d_ws, (char*)d_out);
}
```
